```python
import torch

def _softmax(x, axis=-1):
    return torch.softmax(x, dim=axis)
def _gelu(x):
    return torch.nn.functional.gelu(x, approximate="tanh")
def _silu(x):
    return torch.nn.functional.silu(x)
def _sigmoid(x):
    return torch.sigmoid(x)
def _layernorm(x, axis=-1, eps=1e-5):
    m = x.mean(dim=axis, keepdim=True)
    v = x.var(dim=axis, keepdim=True, unbiased=False)
    return (x - m) / torch.sqrt(v + eps)
def _scatter_add(tgt, idx, src, axis=0):
    return tgt.clone().index_add_(axis, idx, src)
def _segment_sum(src, seg, n):
    out = torch.zeros((n,) + src.shape[1:], dtype=src.dtype)
    return out.index_add_(0, seg, src)
def _pad(x, pw):
    flat = tuple(v for pair in reversed(pw) for v in pair)
    return torch.nn.functional.pad(x, flat)

kernel_name = 'recurrent_scan__mha_block_cached__glu_gated_swiglu__decode'

def setup_inputs():

    _w = (lambda a: (lambda t: t.to(torch.bfloat16) if t.is_floating_point() else t)(torch.as_tensor(a)))
    def _A_setup():
        B, Sq, D = 8, 1, 576
        H, Sk, Dh = 32, 4096, 128
        return {
            "x": _w((lambda *s: torch.randn(*s, dtype=torch.bfloat16) * 0.05)(B, Sq, D)),
            "k_cache": _w((lambda *s: torch.randn(*s, dtype=torch.bfloat16) * 3.2)(B, H, Sk, Dh)),
            "v_cache": _w((lambda *s: torch.randn(*s, dtype=torch.bfloat16) * 3.2)(B, H, Sk, Dh)),
            "wq": _w((lambda *s: torch.randn(*s, dtype=torch.bfloat16) * 3.2)(D, H * Dh) * (D ** -0.5)),
            "wo": _w((lambda *s: torch.randn(*s, dtype=torch.bfloat16) * 3.2)(H * Dh, D) * ((H * Dh) ** -0.5)),
        }
    _A_in = _A_setup()

    def _B_setup():
        B, S, D, F = 8, 1, 576, 4608
        return {
            "x": _w((lambda *s: torch.randn(*s, dtype=torch.bfloat16) * 0.05)(B, S, D)),
            "w1": _w((lambda *s: torch.randn(*s, dtype=torch.bfloat16) * 0.4)(D, F) * (2.0 / D) ** 0.5),
            "w3": _w((lambda *s: torch.randn(*s, dtype=torch.bfloat16) * 0.4)(D, F) * (2.0 / D) ** 0.5),
            "w2": _w((lambda *s: torch.randn(*s, dtype=torch.bfloat16) * 0.4)(F, D) * (2.0 / F) ** 0.5),
        }
    _B_in = _B_setup()

    return {
        "A_x": _A_in["x"],
        "A_k_cache": _A_in["k_cache"],
        "A_v_cache": _A_in["v_cache"],
        "A_wq": _A_in["wq"],
        "A_wo": _A_in["wo"],
        "B_x": _B_in["x"],
        "B_w1": _B_in["w1"],
        "B_w3": _B_in["w3"],
        "B_w2": _B_in["w2"],
    }

def reference(A_x, A_k_cache, A_v_cache, A_wq, A_wo, B_x, B_w1, B_w3, B_w2):
    def _A_ref(x, k_cache, v_cache, wq, wo):
        B, Sq, D = x.shape
        H, Dh = 32, 128
        q = (lambda x, *d: x.permute(*d))((x @ wq).reshape(B, Sq, H, Dh), 0, 2, 1, 3)
        scores = torch.einsum("bhqd,bhkd->bhqk", q, k_cache) * (Dh ** -0.5)
        p = _softmax(scores, axis=-1)
        o = torch.einsum("bhqk,bhkd->bhqd", p, v_cache)
        return (lambda x, *d: x.permute(*d))(o, 0, 2, 1, 3).reshape(B, Sq, H * Dh) @ wo

    def _B_ref(x, w1, w3, w2):
        gate = torch.einsum("bsd,df->bsf", x, w1)
        up = torch.einsum("bsd,df->bsf", x, w3)
        h = _silu(gate) * up
        out = torch.einsum("bsf,fd->bsd", h, w2)
        return out

    x = A_x
    xn = _layernorm(x, axis=-1)
    s = _A_ref(xn, A_k_cache, A_v_cache, A_wq, A_wo)
    g = _silu(_B_ref(xn, B_w1, B_w3, B_w2))
    return x + s * g
```

```cpp
#include <hip/hip_runtime.h>
#include <hip/hip_bf16.h>
#include <cstdint>

#define MIMIC 0

constexpr int B = 8, D = 576, H = 32, SK = 4096, DH = 128, F = 4608;
typedef unsigned u32x4 __attribute__((ext_vector_type(4)));
typedef __bf16 bf16x2 __attribute__((ext_vector_type(2)));
typedef unsigned short u16;

__device__ __forceinline__ float bf2f(u16 v) { return __uint_as_float(((unsigned)v) << 16); }
__device__ __forceinline__ u16 f2bf(float f) { __bf16 b = (__bf16)f; return __builtin_bit_cast(u16, b); }
__device__ __forceinline__ float rbf(float f) { return MIMIC ? bf2f(f2bf(f)) : f; }
__device__ __forceinline__ float dot2(unsigned k, unsigned q, float acc) {
  return __builtin_amdgcn_fdot2_f32_bf16(__builtin_bit_cast(bf16x2, k), __builtin_bit_cast(bf16x2, q), acc, false);
}
__device__ __forceinline__ float wsum(float v) { for (int o = 32; o; o >>= 1) v += __shfl_xor(v, o); return v; }
__device__ __forceinline__ float wmax(float v) { for (int o = 32; o; o >>= 1) v = fmaxf(v, __shfl_xor(v, o)); return v; }
__device__ __forceinline__ float silu(float x) { return x / (1.f + __expf(-x)); }

__global__ void __launch_bounds__(512) k1(const u16* x, const u16* wq, const u16* w1, const u16* w3, float* qf, float* hf) {
  __shared__ float xn[B][D];
  __shared__ float red[2][8][B][64];
  int tid = threadIdx.x, lane = tid & 63, w = tid >> 6;
  {
    float v[9]; float s = 0.f;
    for (int i = 0; i < 9; ++i) { v[i] = bf2f(x[w * D + lane + 64 * i]); s += v[i]; }
    float mean = wsum(s) / D; float ss = 0.f;
    for (int i = 0; i < 9; ++i) { float d = v[i] - mean; ss += d * d; }
    float var = wsum(ss) / D;
    float m = rbf(mean), vr = rbf(var);
    float den = rbf(sqrtf(rbf(vr + 1e-5f)));
    for (int i = 0; i < 9; ++i) xn[w][lane + 64 * i] = rbf(rbf(v[i] - m) / den);
  }
  __syncthreads();
  int blk = blockIdx.x;
  bool isq = blk < 64;
  int col = (isq ? blk : blk - 64) * 64 + lane;
  const u16* wa = isq ? wq : w1; int ld = isq ? H * DH : F;
  float a[B] = {}, c[B] = {};
  for (int k = w * 72; k < w * 72 + 72; ++k) {
    float wv = bf2f(wa[(size_t)k * ld + col]);
    float wu = isq ? 0.f : bf2f(w3[(size_t)k * ld + col]);
    for (int b = 0; b < B; ++b) { a[b] += xn[b][k] * wv; c[b] += xn[b][k] * wu; }
  }
  for (int b = 0; b < B; ++b) { red[0][w][b][lane] = a[b]; red[1][w][b][lane] = c[b]; }
  __syncthreads();
  { int b = w; float g = 0.f, u = 0.f;
    for (int j = 0; j < 8; ++j) { g += red[0][j][b][lane]; u += red[1][j][b][lane]; }
    if (isq) qf[b * H * DH + col] = rbf(g);
    else { g = rbf(g); u = rbf(u); hf[b * F + col] = rbf(rbf(silu(g)) * u); }
  }
}

__global__ void __launch_bounds__(1024) k2(const u16* kc, const u16* vc, const float* qf, float* of) {
  __shared__ float sm[16], sl[16], so[16][DH];
  int bh = blockIdx.x, tid = threadIdx.x, lane = tid & 63, w = tid >> 6, r = lane >> 4, c = lane & 15;
  const u32x4* kp = (const u32x4*)(kc + (size_t)bh * SK * DH);
  const u32x4* vp = (const u32x4*)(vc + (size_t)bh * SK * DH);
  float q[8];
  for (int j = 0; j < 8; ++j) q[j] = qf[bh * DH + 8 * c + j];
  float m = -INFINITY, l = 0.f, acc[8] = {};
  for (int t0 = w * 16; t0 < SK; t0 += 256) {
    u32x4 kk[4], vv[4];
    for (int i = 0; i < 4; ++i) { kk[i] = __builtin_nontemporal_load(kp + (t0 + 4 * i) * 16 + lane); vv[i] = __builtin_nontemporal_load(vp + (t0 + 4 * i) * 16 + lane); }
    float s[4];
    for (int i = 0; i < 4; ++i) {
      float d = 0.f;
      for (int j = 0; j < 4; ++j) { d += q[2 * j] * __uint_as_float(kk[i][j] << 16); d += q[2 * j + 1] * __uint_as_float(kk[i][j] & 0xffff0000u); }
      for (int o = 1; o < 16; o <<= 1) d += __shfl_xor(d, o);
      s[i] = rbf(rbf(d) * 0.08838834764831845f);
    }
    float mx = fmaxf(fmaxf(s[0], s[1]), fmaxf(s[2], s[3]));
    float mn = fmaxf(m, mx), al = __expf(m - mn);
    l *= al; for (int j = 0; j < 8; ++j) acc[j] *= al;
    m = mn;
    for (int i = 0; i < 4; ++i) {
      float p = __expf(s[i] - m); l += p;
      for (int j = 0; j < 4; ++j) { acc[2 * j] += p * __uint_as_float(vv[i][j] << 16); acc[2 * j + 1] += p * __uint_as_float(vv[i][j] & 0xffff0000u); }
    }
  }
  float M = wmax(m); float al = __expf(m - M); l *= al;
  l += __shfl_xor(l, 16); l += __shfl_xor(l, 32);
  for (int j = 0; j < 8; ++j) { float a = acc[j] * al; a += __shfl_xor(a, 16); a += __shfl_xor(a, 32); acc[j] = a; }
  if (lane == 0) { sm[w] = M; sl[w] = l; }
  if (r == 0) for (int j = 0; j < 8; ++j) so[w][8 * c + j] = acc[j];
  __syncthreads();
  if (tid < DH) {
    float MM = -INFINITY; for (int j = 0; j < 16; ++j) MM = fmaxf(MM, sm[j]);
    float L = 0.f, o = 0.f;
    for (int j = 0; j < 16; ++j) { float e = __expf(sm[j] - MM); L += e * sl[j]; o += e * so[j][tid]; }
    of[bh * DH + tid] = rbf(o / L);
  }
}

__global__ void __launch_bounds__(1024) k3(const u16* x, const u16* wo, const u16* w2, const float* of, const float* hf, u16* out) {
  __shared__ float red[2][64][B][16];
  int tid = threadIdx.x, cl = tid & 15, ks = tid >> 4, col = blockIdx.x * 16 + cl;
  float a[B] = {}, c[B] = {};
  for (int k = ks; k < H * DH; k += 64) { float wv = bf2f(wo[(size_t)k * D + col]); for (int b = 0; b < B; ++b) a[b] += of[b * H * DH + k] * wv; }
  for (int k = ks; k < F; k += 64) { float wv = bf2f(w2[(size_t)k * D + col]); for (int b = 0; b < B; ++b) c[b] += hf[b * F + k] * wv; }
  for (int b = 0; b < B; ++b) { red[0][ks][b][cl] = a[b]; red[1][ks][b][cl] = c[b]; }
  __syncthreads();
  if (tid < B * 16) {
    int b = tid >> 4; float s = 0.f, f = 0.f;
    for (int j = 0; j < 64; ++j) { s += red[0][j][b][cl]; f += red[1][j][b][cl]; }
    s = rbf(s); f = rbf(f);
    float g = rbf(silu(f));
    out[b * D + col] = f2bf(bf2f(x[b * D + col]) + rbf(s * g));
  }
}

extern "C" void kernel_launch(void* const* d_in, const int* in_sizes, int n_in, void* d_out, int out_size, void* d_ws, size_t ws_size, hipStream_t stream) {
  const u16* x = (const u16*)d_in[0]; const u16* kc = (const u16*)d_in[1]; const u16* vc = (const u16*)d_in[2];
  const u16* wq = (const u16*)d_in[3]; const u16* wo = (const u16*)d_in[4];
  const u16* w1 = (const u16*)d_in[6]; const u16* w3 = (const u16*)d_in[7]; const u16* w2 = (const u16*)d_in[8];
  float* qf = (float*)d_ws; float* hf = qf + B * H * DH; float* of = hf + B * F;
  k1<<<(H * DH + F) / 64, 512, 0, stream>>>(x, wq, w1, w3, qf, hf);
  k2<<<B * H, 1024, 0, stream>>>(kc, vc, qf, of);
  k3<<<D / 16, 1024, 0, stream>>>(x, wo, w2, of, hf, (u16*)d_out);
}
```

```cpp
#include <hip/hip_runtime.h>
#include <hip/hip_bf16.h>
#include <cstdint>

#define PF 5
#define EXT0 1
#define WOD 2
#define ABL 0
#define AUX_HO 16

namespace {

typedef unsigned u32;
typedef unsigned long long u64;
typedef unsigned short u16;
typedef u32 u32x4 __attribute__((ext_vector_type(4)));
typedef u32 u32x2 __attribute__((ext_vector_type(2)));
typedef float f32x4 __attribute__((ext_vector_type(4)));
typedef __bf16 bf16x8 __attribute__((ext_vector_type(8)));
typedef __bf16 bf16x2 __attribute__((ext_vector_type(2)));
#define GAS __attribute__((address_space(1)))
#define LAS __attribute__((address_space(3)))

constexpr int NB = 8, DM = 576, NH = 32, SK = 4096, DH = 128, FF = 4608;
constexpr int NTASK = FF / 64;
constexpr int NCONS = DM / 32;
constexpr int NT = 2, SC1 = 16;

constexpr u32 WS_EPOCH = 0, WS_F1 = 256, WS_F2 = WS_F1 + 256 * 8, WS_ATT = 4096;
constexpr u32 WS_MLP = WS_ATT + NB * NH * DM * 4, WS_END = WS_MLP + NTASK * NB * DM * 4;
constexpr u32 WS_DBG = 4u << 20;
constexpr u32 EMPTY = 0xffffffffu;

constexpr int XN_LD = 1168;
constexpr int L_XN = 0, L_QP = L_XN + NB * XN_LD, L_QB = L_QP + 12 * DH * 4, L_PS = L_QB + DH * 2, L_WM = L_PS + SK * 2, L_WS = L_WM + 64,
              L_OP = L_WS + 64, L_OB = L_OP + 4 * DH * 4, L_TG = L_OB + 9 * DH * 2, L_END = L_TG + 16;

struct Args { const char* x; const char* k; const char* v; const char* wq; const char* wo; const char* w1; const char* w3; const char* w2; char* out; char* ws; };

typedef __amdgpu_buffer_rsrc_t rsrc_t;
__device__ __forceinline__ rsrc_t mk(const char* p, u32 n) { return __builtin_amdgcn_make_buffer_rsrc((void*)p, 0, n, 0x00020000); }
#define LD128(r, vo, so, aux) __builtin_amdgcn_raw_buffer_load_b128(r, vo, so, aux)
#define LD64(r, vo, so, aux) __builtin_amdgcn_raw_buffer_load_b64(r, vo, so, aux)
#define LD32(r, vo, so, aux) __builtin_amdgcn_raw_buffer_load_b32(r, vo, so, aux)
#define ST128(v, r, vo, so, aux) __builtin_amdgcn_raw_buffer_store_b128(v, r, vo, so, aux)
#define ST64(v, r, vo, so, aux) __builtin_amdgcn_raw_buffer_store_b64(v, r, vo, so, aux)
#define PIN() __builtin_amdgcn_sched_barrier(0)
#define USE(x) asm volatile("" ::"v"(x) : "memory")
#define BAR() asm volatile("s_waitcnt lgkmcnt(0)\n\ts_barrier" ::: "memory")
#define DRAIN() asm volatile("s_waitcnt vmcnt(0)" ::: "memory")

__device__ __forceinline__ u32 pk(float lo, float hi) { bf16x2 t = {(__bf16)lo, (__bf16)hi}; return __builtin_bit_cast(u32, t); }
__device__ __forceinline__ float rb(float x) { return __uint_as_float(pk(x, x) << 16); }
__device__ __forceinline__ float lo16(u32 v) { return __uint_as_float(v << 16); }
__device__ __forceinline__ float hi16(u32 v) { return __uint_as_float(v & 0xffff0000u); }
__device__ __forceinline__ float dot2(u32 a, u32 b, float c) {
  return __builtin_amdgcn_fdot2_f32_bf16(__builtin_bit_cast(bf16x2, a), __builtin_bit_cast(bf16x2, b), c, false);
}
__device__ __forceinline__ f32x4 mfma(u32x4 a, u32x4 b, f32x4 c) {
  return __builtin_amdgcn_mfma_f32_16x16x32_bf16(__builtin_bit_cast(bf16x8, a), __builtin_bit_cast(bf16x8, b), c, 0, 0, 0);
}
template <int CTRL> __device__ __forceinline__ float dpp(float x) {
  return __builtin_bit_cast(float, __builtin_amdgcn_mov_dpp(__builtin_bit_cast(int, x), CTRL, 0xf, 0xf, true));
}
constexpr int X1 = 0xB1, X2 = 0x4E, HMIR = 0x141, MIR = 0x140, ROR8 = 0x128;
__device__ __forceinline__ float sum16(float x) { x += dpp<X1>(x); x += dpp<X2>(x); x += dpp<HMIR>(x); x += dpp<MIR>(x); return x; }
__device__ __forceinline__ float max16(float x) { x = fmaxf(x, dpp<X1>(x)); x = fmaxf(x, dpp<X2>(x)); x = fmaxf(x, dpp<HMIR>(x)); x = fmaxf(x, dpp<MIR>(x)); return x; }
__device__ __forceinline__ float sumrows(float x) {
  auto s = __builtin_amdgcn_permlane16_swap(__float_as_uint(x), __float_as_uint(x), false, false);
  x = __uint_as_float(s[0]) + __uint_as_float(s[1]);
  auto t = __builtin_amdgcn_permlane32_swap(__float_as_uint(x), __float_as_uint(x), false, false);
  return __uint_as_float(t[0]) + __uint_as_float(t[1]);
}
__device__ __forceinline__ float maxrows(float x) {
  auto s = __builtin_amdgcn_permlane16_swap(__float_as_uint(x), __float_as_uint(x), false, false);
  x = fmaxf(__uint_as_float(s[0]), __uint_as_float(s[1]));
  auto t = __builtin_amdgcn_permlane32_swap(__float_as_uint(x), __float_as_uint(x), false, false);
  return fmaxf(__uint_as_float(t[0]), __uint_as_float(t[1]));
}
__device__ __forceinline__ float silu(float x) { return x / (1.f + __expf(-x)); }
__device__ __forceinline__ u64 now() { return __builtin_amdgcn_s_memrealtime(); }
constexpr u64 SPIN_CAP = 5000000;

#define TS(i) do {} while (0)

struct Diag { u32 s[2][4]; };
__device__ __forceinline__ Diag make_diag(int c) {
  Diag d;
#pragma unroll
  for (int par = 0; par < 2; ++par)
#pragma unroll
    for (int w = 0; w < 4; ++w) {
      const u32 b = par ? 0x0302u : 0x0100u;
      d.s[par][w] = (c >> 1) == w ? ((c & 1) ? (b << 16) | 0x0c0cu : 0x0c0c0000u | b) : 0x0c0c0c0cu;
    }
  return d;
}
template <int PAR> __device__ __forceinline__ f32x4 axpy4(u32 val, u32x4 rows, const Diag& d, f32x4 acc) {
  u32x4 af;
#pragma unroll
  for (int w = 0; w < 4; ++w) af[w] = __builtin_amdgcn_perm(val, val, d.s[PAR][w]);
  return mfma(af, rows, acc);
}
__device__ __forceinline__ float score4(u32x4 k, u32x4 q) {
  float d = dot2(k[0], q[0], 0.f); d = dot2(k[1], q[1], d); d = dot2(k[2], q[2], d); d = dot2(k[3], q[3], d);
  return sum16(d);
}
__device__ __forceinline__ void tr8(const u32x2 (&raw)[8], u32x4 (&f)[4]) {
#pragma unroll
  for (int e = 0; e < 4; ++e)
#pragma unroll
    for (int d = 0; d < 4; ++d) f[e][d] = __builtin_amdgcn_perm(raw[2 * d + 1][e >> 1], raw[2 * d][e >> 1], (e & 1) ? 0x07060302u : 0x05040100u);
}

__global__ void __launch_bounds__(1024) fused_decode(Args a) {
  __shared__ __attribute__((aligned(16))) char lds_[L_END];
  LAS char* lds = (LAS char*)lds_;
  const int tid = threadIdx.x, lane = tid & 63, w = __builtin_amdgcn_readfirstlane(tid >> 6), r = lane >> 4, c = lane & 15;
  const int blk = blockIdx.x, b = blk >> 5, h = blk & 31;
  const char* kbase = a.k + ((size_t)blk << 20);
  const char* vbase = a.v + ((size_t)blk << 20);
  const rsrc_t rk = mk(kbase, 1u << 20);
  const int sw = w & 3;
  const int vA = lane * 16 + sw * 4096, vB = vA + 16384;
#define KVLD(rs, beta, i) LD128(rs, ((i) < 4 ? vA : vB) + ((i) & 3) * 1024, (beta) * 32768, NT)
  float mx = -INFINITY;
  auto put_scores = [&](float sc, int batch) {
    const float s = rb(rb(sc) * 0.08838834764831845f);
    mx = fmaxf(mx, s);
    if (c < 8) *(LAS u16*)(lds + L_PS + batch * 64 + r * 16 + c * 2) = (u16)(__float_as_uint(s) >> 16);
  };
  auto softmax = [&]() {
    const float wm = maxrows(max16(mx));
    if (lane == 0) *(LAS float*)(lds + L_WM + w * 4) = wm;
    BAR();
    const float M = max16(*(LAS float*)(lds + L_WM + c * 4));
    const u32x2 sv = *(LAS u32x2*)(lds + L_PS + tid * 8);
    const float e0 = __expf(lo16(sv[0]) - M), e1 = __expf(hi16(sv[0]) - M), e2 = __expf(lo16(sv[1]) - M), e3 = __expf(hi16(sv[1]) - M);
    const float ws = sumrows(sum16((e0 + e1) + (e2 + e3)));
    if (lane == 0) *(LAS float*)(lds + L_WS + w * 4) = ws;
    BAR();
    const float inv = 1.f / sum16(*(LAS float*)(lds + L_WS + c * 4));
    *(LAS u32x2*)(lds + L_PS + tid * 8) = u32x2{pk(e0 * inv, e1 * inv), pk(e2 * inv, e3 * inv)};
    BAR();
  };

  if (w >= 12) {
    __builtin_amdgcn_s_setprio(2);
    TS(0);
    u32x4 ring[8], ext[8];
#pragma unroll
    for (int i = 0; i < (EXT0 ? 8 : PF); ++i) ring[i] = KVLD(rk, 3, i);
    if (EXT0) {
#pragma unroll
      for (int i = 0; i < 8; ++i) ext[i] = KVLD(rk, 4, i);
    }
    PIN();
    BAR(); BAR(); BAR();
    TS(1);
    const u32x4 q = *(LAS u32x4*)(lds + L_QB + c * 16);
    float sc = 0.f;
    if (EXT0) {
#pragma unroll
      for (int i = 0; i < 8; ++i) { const float d = score4(ring[i], q); sc = (c & 7) == i ? d : sc; PIN(); }
      put_scores(sc, 12 + sw);
#pragma unroll
      for (int i = 0; i < 8; ++i) {
        if (i + PF >= 8) ring[i + PF - 8] = KVLD(rk, 5, i + PF - 8);
        PIN();
        const float d = score4(ext[i], q); sc = (c & 7) == i ? d : sc;
        PIN();
      }
      put_scores(sc, 16 + sw);
    }
#pragma unroll 1
    for (int beta = EXT0 ? 5 : 3; beta < 32; ++beta) {
      const rsrc_t rn = mk(beta < 31 ? kbase : vbase, 1u << 20);
      const int bn = beta < 31 ? beta + 1 : 0;
#pragma unroll
      for (int i = 0; i < 8; ++i) {
        if (i + PF < 8) ring[i + PF] = KVLD(rk, beta, i + PF); else ring[i + PF - 8] = KVLD(rn, bn, i + PF - 8);
        PIN();
        const float d = score4(ring[i], q); sc = (c & 7) == i ? d : sc;
        PIN();
      }
      put_scores(sc, beta * 4 + sw);
    }
    const rsrc_t rv = mk(vbase, 1u << 20);
#pragma unroll
    for (int i = PF; i < 8; ++i) ring[i] = KVLD(rv, 0, i);
#pragma unroll
    for (int i = 0; i < 8; ++i) ext[i] = KVLD(rv, 1, i);
    PIN();
    TS(2);
    softmax();
    TS(3);
    const Diag dg = make_diag(c);
    f32x4 acc = {0.f, 0.f, 0.f, 0.f};
    const int po = L_PS + sw * 64 + r * 16;
    u32x4 pv = *(LAS u32x4*)(lds + po), pn = *(LAS u32x4*)(lds + po + 256);
#define VSTEP(i, reg) acc = ((i) & 1) ? axpy4<1>(pv[(i) >> 1], reg, dg, acc) : axpy4<0>(pv[(i) >> 1], reg, dg, acc)
#pragma unroll
    for (int i = 0; i < 8; ++i) { VSTEP(i, ring[i]); PIN(); }
    pv = pn; pn = *(LAS u32x4*)(lds + po + 512);
#pragma unroll
    for (int i = 0; i < 8; ++i) {
      if (i + PF >= 8) ring[i + PF - 8] = KVLD(rv, 2, i + PF - 8);
      PIN();
      VSTEP(i, ext[i]);
      PIN();
    }
#pragma unroll 1
    for (int beta = 2; beta < 32; ++beta) {
      const rsrc_t rn = mk(vbase, beta < 31 ? 1u << 20 : 0u);
      pv = pn; pn = *(LAS u32x4*)(lds + po + (beta < 31 ? beta + 1 : 31) * 256);
#pragma unroll
      for (int i = 0; i < 8; ++i) {
        if (i + PF < 8) ring[i + PF] = KVLD(rv, beta, i + PF); else ring[i + PF - 8] = KVLD(rn, beta < 31 ? beta + 1 : 0, i + PF - 8);
        PIN();
        VSTEP(i, ring[i]);
        PIN();
      }
    }
    if (r < 2) *(LAS f32x4*)(lds + L_OP + sw * 512 + c * 32 + r * 16) = acc;
    TS(4);
    BAR();
    return;
  }

  const rsrc_t rws = mk(a.ws, WS_END);
  u32 xr[5];
  if (w < 8) {
    const rsrc_t rx = mk(a.x + w * (DM * 2), DM * 2);
#pragma unroll
    for (int i = 0; i < 5; ++i) xr[i] = LD32(rx, lane * 4 + i * 256, 0, NT);
  }
  u32x2 ep = {0u, 0u};
  if (w == 10) ep = LD64(rws, WS_EPOCH, 0, AUX_HO);
  u32x4 wb[12], kb[8];
  {
    const rsrc_t rq = mk(a.wq + h * 256, DM * 8192 - h * 256);
    const int vo = (48 * w + 12 * r) * 8192 + c * 16;
#pragma unroll
    for (int i = 0; i < 12; ++i) wb[i] = LD128(rq, vo, i * 8192, NT);
#pragma unroll
    for (int i = 0; i < 8; ++i) kb[i] = KVLD(rk, w >> 2, i);
  }
  PIN();
  if (w < 8) {
    float s = 0.f;
#pragma unroll
    for (int i = 0; i < 5; ++i) s += lo16(xr[i]) + hi16(xr[i]);
    const float mean = sumrows(sum16(s)) * (1.f / DM);
    float ss = 0.f;
#pragma unroll
    for (int i = 0; i < 5; ++i) {
      const float d0 = lo16(xr[i]) - mean, d1 = hi16(xr[i]) - mean;
      ss += (i < 4 || lane < 32) ? d0 * d0 + d1 * d1 : 0.f;
    }
    const float var = sumrows(sum16(ss)) * (1.f / DM);
    const float m = rb(mean), den = rb(sqrtf(rb(rb(var) + 1e-5f)));
#pragma unroll
    for (int i = 0; i < 5; ++i)
      if (i < 4 || lane < 32) *(LAS u32*)(lds + L_XN + w * XN_LD + lane * 4 + i * 256) = pk(rb(lo16(xr[i]) - m) / den, rb(hi16(xr[i]) - m) / den);
  }
  u64 tag = 0;
  if (w == 10) {
    tag = (((u64)(u32)__builtin_amdgcn_readfirstlane(ep[1]) << 32) | (u32)__builtin_amdgcn_readfirstlane(ep[0])) + 1;
    if (lane == 0) *(LAS u64*)(lds + L_TG) = tag;
  }
  BAR();
  {
    const Diag dg = make_diag(c);
    const int xo = L_XN + b * XN_LD + (48 * w + 12 * r) * 2;
    const u32x2 x0 = *(LAS u32x2*)(lds + xo), x1 = *(LAS u32x2*)(lds + xo + 8), x2 = *(LAS u32x2*)(lds + xo + 16);
    const u32 xv[6] = {x0[0], x0[1], x1[0], x1[1], x2[0], x2[1]};
    f32x4 acc = {0.f, 0.f, 0.f, 0.f};
#pragma unroll
    for (int i = 0; i < 12; ++i) acc = (i & 1) ? axpy4<1>(xv[i >> 1], wb[i], dg, acc) : axpy4<0>(xv[i >> 1], wb[i], dg, acc);
    if (r < 2) *(LAS f32x4*)(lds + L_QP + w * 512 + c * 32 + r * 16) = acc;
  }
  BAR();
  if (w < 2) {
    float s = 0.f;
#pragma unroll
    for (int j = 0; j < 12; ++j) s += *(LAS float*)(lds + L_QP + j * 512 + tid * 4);
    *(LAS u16*)(lds + L_QB + tid * 2) = (u16)pk(s, s);
  }
  BAR();
  {
    const u32x4 q = *(LAS u32x4*)(lds + L_QB + c * 16);
    float sc = 0.f;
#pragma unroll
    for (int i = 0; i < 8; ++i) { const float d = score4(kb[i], q); sc = (c & 7) == i ? d : sc; }
    put_scores(sc, w);
  }
  PIN();

  if (w < 9) {
    u32x4 wf[4][4];
    const rsrc_t ro = mk(a.wo + (size_t)h * (DH * DM * 2), DH * DM * 2);
    const int vo = r * 8 * (DM * 2) + w * 128 + c * 8;
    u32x2 raw[4][8];
#pragma unroll
    for (int n = 0; n < 32; ++n) {
      raw[n >> 3][n & 7] = (ABL & 8) ? u32x2{1u,1u} : LD64(ro, vo, ((n >> 3) * 32 + (n & 7)) * (DM * 2), NT);
      if (n >= WOD) USE(raw[(n - WOD) >> 3][(n - WOD) & 7]);
      if (n >= WOD + 7 && ((n - WOD) & 7) == 7) tr8(raw[(n - WOD) >> 3], wf[(n - WOD) >> 3]);
      PIN();
    }
    tr8(raw[3], wf[3]);
    TS(8);
    PIN();
    softmax();
    BAR();
    TS(10);
    float o0 = 0.f, o1 = 0.f;
#pragma unroll
    for (int j = 0; j < 4; ++j) { const u32x2 t = *(LAS u32x2*)(lds + L_OP + j * 512 + lane * 8); o0 += __uint_as_float(t[0]); o1 += __uint_as_float(t[1]); }
    *(LAS u32*)(lds + L_OB + w * 256 + lane * 4) = pk(o0, o1);
    asm volatile("s_waitcnt lgkmcnt(0)" ::: "memory");
    f32x4 d[4];
#pragma unroll
    for (int e = 0; e < 4; ++e) d[e] = f32x4{0.f, 0.f, 0.f, 0.f};
#pragma unroll
    for (int s = 0; s < 4; ++s) {
      const u32x4 ob = *(LAS u32x4*)(lds + L_OB + w * 256 + s * 64 + r * 16);
#pragma unroll
      for (int e = 0; e < 4; ++e) d[e] = mfma(wf[s][e], ob, d[e]);
    }
    u32x4 o;
#pragma unroll
    for (int e = 0; e < 4; ++e) {
      const float t = (c & 2) ? ((c & 1) ? d[e][3] : d[e][2]) : ((c & 1) ? d[e][1] : d[e][0]);
      o[e] = min(__float_as_uint(t), EMPTY - 1);
    }
    if (c < 4) ST128(o, rws, WS_ATT + blk * (DM * 4) + w * 256 + r * 64 + c * 16, 0, SC1);
    TS(11);
    return;
  }
  if (w == 9) {
    u32x4 hf[2] = {};
    if (blk < NTASK && !(ABL & 2)) {
      const rsrc_t r1 = mk(a.w1 + blk * 128, DM * FF * 2 - blk * 128), r3 = mk(a.w3 + blk * 128, DM * FF * 2 - blk * 128);
      const int vo = r * 8 * (FF * 2) + c * 8;
      const int xo = L_XN + (c & 7) * XN_LD + r * 16;
      u32x2 ra[8], rc[8];
      f32x4 g[4], u[4];
#pragma unroll
      for (int e = 0; e < 4; ++e) { g[e] = f32x4{0.f, 0.f, 0.f, 0.f}; u[e] = g[e]; }
#pragma unroll
      for (int i = 0; i < 8; ++i) ra[i] = LD64(r1, vo, i * (FF * 2), NT);
#pragma unroll
      for (int i = 0; i < 8; ++i) rc[i] = LD64(r3, vo, i * (FF * 2), NT);
#pragma unroll 1
      for (int s = 0; s < 18; ++s) {
        const int sn = (s < 17 ? s + 1 : 17) * 32 * (FF * 2);
        const rsrc_t n1 = mk(a.w1 + blk * 128, s < 17 ? DM * FF * 2 - blk * 128 : 0), n3 = mk(a.w3 + blk * 128, s < 17 ? DM * FF * 2 - blk * 128 : 0);
        const u32x4 xb = *(LAS u32x4*)(lds + xo + s * 64);
        u32x4 f[4];
        tr8(ra, f);
        PIN();
#pragma unroll
        for (int i = 0; i < 8; ++i) ra[i] = LD64(n1, vo, sn + i * (FF * 2), NT);
        PIN();
#pragma unroll
        for (int e = 0; e < 4; ++e) g[e] = mfma(f[e], xb, g[e]);
        PIN();
        tr8(rc, f);
        PIN();
#pragma unroll
        for (int i = 0; i < 8; ++i) rc[i] = LD64(n3, vo, sn + i * (FF * 2), NT);
        PIN();
#pragma unroll
        for (int e = 0; e < 4; ++e) u[e] = mfma(f[e], xb, u[e]);
        PIN();
      }
      float hv[4][4];
#pragma unroll
      for (int p = 0; p < 4; ++p)
#pragma unroll
        for (int e = 0; e < 4; ++e) hv[p][e] = rb(silu(rb(g[e][p]))) * rb(u[e][p]);
#pragma unroll
      for (int s = 0; s < 2; ++s) hf[s] = u32x4{pk(hv[2 * s][0], hv[2 * s][1]), pk(hv[2 * s][2], hv[2 * s][3]), pk(hv[2 * s + 1][0], hv[2 * s + 1][1]), pk(hv[2 * s + 1][2], hv[2 * s + 1][3])};
      TS(9);
    }
    PIN();
    softmax();
    if (blk < NTASK && !(ABL & 2)) {
      const rsrc_t r2 = mk(a.w2 + (size_t)blk * (64 * DM * 2), 64 * DM * 2);
      const int vo = r * 16 * (DM * 2) + c * 8;
      u32x2 ra[8], rc[8];
#pragma unroll
      for (int i = 0; i < 8; ++i) ra[i] = LD64(r2, vo, i * (DM * 2), NT);
#pragma unroll
      for (int i = 0; i < 8; ++i) rc[i] = LD64(r2, vo, (8 + i) * (DM * 2), NT);
      const int so = WS_MLP + (blk * NB + (c & 7)) * (DM * 4) + r * 64 + (c >> 3) * 32;
#pragma unroll 1
      for (int W = 0; W < 9; ++W) {
        const rsrc_t n2 = mk(a.w2 + (size_t)blk * (64 * DM * 2), W < 8 ? 64 * DM * 2 : 0);
        const int sn = (W < 8 ? W + 1 : 8) * 128;
        u32x4 f[4];
        f32x4 d[4];
        tr8(ra, f);
        PIN();
#pragma unroll
        for (int i = 0; i < 8; ++i) ra[i] = LD64(n2, vo, sn + i * (DM * 2), NT);
        PIN();
#pragma unroll
        for (int e = 0; e < 4; ++e) d[e] = mfma(f[e], hf[0], f32x4{0.f, 0.f, 0.f, 0.f});
        PIN();
        tr8(rc, f);
        PIN();
#pragma unroll
        for (int i = 0; i < 8; ++i) rc[i] = LD64(n2, vo, sn + (8 + i) * (DM * 2), NT);
        PIN();
#pragma unroll
        for (int e = 0; e < 4; ++e) d[e] = mfma(f[e], hf[1], d[e]);
#pragma unroll
        for (int p = 0; p < 2; ++p) {
          u32x4 o;
#pragma unroll
          for (int e = 0; e < 4; ++e) o[e] = __float_as_uint((c & 8) ? d[e][2 + p] : d[e][p]);
          if (!(ABL & 4)) ST128(o, rws, so + p * 16, W * 256, SC1); else USE(o);
        }
        PIN();
      }
      DRAIN();
      const u32x2 tg = *(LAS u32x2*)(lds + L_TG);
      if (lane == 0) ST64(tg, rws, WS_F2 + blk * 8, 0, SC1);
      TS(12);
    }
    BAR();
    return;
  }
  if (w == 10) {
    if (lane < 48 && !(ABL & 16)) {
#pragma unroll
      for (int i = 0; i < 3; ++i) ST128((u32x4{EMPTY, EMPTY, EMPTY, EMPTY}), rws, WS_ATT + blk * (DM * 4) + lane * 16 + i * 768, 0, SC1);
    }
    DRAIN();
    if (lane == 0) ST64((u32x2{(u32)tag, (u32)(tag >> 32)}), rws, WS_F1 + blk * 8, 0, SC1);
  }
  PIN();
  softmax();
  if (w == 10 && h < NCONS) {
    bool ok = true;
    const u32 t0 = (u32)tag, t1 = (u32)(tag >> 32);
    const u64 start = now();
    if (ABL & 1) while (now() - start < 2600) __builtin_amdgcn_s_sleep(32);
    for (;;) {
      if (ABL & 22) break;
      bool all = true;
#pragma unroll
      for (int i = 0; i < 2; ++i) { const u32x4 f = LD128(rws, WS_F1 + lane * 16 + i * 1024, 0, AUX_HO); all &= f[0] == t0 && f[1] == t1 && f[2] == t0 && f[3] == t1; }
      { const u32x4 f = LD128(rws, WS_F2 + (lane < 36 ? lane : 0) * 16, 0, AUX_HO); all &= f[0] == t0 && f[1] == t1 && f[2] == t0 && f[3] == t1; }
      if (__all(all)) break;
      if (now() - start > SPIN_CAP) { ok = false; break; }
      __builtin_amdgcn_s_sleep(32);
    }
    asm volatile("" ::: "memory");
    if (blk == 0 && lane == 0) ST64((u32x2{t0, t1}), rws, WS_EPOCH, 0, SC1);
    const int col = h * 32 + (lane & 7) * 4;
    float f[4] = {0.f, 0.f, 0.f, 0.f};
    {
      u32x4 t[9];
#pragma unroll
      for (int i = 0; i < 9; ++i) t[i] = (ABL & 6) ? u32x4{0u,0u,0u,0u} : LD128(rws, WS_MLP + ((lane >> 3) * NB + b) * (DM * 4) + col * 4, i * 8 * NB * DM * 4, AUX_HO);
#pragma unroll
      for (int i = 0; i < 9; ++i)
#pragma unroll
        for (int e = 0; e < 4; ++e) f[e] += __uint_as_float(t[i][e]);
    }
    const u32x2 xv = LD64(mk(a.x, NB * DM * 2), (b * DM + col) * 2, 0, NT);
    float g[4];
#pragma unroll
    for (int e = 0; e < 4; ++e) { float t = f[e]; t += dpp<ROR8>(t); g[e] = rb(silu(rb(sumrows(t)))); }
    USE(xv);
    TS(13);
    BAR();
    u32x4 t[4];
    const u64 start2 = now();
    while (ok) {
      bool all = true;
#pragma unroll
      for (int i = 0; i < 4; ++i) t[i] = LD128(rws, WS_ATT + (b * NH + (lane >> 3)) * (DM * 4) + col * 4, i * 8 * DM * 4, AUX_HO);
#pragma unroll
      for (int i = 0; i < 4; ++i) all &= t[i][0] != EMPTY && t[i][1] != EMPTY && t[i][2] != EMPTY && t[i][3] != EMPTY;
      if (__all(all)) break;
      if (now() - start2 > SPIN_CAP) ok = false;
      asm volatile("" ::: "memory");
    }
    float y[4];
#pragma unroll
    for (int e = 0; e < 4; ++e) {
      float s = (__uint_as_float(t[0][e]) + __uint_as_float(t[1][e])) + (__uint_as_float(t[2][e]) + __uint_as_float(t[3][e]));
      s += dpp<ROR8>(s);
      s = rb(sumrows(s));
      const float xe = (e & 1) ? hi16(xv[e >> 1]) : lo16(xv[e >> 1]);
      y[e] = ok ? xe + rb(s * g[e]) : __uint_as_float(0x7fc00000u);
    }
    if (lane < 8) ST64((u32x2{pk(y[0], y[1]), pk(y[2], y[3])}), mk(a.out, NB * DM * 2), (b * DM + col) * 2, 0, 0);
    TS(14);
  } else {
    BAR();
  }
}

}

extern "C" void kernel_launch(void* const* d_in, const int* in_sizes, int n_in, void* d_out, int out_size, void* d_ws, size_t ws_size,
                              hipStream_t stream) {
  Args a;
  a.x = (const char*)d_in[0]; a.k = (const char*)d_in[1]; a.v = (const char*)d_in[2]; a.wq = (const char*)d_in[3]; a.wo = (const char*)d_in[4];
  a.w1 = (const char*)d_in[6]; a.w3 = (const char*)d_in[7]; a.w2 = (const char*)d_in[8];
  a.out = (char*)d_out; a.ws = (char*)d_ws;
  hipLaunchKernelGGL(fused_decode, dim3(NB * NH), dim3(1024), 0, stream, a);
}
```

```cpp
#include <hip/hip_runtime.h>
#include <hip/hip_bf16.h>
#include <cstdint>

#define PF 5
#define EXT0 1
#define WOD 2
#define ABL 0
#define KAUX 2
#define VAUX 2
#define WAUX 2
#define AUX_HO 16

namespace {

typedef unsigned u32;
typedef unsigned long long u64;
typedef unsigned short u16;
typedef u32 u32x4 __attribute__((ext_vector_type(4)));
typedef u32 u32x2 __attribute__((ext_vector_type(2)));
typedef float f32x4 __attribute__((ext_vector_type(4)));
typedef __bf16 bf16x8 __attribute__((ext_vector_type(8)));
typedef __bf16 bf16x2 __attribute__((ext_vector_type(2)));
#define GAS __attribute__((address_space(1)))
#define LAS __attribute__((address_space(3)))

constexpr int NB = 8, DM = 576, NH = 32, SK = 4096, DH = 128, FF = 4608;
constexpr int NTASK = FF / 64;
constexpr int NCONS = DM / 32;
constexpr int NT = 2, SC1 = 16;

constexpr u32 WS_EPOCH = 0, WS_F1 = 256, WS_F2 = WS_F1 + 256 * 8, WS_ATT = 4096;
constexpr u32 WS_MLP = WS_ATT + NB * NH * DM * 4, WS_END = WS_MLP + NTASK * NB * DM * 4;
constexpr u32 WS_DBG = 4u << 20;
constexpr u32 EMPTY = 0xffffffffu;

constexpr int XN_LD = 1168;
constexpr int L_XN = 0, L_QP = L_XN + NB * XN_LD, L_QB = L_QP + 12 * DH * 4, L_PS = L_QB + DH * 2, L_WM = L_PS + SK * 2, L_WS = L_WM + 64,
              L_OP = L_WS + 64, L_OB = L_OP + 4 * DH * 4, L_TG = L_OB + 9 * DH * 2, L_END = L_TG + 16;

struct Args { const char* x; const char* k; const char* v; const char* wq; const char* wo; const char* w1; const char* w3; const char* w2; char* out; char* ws; };

typedef __amdgpu_buffer_rsrc_t rsrc_t;
__device__ __forceinline__ rsrc_t mk(const char* p, u32 n) { return __builtin_amdgcn_make_buffer_rsrc((void*)p, 0, n, 0x00020000); }
#define LD128(r, vo, so, aux) __builtin_amdgcn_raw_buffer_load_b128(r, vo, so, aux)
#define LD64(r, vo, so, aux) __builtin_amdgcn_raw_buffer_load_b64(r, vo, so, aux)
#define LD32(r, vo, so, aux) __builtin_amdgcn_raw_buffer_load_b32(r, vo, so, aux)
#define ST128(v, r, vo, so, aux) __builtin_amdgcn_raw_buffer_store_b128(v, r, vo, so, aux)
#define ST64(v, r, vo, so, aux) __builtin_amdgcn_raw_buffer_store_b64(v, r, vo, so, aux)
#define PIN() __builtin_amdgcn_sched_barrier(0)
#define USE(x) asm volatile("" ::"v"(x) : "memory")
#define BAR() asm volatile("s_waitcnt lgkmcnt(0)\n\ts_barrier" ::: "memory")
#define DRAIN() asm volatile("s_waitcnt vmcnt(0)" ::: "memory")

__device__ __forceinline__ u32 pk(float lo, float hi) { bf16x2 t = {(__bf16)lo, (__bf16)hi}; return __builtin_bit_cast(u32, t); }
__device__ __forceinline__ float rb(float x) { return __uint_as_float(pk(x, x) << 16); }
__device__ __forceinline__ float lo16(u32 v) { return __uint_as_float(v << 16); }
__device__ __forceinline__ float hi16(u32 v) { return __uint_as_float(v & 0xffff0000u); }
__device__ __forceinline__ float dot2(u32 a, u32 b, float c) {
  return __builtin_amdgcn_fdot2_f32_bf16(__builtin_bit_cast(bf16x2, a), __builtin_bit_cast(bf16x2, b), c, false);
}
__device__ __forceinline__ f32x4 mfma(u32x4 a, u32x4 b, f32x4 c) {
  return __builtin_amdgcn_mfma_f32_16x16x32_bf16(__builtin_bit_cast(bf16x8, a), __builtin_bit_cast(bf16x8, b), c, 0, 0, 0);
}
template <int CTRL> __device__ __forceinline__ float dpp(float x) {
  return __builtin_bit_cast(float, __builtin_amdgcn_mov_dpp(__builtin_bit_cast(int, x), CTRL, 0xf, 0xf, true));
}
constexpr int X1 = 0xB1, X2 = 0x4E, HMIR = 0x141, MIR = 0x140, ROR8 = 0x128;
__device__ __forceinline__ float sum16(float x) { x += dpp<X1>(x); x += dpp<X2>(x); x += dpp<HMIR>(x); x += dpp<MIR>(x); return x; }
__device__ __forceinline__ float max16(float x) { x = fmaxf(x, dpp<X1>(x)); x = fmaxf(x, dpp<X2>(x)); x = fmaxf(x, dpp<HMIR>(x)); x = fmaxf(x, dpp<MIR>(x)); return x; }
__device__ __forceinline__ float sumrows(float x) {
  auto s = __builtin_amdgcn_permlane16_swap(__float_as_uint(x), __float_as_uint(x), false, false);
  x = __uint_as_float(s[0]) + __uint_as_float(s[1]);
  auto t = __builtin_amdgcn_permlane32_swap(__float_as_uint(x), __float_as_uint(x), false, false);
  return __uint_as_float(t[0]) + __uint_as_float(t[1]);
}
__device__ __forceinline__ float maxrows(float x) {
  auto s = __builtin_amdgcn_permlane16_swap(__float_as_uint(x), __float_as_uint(x), false, false);
  x = fmaxf(__uint_as_float(s[0]), __uint_as_float(s[1]));
  auto t = __builtin_amdgcn_permlane32_swap(__float_as_uint(x), __float_as_uint(x), false, false);
  return fmaxf(__uint_as_float(t[0]), __uint_as_float(t[1]));
}
__device__ __forceinline__ float silu(float x) { return x / (1.f + __expf(-x)); }
__device__ __forceinline__ u64 now() { return __builtin_amdgcn_s_memrealtime(); }
constexpr u64 SPIN_CAP = 5000000;

#define TS(i) do {} while (0)

struct Diag { u32 s[2][4]; };
__device__ __forceinline__ Diag make_diag(int c) {
  Diag d;
#pragma unroll
  for (int par = 0; par < 2; ++par)
#pragma unroll
    for (int w = 0; w < 4; ++w) {
      const u32 b = par ? 0x0302u : 0x0100u;
      d.s[par][w] = (c >> 1) == w ? ((c & 1) ? (b << 16) | 0x0c0cu : 0x0c0c0000u | b) : 0x0c0c0c0cu;
    }
  return d;
}
template <int PAR> __device__ __forceinline__ f32x4 axpy4(u32 val, u32x4 rows, const Diag& d, f32x4 acc) {
  u32x4 af;
#pragma unroll
  for (int w = 0; w < 4; ++w) af[w] = __builtin_amdgcn_perm(val, val, d.s[PAR][w]);
  return mfma(af, rows, acc);
}
__device__ __forceinline__ float score4(u32x4 k, u32x4 q) {
  float d = dot2(k[0], q[0], 0.f); d = dot2(k[1], q[1], d); d = dot2(k[2], q[2], d); d = dot2(k[3], q[3], d);
  return sum16(d);
}
__device__ __forceinline__ void tr8(const u32x2 (&raw)[8], u32x4 (&f)[4]) {
#pragma unroll
  for (int e = 0; e < 4; ++e)
#pragma unroll
    for (int d = 0; d < 4; ++d) f[e][d] = __builtin_amdgcn_perm(raw[2 * d + 1][e >> 1], raw[2 * d][e >> 1], (e & 1) ? 0x07060302u : 0x05040100u);
}

__global__ void __launch_bounds__(1024) fused_decode(Args a) {
  __shared__ __attribute__((aligned(16))) char lds_[L_END];
  LAS char* lds = (LAS char*)lds_;
  const int tid = threadIdx.x, lane = tid & 63, w = __builtin_amdgcn_readfirstlane(tid >> 6), r = lane >> 4, c = lane & 15;
  const int blk = blockIdx.x, b = blk >> 5, h = blk & 31;
  const char* kbase = a.k + ((size_t)blk << 20);
  const char* vbase = a.v + ((size_t)blk << 20);
  const rsrc_t rk = mk(kbase, 1u << 20);
  const int sw = w & 3;
  const int vA = lane * 16 + sw * 4096, vB = vA + 16384;
#define KVLD_(rs, beta, i, AUX) LD128(rs, ((i) < 4 ? vA : vB) + ((i) & 3) * 1024, (beta) * 32768, AUX)
#define KVLD(rs, beta, i) KVLD_(rs, beta, i, KAUX)
#define VLD(rs, beta, i) KVLD_(rs, beta, i, VAUX)
  float mx = -INFINITY;
  auto put_scores = [&](float sc, int batch) {
    const float s = rb(rb(sc) * 0.08838834764831845f);
    mx = fmaxf(mx, s);
    if (c < 8) *(LAS u16*)(lds + L_PS + batch * 64 + r * 16 + c * 2) = (u16)(__float_as_uint(s) >> 16);
  };
  auto softmax = [&]() {
    const float wm = maxrows(max16(mx));
    if (lane == 0) *(LAS float*)(lds + L_WM + w * 4) = wm;
    BAR();
    const float M = max16(*(LAS float*)(lds + L_WM + c * 4));
    const u32x2 sv = *(LAS u32x2*)(lds + L_PS + tid * 8);
    const float e0 = __expf(lo16(sv[0]) - M), e1 = __expf(hi16(sv[0]) - M), e2 = __expf(lo16(sv[1]) - M), e3 = __expf(hi16(sv[1]) - M);
    const float ws = sumrows(sum16((e0 + e1) + (e2 + e3)));
    if (lane == 0) *(LAS float*)(lds + L_WS + w * 4) = ws;
    BAR();
    const float inv = 1.f / sum16(*(LAS float*)(lds + L_WS + c * 4));
    *(LAS u32x2*)(lds + L_PS + tid * 8) = u32x2{pk(e0 * inv, e1 * inv), pk(e2 * inv, e3 * inv)};
    BAR();
  };

  if (w >= 12) {
    __builtin_amdgcn_s_setprio(2);
    TS(0);
    u32x4 ring[8], ext[8];
#pragma unroll
    for (int i = 0; i < (EXT0 ? 8 : PF); ++i) ring[i] = KVLD(rk, 3, i);
    if (EXT0) {
#pragma unroll
      for (int i = 0; i < 8; ++i) ext[i] = KVLD(rk, 4, i);
    }
    PIN();
    BAR(); BAR(); BAR();
    TS(1);
    const u32x4 q = *(LAS u32x4*)(lds + L_QB + c * 16);
    float sc = 0.f;
    if (EXT0) {
#pragma unroll
      for (int i = 0; i < 8; ++i) { const float d = score4(ring[i], q); sc = (c & 7) == i ? d : sc; PIN(); }
      put_scores(sc, 12 + sw);
#pragma unroll
      for (int i = 0; i < 8; ++i) {
        if (i + PF >= 8) ring[i + PF - 8] = KVLD(rk, 5, i + PF - 8);
        PIN();
        const float d = score4(ext[i], q); sc = (c & 7) == i ? d : sc;
        PIN();
      }
      put_scores(sc, 16 + sw);
    }
#pragma unroll 1
    for (int beta = EXT0 ? 5 : 3; beta < 32; ++beta) {
      const rsrc_t rn = mk(beta < 31 ? kbase : vbase, 1u << 20);
      const int bn = beta < 31 ? beta + 1 : 0;
#pragma unroll
      for (int i = 0; i < 8; ++i) {
        if (i + PF < 8) ring[i + PF] = KVLD(rk, beta, i + PF); else ring[i + PF - 8] = KVLD(rn, bn, i + PF - 8);
        PIN();
        const float d = score4(ring[i], q); sc = (c & 7) == i ? d : sc;
        PIN();
      }
      put_scores(sc, beta * 4 + sw);
    }
    const rsrc_t rv = mk(vbase, 1u << 20);
#pragma unroll
    for (int i = PF; i < 8; ++i) ring[i] = VLD(rv, 0, i);
#pragma unroll
    for (int i = 0; i < 8; ++i) ext[i] = VLD(rv, 1, i);
    PIN();
    TS(2);
    softmax();
    TS(3);
    const Diag dg = make_diag(c);
    f32x4 acc = {0.f, 0.f, 0.f, 0.f};
    const int po = L_PS + sw * 64 + r * 16;
    u32x4 pv = *(LAS u32x4*)(lds + po), pn = *(LAS u32x4*)(lds + po + 256);
#define VSTEP(i, reg) acc = ((i) & 1) ? axpy4<1>(pv[(i) >> 1], reg, dg, acc) : axpy4<0>(pv[(i) >> 1], reg, dg, acc)
#pragma unroll
    for (int i = 0; i < 8; ++i) { VSTEP(i, ring[i]); PIN(); }
    pv = pn; pn = *(LAS u32x4*)(lds + po + 512);
#pragma unroll
    for (int i = 0; i < 8; ++i) {
      if (i + PF >= 8) ring[i + PF - 8] = VLD(rv, 2, i + PF - 8);
      PIN();
      VSTEP(i, ext[i]);
      PIN();
    }
#pragma unroll 1
    for (int beta = 2; beta < 32; ++beta) {
      const rsrc_t rn = mk(vbase, beta < 31 ? 1u << 20 : 0u);
      pv = pn; pn = *(LAS u32x4*)(lds + po + (beta < 31 ? beta + 1 : 31) * 256);
#pragma unroll
      for (int i = 0; i < 8; ++i) {
        if (i + PF < 8) ring[i + PF] = VLD(rv, beta, i + PF); else ring[i + PF - 8] = VLD(rn, beta < 31 ? beta + 1 : 0, i + PF - 8);
        PIN();
        VSTEP(i, ring[i]);
        PIN();
      }
    }
    if (r < 2) *(LAS f32x4*)(lds + L_OP + sw * 512 + c * 32 + r * 16) = acc;
    TS(4);
    BAR();
    return;
  }

  const rsrc_t rws = mk(a.ws, WS_END);
  u32 xr[5];
  if (w < 8) {
    const rsrc_t rx = mk(a.x + w * (DM * 2), DM * 2);
#pragma unroll
    for (int i = 0; i < 5; ++i) xr[i] = LD32(rx, lane * 4 + i * 256, 0, NT);
  }
  u32x2 ep = {0u, 0u};
  if (w == 10) ep = LD64(rws, WS_EPOCH, 0, AUX_HO);
  u32x4 wb[12], kb[8];
  {
    const rsrc_t rq = mk(a.wq + h * 256, DM * 8192 - h * 256);
    const int vo = (48 * w + 12 * r) * 8192 + c * 16;
#pragma unroll
    for (int i = 0; i < 12; ++i) wb[i] = LD128(rq, vo, i * 8192, WAUX);
#pragma unroll
    for (int i = 0; i < 8; ++i) kb[i] = KVLD(rk, w >> 2, i);
  }
  PIN();
  if (w < 8) {
    float s = 0.f;
#pragma unroll
    for (int i = 0; i < 5; ++i) s += lo16(xr[i]) + hi16(xr[i]);
    const float mean = sumrows(sum16(s)) * (1.f / DM);
    float ss = 0.f;
#pragma unroll
    for (int i = 0; i < 5; ++i) {
      const float d0 = lo16(xr[i]) - mean, d1 = hi16(xr[i]) - mean;
      ss += (i < 4 || lane < 32) ? d0 * d0 + d1 * d1 : 0.f;
    }
    const float var = sumrows(sum16(ss)) * (1.f / DM);
    const float m = rb(mean), den = rb(sqrtf(rb(rb(var) + 1e-5f)));
#pragma unroll
    for (int i = 0; i < 5; ++i)
      if (i < 4 || lane < 32) *(LAS u32*)(lds + L_XN + w * XN_LD + lane * 4 + i * 256) = pk(rb(lo16(xr[i]) - m) / den, rb(hi16(xr[i]) - m) / den);
  }
  u64 tag = 0;
  if (w == 10) {
    tag = (((u64)(u32)__builtin_amdgcn_readfirstlane(ep[1]) << 32) | (u32)__builtin_amdgcn_readfirstlane(ep[0])) + 1;
    if (lane == 0) *(LAS u64*)(lds + L_TG) = tag;
  }
  BAR();
  {
    const Diag dg = make_diag(c);
    const int xo = L_XN + b * XN_LD + (48 * w + 12 * r) * 2;
    const u32x2 x0 = *(LAS u32x2*)(lds + xo), x1 = *(LAS u32x2*)(lds + xo + 8), x2 = *(LAS u32x2*)(lds + xo + 16);
    const u32 xv[6] = {x0[0], x0[1], x1[0], x1[1], x2[0], x2[1]};
    f32x4 acc = {0.f, 0.f, 0.f, 0.f};
#pragma unroll
    for (int i = 0; i < 12; ++i) acc = (i & 1) ? axpy4<1>(xv[i >> 1], wb[i], dg, acc) : axpy4<0>(xv[i >> 1], wb[i], dg, acc);
    if (r < 2) *(LAS f32x4*)(lds + L_QP + w * 512 + c * 32 + r * 16) = acc;
  }
  BAR();
  if (w < 2) {
    float s = 0.f;
#pragma unroll
    for (int j = 0; j < 12; ++j) s += *(LAS float*)(lds + L_QP + j * 512 + tid * 4);
    *(LAS u16*)(lds + L_QB + tid * 2) = (u16)pk(s, s);
  }
  BAR();
  {
    const u32x4 q = *(LAS u32x4*)(lds + L_QB + c * 16);
    float sc = 0.f;
#pragma unroll
    for (int i = 0; i < 8; ++i) { const float d = score4(kb[i], q); sc = (c & 7) == i ? d : sc; }
    put_scores(sc, w);
  }
  PIN();

  if (w < 9) {
    u32x4 wf[4][4];
    const rsrc_t ro = mk(a.wo + (size_t)h * (DH * DM * 2), DH * DM * 2);
    const int vo = r * 8 * (DM * 2) + w * 128 + c * 8;
    u32x2 raw[4][8];
#pragma unroll
    for (int n = 0; n < 32; ++n) {
      raw[n >> 3][n & 7] = (ABL & 8) ? u32x2{1u,1u} : LD64(ro, vo, ((n >> 3) * 32 + (n & 7)) * (DM * 2), WAUX);
      if (n >= WOD) USE(raw[(n - WOD) >> 3][(n - WOD) & 7]);
      if (n >= WOD + 7 && ((n - WOD) & 7) == 7) tr8(raw[(n - WOD) >> 3], wf[(n - WOD) >> 3]);
      PIN();
    }
    tr8(raw[3], wf[3]);
    TS(8);
    PIN();
    softmax();
    BAR();
    TS(10);
    float o0 = 0.f, o1 = 0.f;
#pragma unroll
    for (int j = 0; j < 4; ++j) { const u32x2 t = *(LAS u32x2*)(lds + L_OP + j * 512 + lane * 8); o0 += __uint_as_float(t[0]); o1 += __uint_as_float(t[1]); }
    *(LAS u32*)(lds + L_OB + w * 256 + lane * 4) = pk(o0, o1);
    asm volatile("s_waitcnt lgkmcnt(0)" ::: "memory");
    f32x4 d[4];
#pragma unroll
    for (int e = 0; e < 4; ++e) d[e] = f32x4{0.f, 0.f, 0.f, 0.f};
#pragma unroll
    for (int s = 0; s < 4; ++s) {
      const u32x4 ob = *(LAS u32x4*)(lds + L_OB + w * 256 + s * 64 + r * 16);
#pragma unroll
      for (int e = 0; e < 4; ++e) d[e] = mfma(wf[s][e], ob, d[e]);
    }
    u32x4 o;
#pragma unroll
    for (int e = 0; e < 4; ++e) {
      const float t = (c & 2) ? ((c & 1) ? d[e][3] : d[e][2]) : ((c & 1) ? d[e][1] : d[e][0]);
      o[e] = min(__float_as_uint(t), EMPTY - 1);
    }
    if (c < 4) ST128(o, rws, WS_ATT + blk * (DM * 4) + w * 256 + r * 64 + c * 16, 0, SC1);
    TS(11);
    return;
  }
  if (w == 9) {
    u32x4 hf[2] = {};
    if (blk < NTASK && !(ABL & 2)) {
      const rsrc_t r1 = mk(a.w1 + blk * 128, DM * FF * 2 - blk * 128), r3 = mk(a.w3 + blk * 128, DM * FF * 2 - blk * 128);
      const int vo = r * 8 * (FF * 2) + c * 8;
      const int xo = L_XN + (c & 7) * XN_LD + r * 16;
      u32x2 ra[8], rc[8];
      f32x4 g[4], u[4];
#pragma unroll
      for (int e = 0; e < 4; ++e) { g[e] = f32x4{0.f, 0.f, 0.f, 0.f}; u[e] = g[e]; }
#pragma unroll
      for (int i = 0; i < 8; ++i) ra[i] = LD64(r1, vo, i * (FF * 2), WAUX);
#pragma unroll
      for (int i = 0; i < 8; ++i) rc[i] = LD64(r3, vo, i * (FF * 2), WAUX);
#pragma unroll 1
      for (int s = 0; s < 18; ++s) {
        const int sn = (s < 17 ? s + 1 : 17) * 32 * (FF * 2);
        const rsrc_t n1 = mk(a.w1 + blk * 128, s < 17 ? DM * FF * 2 - blk * 128 : 0), n3 = mk(a.w3 + blk * 128, s < 17 ? DM * FF * 2 - blk * 128 : 0);
        const u32x4 xb = *(LAS u32x4*)(lds + xo + s * 64);
        u32x4 f[4];
        tr8(ra, f);
        PIN();
#pragma unroll
        for (int i = 0; i < 8; ++i) ra[i] = LD64(n1, vo, sn + i * (FF * 2), WAUX);
        PIN();
#pragma unroll
        for (int e = 0; e < 4; ++e) g[e] = mfma(f[e], xb, g[e]);
        PIN();
        tr8(rc, f);
        PIN();
#pragma unroll
        for (int i = 0; i < 8; ++i) rc[i] = LD64(n3, vo, sn + i * (FF * 2), WAUX);
        PIN();
#pragma unroll
        for (int e = 0; e < 4; ++e) u[e] = mfma(f[e], xb, u[e]);
        PIN();
      }
      float hv[4][4];
#pragma unroll
      for (int p = 0; p < 4; ++p)
#pragma unroll
        for (int e = 0; e < 4; ++e) hv[p][e] = rb(silu(rb(g[e][p]))) * rb(u[e][p]);
#pragma unroll
      for (int s = 0; s < 2; ++s) hf[s] = u32x4{pk(hv[2 * s][0], hv[2 * s][1]), pk(hv[2 * s][2], hv[2 * s][3]), pk(hv[2 * s + 1][0], hv[2 * s + 1][1]), pk(hv[2 * s + 1][2], hv[2 * s + 1][3])};
      TS(9);
    }
    PIN();
    softmax();
    if (blk < NTASK && !(ABL & 2)) {
      const rsrc_t r2 = mk(a.w2 + (size_t)blk * (64 * DM * 2), 64 * DM * 2);
      const int vo = r * 16 * (DM * 2) + c * 8;
      u32x2 ra[8], rc[8];
#pragma unroll
      for (int i = 0; i < 8; ++i) ra[i] = LD64(r2, vo, i * (DM * 2), WAUX);
#pragma unroll
      for (int i = 0; i < 8; ++i) rc[i] = LD64(r2, vo, (8 + i) * (DM * 2), WAUX);
      const int so = WS_MLP + (blk * NB + (c & 7)) * (DM * 4) + r * 64 + (c >> 3) * 32;
#pragma unroll 1
      for (int W = 0; W < 9; ++W) {
        const rsrc_t n2 = mk(a.w2 + (size_t)blk * (64 * DM * 2), W < 8 ? 64 * DM * 2 : 0);
        const int sn = (W < 8 ? W + 1 : 8) * 128;
        u32x4 f[4];
        f32x4 d[4];
        tr8(ra, f);
        PIN();
#pragma unroll
        for (int i = 0; i < 8; ++i) ra[i] = LD64(n2, vo, sn + i * (DM * 2), WAUX);
        PIN();
#pragma unroll
        for (int e = 0; e < 4; ++e) d[e] = mfma(f[e], hf[0], f32x4{0.f, 0.f, 0.f, 0.f});
        PIN();
        tr8(rc, f);
        PIN();
#pragma unroll
        for (int i = 0; i < 8; ++i) rc[i] = LD64(n2, vo, sn + (8 + i) * (DM * 2), WAUX);
        PIN();
#pragma unroll
        for (int e = 0; e < 4; ++e) d[e] = mfma(f[e], hf[1], d[e]);
#pragma unroll
        for (int p = 0; p < 2; ++p) {
          u32x4 o;
#pragma unroll
          for (int e = 0; e < 4; ++e) o[e] = __float_as_uint((c & 8) ? d[e][2 + p] : d[e][p]);
          if (!(ABL & 4)) ST128(o, rws, so + p * 16, W * 256, SC1); else USE(o);
        }
        PIN();
      }
      DRAIN();
      const u32x2 tg = *(LAS u32x2*)(lds + L_TG);
      if (lane == 0) ST64(tg, rws, WS_F2 + blk * 8, 0, SC1);
      TS(12);
    }
    BAR();
    return;
  }
  if (w == 10) {
    if (lane < 48 && !(ABL & 16)) {
#pragma unroll
      for (int i = 0; i < 3; ++i) ST128((u32x4{EMPTY, EMPTY, EMPTY, EMPTY}), rws, WS_ATT + blk * (DM * 4) + lane * 16 + i * 768, 0, SC1);
    }
    DRAIN();
    if (lane == 0) ST64((u32x2{(u32)tag, (u32)(tag >> 32)}), rws, WS_F1 + blk * 8, 0, SC1);
  }
  PIN();
  softmax();
  if (w == 10 && h < NCONS) {
    bool ok = true;
    const u32 t0 = (u32)tag, t1 = (u32)(tag >> 32);
    const u64 start = now();
    if (ABL & 1) while (now() - start < 2600) __builtin_amdgcn_s_sleep(32);
    for (;;) {
      if (ABL & 22) break;
      bool all = true;
#pragma unroll
      for (int i = 0; i < 2; ++i) { const u32x4 f = LD128(rws, WS_F1 + lane * 16 + i * 1024, 0, AUX_HO); all &= f[0] == t0 && f[1] == t1 && f[2] == t0 && f[3] == t1; }
      { const u32x4 f = LD128(rws, WS_F2 + (lane < 36 ? lane : 0) * 16, 0, AUX_HO); all &= f[0] == t0 && f[1] == t1 && f[2] == t0 && f[3] == t1; }
      if (__all(all)) break;
      if (now() - start > SPIN_CAP) { ok = false; break; }
      __builtin_amdgcn_s_sleep(32);
    }
    asm volatile("" ::: "memory");
    if (blk == 0 && lane == 0) ST64((u32x2{t0, t1}), rws, WS_EPOCH, 0, SC1);
    const int col = h * 32 + (lane & 7) * 4;
    float f[4] = {0.f, 0.f, 0.f, 0.f};
    {
      u32x4 t[9];
#pragma unroll
      for (int i = 0; i < 9; ++i) t[i] = (ABL & 6) ? u32x4{0u,0u,0u,0u} : LD128(rws, WS_MLP + ((lane >> 3) * NB + b) * (DM * 4) + col * 4, i * 8 * NB * DM * 4, AUX_HO);
#pragma unroll
      for (int i = 0; i < 9; ++i)
#pragma unroll
        for (int e = 0; e < 4; ++e) f[e] += __uint_as_float(t[i][e]);
    }
    const u32x2 xv = LD64(mk(a.x, NB * DM * 2), (b * DM + col) * 2, 0, NT);
    float g[4];
#pragma unroll
    for (int e = 0; e < 4; ++e) { float t = f[e]; t += dpp<ROR8>(t); g[e] = rb(silu(rb(sumrows(t)))); }
    USE(xv);
    TS(13);
    BAR();
    u32x4 t[4];
    const u64 start2 = now();
    while (ok) {
      bool all = true;
#pragma unroll
      for (int i = 0; i < 4; ++i) t[i] = LD128(rws, WS_ATT + (b * NH + (lane >> 3)) * (DM * 4) + col * 4, i * 8 * DM * 4, AUX_HO);
#pragma unroll
      for (int i = 0; i < 4; ++i) all &= t[i][0] != EMPTY && t[i][1] != EMPTY && t[i][2] != EMPTY && t[i][3] != EMPTY;
      if (__all(all)) break;
      if (now() - start2 > SPIN_CAP) ok = false;
      asm volatile("" ::: "memory");
    }
    float y[4];
#pragma unroll
    for (int e = 0; e < 4; ++e) {
      float s = (__uint_as_float(t[0][e]) + __uint_as_float(t[1][e])) + (__uint_as_float(t[2][e]) + __uint_as_float(t[3][e]));
      s += dpp<ROR8>(s);
      s = rb(sumrows(s));
      const float xe = (e & 1) ? hi16(xv[e >> 1]) : lo16(xv[e >> 1]);
      y[e] = ok ? xe + rb(s * g[e]) : __uint_as_float(0x7fc00000u);
    }
    if (lane < 8) ST64((u32x2{pk(y[0], y[1]), pk(y[2], y[3])}), mk(a.out, NB * DM * 2), (b * DM + col) * 2, 0, 0);
    TS(14);
  } else {
    BAR();
  }
}

}

extern "C" void kernel_launch(void* const* d_in, const int* in_sizes, int n_in, void* d_out, int out_size, void* d_ws, size_t ws_size,
                              hipStream_t stream) {
  Args a;
  a.x = (const char*)d_in[0]; a.k = (const char*)d_in[1]; a.v = (const char*)d_in[2]; a.wq = (const char*)d_in[3]; a.wo = (const char*)d_in[4];
  a.w1 = (const char*)d_in[6]; a.w3 = (const char*)d_in[7]; a.w2 = (const char*)d_in[8];
  a.out = (char*)d_out; a.ws = (char*)d_ws;
  hipLaunchKernelGGL(fused_decode, dim3(NB * NH), dim3(1024), 0, stream, a);
}
```

```cpp
#include <hip/hip_runtime.h>
#include <hip/hip_bf16.h>
#include <cstdint>

#define PF 5
#define DEEP_MLP 0x11111111u
#define DEEP_ALL 0u
#define INIT 8
#define VEXT 8
#define ALIGN 512
#define ROT 1
#define WOD 1
#define ABL 0
#define KAUX 2
#define VAUX 2
#define WAUX 2
#define AUX_HO 16

namespace {

typedef unsigned u32;
typedef unsigned long long u64;
typedef unsigned short u16;
typedef u32 u32x4 __attribute__((ext_vector_type(4)));
typedef u32 u32x2 __attribute__((ext_vector_type(2)));
typedef float f32x4 __attribute__((ext_vector_type(4)));
typedef __bf16 bf16x8 __attribute__((ext_vector_type(8)));
typedef __bf16 bf16x2 __attribute__((ext_vector_type(2)));
#define GAS __attribute__((address_space(1)))
#define LAS __attribute__((address_space(3)))

constexpr int NB = 8, DM = 576, NH = 32, SK = 4096, DH = 128, FF = 4608;
constexpr int NTASK = FF / 64;
constexpr int NCONS = DM / 32;
constexpr int NT = 2, SC1 = 16;

constexpr u32 WS_EPOCH = 0, WS_F1 = 256, WS_F2 = WS_F1 + 256 * 8, WS_ATT = 4096;
constexpr u32 WS_MLP = WS_ATT + NB * NH * DM * 4, WS_END = WS_MLP + NTASK * NB * DM * 4;
constexpr u32 WS_DBG = 4u << 20;
constexpr u32 EMPTY = 0xffffffffu;

constexpr int XN_LD = 1168;
constexpr int L_XN = 0, L_QP = L_XN + NB * XN_LD, L_QB = L_QP + 12 * DH * 4, L_PS = L_QB + DH * 2, L_WM = L_PS + SK * 2, L_WS = L_WM + 64,
              L_OP = L_WS + 64, L_OB = L_OP + 4 * DH * 4, L_TG = L_OB + 9 * DH * 2, L_XB = L_TG + 16, L_MO = L_XB + DM * 2, L_END = L_MO + NB * DM * 4;

struct Args { const char* x; const char* k; const char* v; const char* wq; const char* wo; const char* w1; const char* w3; const char* w2; char* out; char* ws; };

typedef __amdgpu_buffer_rsrc_t rsrc_t;
__device__ __forceinline__ rsrc_t mk(const char* p, u32 n) { return __builtin_amdgcn_make_buffer_rsrc((void*)p, 0, n, 0x00020000); }
#define LD128(r, vo, so, aux) __builtin_amdgcn_raw_buffer_load_b128(r, vo, so, aux)
#define LD64(r, vo, so, aux) __builtin_amdgcn_raw_buffer_load_b64(r, vo, so, aux)
#define LD32(r, vo, so, aux) __builtin_amdgcn_raw_buffer_load_b32(r, vo, so, aux)
#define ST128(v, r, vo, so, aux) __builtin_amdgcn_raw_buffer_store_b128(v, r, vo, so, aux)
#define ST64(v, r, vo, so, aux) __builtin_amdgcn_raw_buffer_store_b64(v, r, vo, so, aux)
#define PIN() __builtin_amdgcn_sched_barrier(0)
#define USE(x) asm volatile("" ::"v"(x) : "memory")
#define BAR() asm volatile("s_waitcnt lgkmcnt(0)\n\ts_barrier" ::: "memory")
#define LWAIT() asm volatile("s_waitcnt lgkmcnt(0)" ::: "memory")
#define DRAIN() asm volatile("s_waitcnt vmcnt(0)" ::: "memory")

__device__ __forceinline__ u32 pk(float lo, float hi) { bf16x2 t = {(__bf16)lo, (__bf16)hi}; return __builtin_bit_cast(u32, t); }
__device__ __forceinline__ float rb(float x) { return __uint_as_float(pk(x, x) << 16); }
__device__ __forceinline__ float lo16(u32 v) { return __uint_as_float(v << 16); }
__device__ __forceinline__ float hi16(u32 v) { return __uint_as_float(v & 0xffff0000u); }
__device__ __forceinline__ float dot2(u32 a, u32 b, float c) {
  return __builtin_amdgcn_fdot2_f32_bf16(__builtin_bit_cast(bf16x2, a), __builtin_bit_cast(bf16x2, b), c, false);
}
__device__ __forceinline__ f32x4 mfma(u32x4 a, u32x4 b, f32x4 c) {
  return __builtin_amdgcn_mfma_f32_16x16x32_bf16(__builtin_bit_cast(bf16x8, a), __builtin_bit_cast(bf16x8, b), c, 0, 0, 0);
}
template <int CTRL> __device__ __forceinline__ float dpp(float x) {
  return __builtin_bit_cast(float, __builtin_amdgcn_mov_dpp(__builtin_bit_cast(int, x), CTRL, 0xf, 0xf, true));
}
constexpr int X1 = 0xB1, X2 = 0x4E, HMIR = 0x141, MIR = 0x140, ROR8 = 0x128;
__device__ __forceinline__ float sum16(float x) { x += dpp<X1>(x); x += dpp<X2>(x); x += dpp<HMIR>(x); x += dpp<MIR>(x); return x; }
__device__ __forceinline__ float max16(float x) { x = fmaxf(x, dpp<X1>(x)); x = fmaxf(x, dpp<X2>(x)); x = fmaxf(x, dpp<HMIR>(x)); x = fmaxf(x, dpp<MIR>(x)); return x; }
__device__ __forceinline__ float sumrows(float x) {
  auto s = __builtin_amdgcn_permlane16_swap(__float_as_uint(x), __float_as_uint(x), false, false);
  x = __uint_as_float(s[0]) + __uint_as_float(s[1]);
  auto t = __builtin_amdgcn_permlane32_swap(__float_as_uint(x), __float_as_uint(x), false, false);
  return __uint_as_float(t[0]) + __uint_as_float(t[1]);
}
__device__ __forceinline__ float maxrows(float x) {
  auto s = __builtin_amdgcn_permlane16_swap(__float_as_uint(x), __float_as_uint(x), false, false);
  x = fmaxf(__uint_as_float(s[0]), __uint_as_float(s[1]));
  auto t = __builtin_amdgcn_permlane32_swap(__float_as_uint(x), __float_as_uint(x), false, false);
  return fmaxf(__uint_as_float(t[0]), __uint_as_float(t[1]));
}
__device__ __forceinline__ float fdiv(float a, float d, float rd) { const float q = a * rd; return fmaf(fmaf(-q, d, a), rd, q); }
__device__ __forceinline__ float silu(float x) { const float d = 1.f + __expf(-x); return d < INFINITY ? fdiv(x, d, __builtin_amdgcn_rcpf(d)) : -0.f; }
__device__ __forceinline__ u64 now() { return __builtin_amdgcn_s_memrealtime(); }
constexpr u64 SPIN_CAP = 5000000;

#define TS(i) do {} while (0)
#define TSW(ww, i) do {} while (0)

struct Diag { u32 s[2][4]; };
__device__ __forceinline__ Diag make_diag(int c) {
  Diag d;
#pragma unroll
  for (int par = 0; par < 2; ++par)
#pragma unroll
    for (int w = 0; w < 4; ++w) {
      const u32 b = par ? 0x0302u : 0x0100u;
      d.s[par][w] = (c >> 1) == w ? ((c & 1) ? (b << 16) | 0x0c0cu : 0x0c0c0000u | b) : 0x0c0c0c0cu;
    }
  return d;
}
template <int PAR> __device__ __forceinline__ f32x4 axpy4(u32 val, u32x4 rows, const Diag& d, f32x4 acc) {
  if (ABL & 128) { USE(rows); return acc; }
  u32x4 af;
#pragma unroll
  for (int w = 0; w < 4; ++w) af[w] = __builtin_amdgcn_perm(val, val, d.s[PAR][w]);
  return mfma(af, rows, acc);
}
__device__ __forceinline__ float score4(u32x4 k, u32x4 q) {
  if (ABL & 128) { USE(k); return 1.f; }
  float d = dot2(k[0], q[0], 0.f); d = dot2(k[1], q[1], d); d = dot2(k[2], q[2], d); d = dot2(k[3], q[3], d);
  return sum16(d);
}
__device__ __forceinline__ void tr8(const u32x2 (&raw)[8], u32x4 (&f)[4]) {
#pragma unroll
  for (int e = 0; e < 4; ++e)
#pragma unroll
    for (int d = 0; d < 4; ++d) f[e][d] = __builtin_amdgcn_perm(raw[2 * d + 1][e >> 1], raw[2 * d][e >> 1], (e & 1) ? 0x07060302u : 0x05040100u);
}

__global__ void __launch_bounds__(1024) fused_decode(Args a) {
  __shared__ __attribute__((aligned(16))) char lds_[L_END];
  LAS char* lds = (LAS char*)lds_;
  const int tid = threadIdx.x, lane = tid & 63, w = __builtin_amdgcn_readfirstlane(tid >> 6), r = lane >> 4, c = lane & 15;
  const int blk = blockIdx.x, b = blk >> 5, h = blk & 31;
  const char* kbase = a.k + ((size_t)blk << 20);
  const char* vbase = a.v + ((size_t)blk << 20);
  const rsrc_t rk = mk(kbase, 1u << 20);
  const int sw = w & 3;
  const int vA = lane * 16 + sw * 4096, vB = vA + 16384;
#define KVLD_(rs, beta, i, AUX) LD128(rs, ((i) < 4 ? vA : vB) + ((i) & 3) * 1024, (beta) * 32768, AUX)
#define KVLD(rs, beta, i) KVLD_(rs, beta, i, KAUX)
#define VLD(rs, beta, i) KVLD_(rs, beta, i, VAUX)
  float mx = -INFINITY;
  auto put_scores = [&](float sc, int batch) {
    const float s = rb(rb(sc) * 0.08838834764831845f);
    mx = fmaxf(mx, s);
    if (c < 8) *(LAS u16*)(lds + L_PS + batch * 64 + r * 16 + c * 2) = (u16)(__float_as_uint(s) >> 16);
  };
  auto softmax = [&]() {
    const float wm = maxrows(max16(mx));
    if (lane == 0) *(LAS float*)(lds + L_WM + w * 4) = wm;
    BAR();
    const float M = max16(*(LAS float*)(lds + L_WM + c * 4));
    const u32x2 sv = *(LAS u32x2*)(lds + L_PS + tid * 8);
    const float e0 = __expf(lo16(sv[0]) - M), e1 = __expf(hi16(sv[0]) - M), e2 = __expf(lo16(sv[1]) - M), e3 = __expf(hi16(sv[1]) - M);
    const float ws = sumrows(sum16((e0 + e1) + (e2 + e3)));
    if (lane == 0) *(LAS float*)(lds + L_WS + w * 4) = ws;
    BAR();
    const float inv = 1.f / sum16(*(LAS float*)(lds + L_WS + c * 4));
    *(LAS u32x2*)(lds + L_PS + tid * 8) = u32x2{pk(e0 * inv, e1 * inv), pk(e2 * inv, e3 * inv)};
    BAR();
  };

  if (w >= 12) {
    TSW(12, 0);
    const u32 deep = (blk < NTASK ? DEEP_MLP : 0u) | DEEP_ALL;
    u32x4 ring[8], ext[8];
    BAR();
#pragma unroll
    for (int i = 0; i < (INIT < 8 ? INIT : 8); ++i) { ring[i] = KVLD(rk, 0, i); PIN(); }
    if (INIT > 8) {
#pragma unroll
      for (int i = 0; i < 8; ++i) { ext[i] = KVLD(rk, 1, i); PIN(); }
    }
    __builtin_amdgcn_s_setprio(2);
    BAR(); BAR();
    TSW(12, 1);
    const u32x4 q = *(LAS u32x4*)(lds + L_QB + c * 16);
    float sc = 0.f;
    if (INIT > 8) {
#pragma unroll
      for (int i = 0; i < 8; ++i) { const float d = score4(ring[i], q); sc = (c & 7) == i ? d : sc; PIN(); }
      put_scores(sc, sw);
#pragma unroll
      for (int i = 0; i < 8; ++i) {
        if (i + PF >= 8) { USE(ext[i]); ring[i + PF - 8] = KVLD(rk, 2, i + PF - 8); }
        PIN();
        const float d = score4(ext[i], q); sc = (c & 7) == i ? d : sc;
        PIN();
      }
      put_scores(sc, 4 + sw);
    } else {
#pragma unroll
      for (int i = 0; i < 8; ++i) {
        if (i + PF >= INIT) { USE(ring[i]); if (i + PF < 8) ring[i + PF] = KVLD(rk, 0, i + PF); else ring[i + PF - 8] = KVLD(rk, 1, i + PF - 8); }
        PIN();
        const float d = score4(ring[i], q); sc = (c & 7) == i ? d : sc;
        PIN();
      }
      put_scores(sc, sw);
    }
#pragma unroll 1
    for (int beta = INIT > 8 ? 2 : 1; beta < 32; ++beta) {
      const rsrc_t rn = mk(beta < 31 ? kbase : vbase, 1u << 20);
      const int bn = beta < 31 ? beta + 1 : 0;
      const bool shallow = !((deep >> beta) & 1);
#pragma unroll
      for (int i = 0; i < 8; ++i) {
        if (shallow) USE(ring[i]);
        if (i + PF < 8) ring[i + PF] = KVLD(rk, beta, i + PF); else ring[i + PF - 8] = KVLD(rn, bn, i + PF - 8);
        PIN();
        const float d = score4(ring[i], q); sc = (c & 7) == i ? d : sc;
        PIN();
      }
      put_scores(sc, beta * 4 + sw);
    }
    const rsrc_t rv = mk(vbase, 1u << 20);
#pragma unroll
    for (int i = PF; i < 8; ++i) ring[i] = VLD(rv, 0, i);
#pragma unroll
    for (int i = 0; i < 8; ++i) ext[i] = VLD(rv, 1, i);
    PIN();
    TSW(12, 2);
    softmax();
    TSW(12, 3);
    const Diag dg = make_diag(c);
    f32x4 acc = {0.f, 0.f, 0.f, 0.f};
    const int po = L_PS + sw * 64 + r * 16;
    u32x4 pv = *(LAS u32x4*)(lds + po), pn = *(LAS u32x4*)(lds + po + 256);
#define VSTEP(i, reg) acc = ((i) & 1) ? axpy4<1>(pv[(i) >> 1], reg, dg, acc) : axpy4<0>(pv[(i) >> 1], reg, dg, acc)
#pragma unroll
    for (int i = 0; i < 8; ++i) { VSTEP(i, ring[i]); PIN(); }
    pv = pn; pn = *(LAS u32x4*)(lds + po + 512);
#pragma unroll
    for (int i = 0; i < 8; ++i) {
      if (i + PF >= 8) { USE(ext[i]); ring[i + PF - 8] = VLD(rv, 2, i + PF - 8); }
      PIN();
      VSTEP(i, ext[i]);
      PIN();
    }
#pragma unroll 1
    for (int beta = 2; beta < 32; ++beta) {
      const rsrc_t rn = mk(vbase, beta < 31 ? 1u << 20 : 0u);
      pv = pn; pn = *(LAS u32x4*)(lds + po + (beta < 31 ? beta + 1 : 31) * 256);
      const bool shallow = !((deep >> beta) & 1);
#pragma unroll
      for (int i = 0; i < 8; ++i) {
        if (shallow) USE(ring[i]);
        if (i + PF < 8) ring[i + PF] = VLD(rv, beta, i + PF); else ring[i + PF - 8] = VLD(rn, beta < 31 ? beta + 1 : 0, i + PF - 8);
        PIN();
        VSTEP(i, ring[i]);
        PIN();
      }
    }
    if (r < 2) *(LAS f32x4*)(lds + L_OP + sw * 512 + c * 32 + r * 16) = acc;
    TSW(12, 4);
    BAR();
    return;
  }

  const rsrc_t rws = mk(a.ws, WS_END);
  u32 xr[5];
  if (w == 0) {
    const rsrc_t rx = mk(a.x + b * (DM * 2), DM * 2);
#pragma unroll
    for (int i = 0; i < 5; ++i) xr[i] = LD32(rx, lane * 4 + i * 256, 0, NT);
  }
  PIN();
  const int wr = ROT ? (w + b + (b >> 1)) % 12 : w;
  u32x4 wb[12];
  {
    const rsrc_t rq = mk(a.wq + h * 256, DM * 8192 - h * 256);
    const int vo = (48 * wr + 12 * r) * 8192 + c * 16;
#pragma unroll
    for (int i = 0; i < 12; ++i) wb[i] = (ABL & 32) ? u32x4{1u,1u,1u,1u} : LD128(rq, vo, i * 8192, WAUX);
  }
  PIN();
  u32x2 ep = {0u, 0u};
  if (w == 10) {
    ep = LD64(rws, WS_EPOCH, 0, AUX_HO);
    if (lane < 48 && !(ABL & 16)) {
#pragma unroll
      for (int i = 0; i < 3; ++i) ST128((u32x4{EMPTY, EMPTY, EMPTY, EMPTY}), rws, WS_ATT + blk * (DM * 4) + lane * 16 + i * 768, 0, SC1);
    }
  }
  PIN();
  TSW(0, 16); TSW(11, 17);
  const Diag dgq = make_diag(c);
  if (w == 0) {
    float s = 0.f;
#pragma unroll
    for (int i = 0; i < 5; ++i) s += lo16(xr[i]) + hi16(xr[i]);
    const float mean = sumrows(sum16(s)) * (1.f / DM);
    float ss = 0.f;
#pragma unroll
    for (int i = 0; i < 5; ++i) {
      const float d0 = lo16(xr[i]) - mean, d1 = hi16(xr[i]) - mean;
      ss += (i < 4 || lane < 32) ? d0 * d0 + d1 * d1 : 0.f;
    }
    const float var = sumrows(sum16(ss)) * (1.f / DM);
    const float m = rb(mean), den = rb(__builtin_amdgcn_sqrtf(rb(rb(var) + 1e-5f))), rd = __builtin_amdgcn_rcpf(den);
#pragma unroll
    for (int i = 0; i < 5; ++i)
      if (i < 4 || lane < 32) *(LAS u32*)(lds + L_XB + lane * 4 + i * 256) = pk(fdiv(rb(lo16(xr[i]) - m), den, rd), fdiv(rb(hi16(xr[i]) - m), den, rd));
  }
  TSW(0, 19);
  BAR();
  TSW(0, 21);
  {
    const int xo = L_XB + (48 * wr + 12 * r) * 2;
    const u32x2 x0 = *(LAS u32x2*)(lds + xo), x1 = *(LAS u32x2*)(lds + xo + 8), x2 = *(LAS u32x2*)(lds + xo + 16);
    const u32 xv[6] = {x0[0], x0[1], x1[0], x1[1], x2[0], x2[1]};
    f32x4 acc0 = {0.f, 0.f, 0.f, 0.f}, acc1 = acc0;
#pragma unroll
    for (int i = 0; i < 6; ++i) { acc0 = axpy4<0>(xv[i], wb[2 * i], dgq, acc0); acc1 = axpy4<1>(xv[i], wb[2 * i + 1], dgq, acc1); }
    if (r < 2) *(LAS f32x4*)(lds + L_QP + wr * 512 + c * 32 + r * 16) = acc0 + acc1;
  }
  TSW(0, 22); TSW(11, 23);
  BAR();
  TSW(0, 24);
  if (w < 2) {
    float s = 0.f;
#pragma unroll
    for (int j = 0; j < 12; ++j) s += *(LAS float*)(lds + L_QP + j * 512 + tid * 4);
    *(LAS u16*)(lds + L_QB + tid * 2) = (u16)pk(s, s);
  }
  u64 tag = 0;
  if (w == 10) {
    tag = (((u64)(u32)__builtin_amdgcn_readfirstlane(ep[1]) << 32) | (u32)__builtin_amdgcn_readfirstlane(ep[0])) + 1;
    if (lane == 0) *(LAS u64*)(lds + L_TG) = tag;
  }
  BAR();
  PIN();

  if (w < 9) {
    u32x4 wf[4][4];
    const rsrc_t ro = mk(a.wo + (size_t)h * (DH * DM * 2), DH * DM * 2);
    const int vo = r * 8 * (DM * 2) + w * 128 + c * 8;
    u32x2 raw[4][8];
#pragma unroll
    for (int n = 0; n < 32; ++n) {
      raw[n >> 3][n & 7] = (ABL & 8) ? u32x2{1u,1u} : LD64(ro, vo, ((n >> 3) * 32 + (n & 7)) * (DM * 2), WAUX);
      if (n >= WOD) USE(raw[(n - WOD) >> 3][(n - WOD) & 7]);
      if (n >= WOD + 7 && ((n - WOD) & 7) == 7) tr8(raw[(n - WOD) >> 3], wf[(n - WOD) >> 3]);
      PIN();
    }
    tr8(raw[3], wf[3]);
    TSW(0, 8);
    PIN();
    softmax();
    BAR();
    TSW(0, 10);
    float o0 = 0.f, o1 = 0.f;
#pragma unroll
    for (int j = 0; j < 4; ++j) { const u32x2 t = *(LAS u32x2*)(lds + L_OP + j * 512 + lane * 8); o0 += __uint_as_float(t[0]); o1 += __uint_as_float(t[1]); }
    *(LAS u32*)(lds + L_OB + w * 256 + lane * 4) = pk(o0, o1);
    LWAIT();
    TSW(0, 25);
    f32x4 d[4];
#pragma unroll
    for (int e = 0; e < 4; ++e) d[e] = f32x4{0.f, 0.f, 0.f, 0.f};
#pragma unroll
    for (int s = 0; s < 4; ++s) {
      const u32x4 ob = *(LAS u32x4*)(lds + L_OB + w * 256 + s * 64 + r * 16);
#pragma unroll
      for (int e = 0; e < 4; ++e) d[e] = mfma(wf[s][e], ob, d[e]);
    }
    u32x4 o;
#pragma unroll
    for (int e = 0; e < 4; ++e) {
      const float t = (c & 2) ? ((c & 1) ? d[e][3] : d[e][2]) : ((c & 1) ? d[e][1] : d[e][0]);
      o[e] = min(__float_as_uint(t), EMPTY - 1);
    }
    if (c < 4) ST128(o, rws, WS_ATT + blk * (DM * 4) + w * 256 + r * 64 + c * 16, 0, SC1);
    TSW(0, 11); TSW(8, 26);
    return;
  }
  if (w == 9) {
    u32x4 hf[2] = {};
    if (blk < NTASK && !(ABL & 2)) {
      {
        const rsrc_t rx = mk(a.x, NB * DM * 2);
        u32x4 xa[9];
#pragma unroll
        for (int i = 0; i < 9; ++i) xa[i] = LD128(rx, (lane >> 3) * (DM * 2) + (lane & 7) * 16, i * 128, NT);
        float s = 0.f;
#pragma unroll
        for (int i = 0; i < 9; ++i)
#pragma unroll
          for (int j = 0; j < 4; ++j) s += lo16(xa[i][j]) + hi16(xa[i][j]);
        s += dpp<X1>(s); s += dpp<X2>(s); s += dpp<HMIR>(s);
        const float mean = s * (1.f / DM);
        float ss = 0.f;
#pragma unroll
        for (int i = 0; i < 9; ++i)
#pragma unroll
          for (int j = 0; j < 4; ++j) { const float d0 = lo16(xa[i][j]) - mean, d1 = hi16(xa[i][j]) - mean; ss += d0 * d0 + d1 * d1; }
        ss += dpp<X1>(ss); ss += dpp<X2>(ss); ss += dpp<HMIR>(ss);
        const float m = rb(mean), den = rb(__builtin_amdgcn_sqrtf(rb(rb(ss * (1.f / DM)) + 1e-5f))), rd = __builtin_amdgcn_rcpf(den);
#pragma unroll
        for (int i = 0; i < 9; ++i) {
          u32x4 o;
#pragma unroll
          for (int j = 0; j < 4; ++j) o[j] = pk(fdiv(rb(lo16(xa[i][j]) - m), den, rd), fdiv(rb(hi16(xa[i][j]) - m), den, rd));
          *(LAS u32x4*)(lds + L_XN + (lane >> 3) * XN_LD + (lane & 7) * 16 + i * 128) = o;
        }
        LWAIT();
        PIN();
      }
      const rsrc_t r1 = mk(a.w1 + blk * 128, DM * FF * 2 - blk * 128), r3 = mk(a.w3 + blk * 128, DM * FF * 2 - blk * 128);
      const int vo = r * 8 * (FF * 2) + c * 8;
      const int xo = L_XN + (c & 7) * XN_LD + r * 16;
      u32x2 ra[8], rc[8];
      f32x4 g[4], u[4];
#pragma unroll
      for (int e = 0; e < 4; ++e) { g[e] = f32x4{0.f, 0.f, 0.f, 0.f}; u[e] = g[e]; }
#pragma unroll
      for (int i = 0; i < 8; ++i) ra[i] = LD64(r1, vo, i * (FF * 2), WAUX);
#pragma unroll 1
      for (int s = 0; s < 18; ++s) {
        const int sc_ = s * 32 * (FF * 2), sn = (s < 17 ? s + 1 : 17) * 32 * (FF * 2);
        const rsrc_t n1 = mk(a.w1 + blk * 128, s < 17 ? DM * FF * 2 - blk * 128 : 0);
        const u32x4 xb = *(LAS u32x4*)(lds + xo + s * 64);
        u32x4 f[4];
#pragma unroll
        for (int i = 0; i < 8; ++i) { USE(ra[i]); rc[i] = LD64(r3, vo, sc_ + i * (FF * 2), WAUX); PIN(); }
        tr8(ra, f);
#pragma unroll
        for (int e = 0; e < 4; ++e) g[e] = mfma(f[e], xb, g[e]);
        PIN();
#pragma unroll
        for (int i = 0; i < 8; ++i) { USE(rc[i]); ra[i] = LD64(n1, vo, sn + i * (FF * 2), WAUX); PIN(); }
        tr8(rc, f);
#pragma unroll
        for (int e = 0; e < 4; ++e) u[e] = mfma(f[e], xb, u[e]);
        PIN();
      }
      float hv[4][4];
#pragma unroll
      for (int p = 0; p < 4; ++p)
#pragma unroll
        for (int e = 0; e < 4; ++e) hv[p][e] = rb(silu(rb(g[e][p]))) * rb(u[e][p]);
#pragma unroll
      for (int s = 0; s < 2; ++s) hf[s] = u32x4{pk(hv[2 * s][0], hv[2 * s][1]), pk(hv[2 * s][2], hv[2 * s][3]), pk(hv[2 * s + 1][0], hv[2 * s + 1][1]), pk(hv[2 * s + 1][2], hv[2 * s + 1][3])};
      TS(9);
    }
    PIN();
    softmax();
    if (blk < NTASK && !(ABL & 2)) {
      const rsrc_t r2 = mk(a.w2 + (size_t)blk * (64 * DM * 2), 64 * DM * 2);
      const int vo = r * 16 * (DM * 2) + c * 8;
      u32x2 ra[8], rc[8];
#pragma unroll
      for (int i = 0; i < 8; ++i) ra[i] = LD64(r2, vo, i * (DM * 2), WAUX);
      const int so = L_MO + (c & 7) * (DM * 4) + r * 64 + (c >> 3) * 32;
#pragma unroll 1
      for (int W = 0; W < 9; ++W) {
        const rsrc_t n2 = mk(a.w2 + (size_t)blk * (64 * DM * 2), W < 8 ? 64 * DM * 2 : 0);
        const int sn = (W < 8 ? W + 1 : 8) * 128;
        u32x4 f[4];
        f32x4 d[4];
#pragma unroll
        for (int i = 0; i < 8; ++i) { USE(ra[i]); rc[i] = LD64(r2, vo, W * 128 + (8 + i) * (DM * 2), WAUX); PIN(); }
        tr8(ra, f);
#pragma unroll
        for (int e = 0; e < 4; ++e) d[e] = mfma(f[e], hf[0], f32x4{0.f, 0.f, 0.f, 0.f});
        PIN();
#pragma unroll
        for (int i = 0; i < 8; ++i) { USE(rc[i]); ra[i] = LD64(n2, vo, sn + i * (DM * 2), WAUX); PIN(); }
        tr8(rc, f);
#pragma unroll
        for (int e = 0; e < 4; ++e) d[e] = mfma(f[e], hf[1], d[e]);
#pragma unroll
        for (int p = 0; p < 2; ++p) {
          u32x4 o;
#pragma unroll
          for (int e = 0; e < 4; ++e) o[e] = __float_as_uint((c & 8) ? d[e][2 + p] : d[e][p]);
          *(LAS u32x4*)(lds + so + p * 16 + W * 256) = o;
        }
        PIN();
      }
      LWAIT();
      TS(15);
      if (ALIGN) { const u64 t = now() | (ALIGN - 1); while (now() <= t) __builtin_amdgcn_s_sleep(2); }
      if (!(ABL & 4)) {
#pragma unroll
        for (int i = 0; i < 18; ++i) ST128(*(LAS u32x4*)(lds + L_MO + lane * 16 + i * 1024), rws, WS_MLP + blk * (NB * DM * 4) + lane * 16, i * 1024, SC1);
      }
      DRAIN();
      const u32x2 tg = *(LAS u32x2*)(lds + L_TG);
      if (lane == 0) ST64(tg, rws, WS_F2 + blk * 8, 0, SC1);
      TS(12);
    }
    BAR();
    return;
  }
  if (w == 10) {
    DRAIN();
    if (lane == 0) ST64((u32x2{(u32)tag, (u32)(tag >> 32)}), rws, WS_F1 + blk * 8, 0, SC1);
  }
  PIN();
  softmax();
  if (w == 10 && h < NCONS) {
    bool ok = true;
    const u32 t0 = (u32)tag, t1 = (u32)(tag >> 32);
    const int col = h * 32 + (lane & 7) * 4;
    const u32x2 xv = LD64(mk(a.x, NB * DM * 2), (b * DM + col) * 2, 0, NT);
    USE(xv);
    BAR();
    const u64 start = now();
    if (ABL & 1) while (now() - start < 2600) __builtin_amdgcn_s_sleep(32);
    for (;;) {
      if (ABL & 22) break;
      bool all = true;
#pragma unroll
      for (int i = 0; i < 2; ++i) { const u32x4 f = LD128(rws, WS_F1 + lane * 16 + i * 1024, 0, AUX_HO); all &= f[0] == t0 && f[1] == t1 && f[2] == t0 && f[3] == t1; }
      { const u32x4 f = LD128(rws, WS_F2 + (lane < 36 ? lane : 0) * 16, 0, AUX_HO); all &= f[0] == t0 && f[1] == t1 && f[2] == t0 && f[3] == t1; }
      if (__all(all)) break;
      if (now() - start > SPIN_CAP) { ok = false; break; }
    }
    asm volatile("" ::: "memory");
    if (blk == 0 && lane == 0) ST64((u32x2{t0, t1}), rws, WS_EPOCH, 0, SC1);
    float f[4] = {0.f, 0.f, 0.f, 0.f};
    {
      u32x4 t[9];
#pragma unroll
      for (int i = 0; i < 9; ++i) t[i] = (ABL & 6) ? u32x4{0u,0u,0u,0u} : LD128(rws, WS_MLP + ((lane >> 3) * NB + b) * (DM * 4) + col * 4, i * 8 * NB * DM * 4, AUX_HO);
#pragma unroll
      for (int i = 0; i < 9; ++i)
#pragma unroll
        for (int e = 0; e < 4; ++e) f[e] += __uint_as_float(t[i][e]);
    }
    float g[4];
#pragma unroll
    for (int e = 0; e < 4; ++e) { float t = f[e]; t += dpp<ROR8>(t); g[e] = rb(silu(rb(sumrows(t)))); }
    TS(13);
    u32x4 t[4];
    const u64 start2 = now();
    while (ok) {
      bool all = true;
#pragma unroll
      for (int i = 0; i < 4; ++i) t[i] = LD128(rws, WS_ATT + (b * NH + (lane >> 3)) * (DM * 4) + col * 4, i * 8 * DM * 4, AUX_HO);
#pragma unroll
      for (int i = 0; i < 4; ++i) all &= t[i][0] != EMPTY && t[i][1] != EMPTY && t[i][2] != EMPTY && t[i][3] != EMPTY;
      if (__all(all)) break;
      if (now() - start2 > SPIN_CAP) ok = false;
      asm volatile("" ::: "memory");
    }
    float y[4];
#pragma unroll
    for (int e = 0; e < 4; ++e) {
      float s = (__uint_as_float(t[0][e]) + __uint_as_float(t[1][e])) + (__uint_as_float(t[2][e]) + __uint_as_float(t[3][e]));
      s += dpp<ROR8>(s);
      s = rb(sumrows(s));
      const float xe = (e & 1) ? hi16(xv[e >> 1]) : lo16(xv[e >> 1]);
      y[e] = ok ? xe + rb(s * g[e]) : __uint_as_float(0x7fc00000u);
    }
    if (lane < 8) ST64((u32x2{pk(y[0], y[1]), pk(y[2], y[3])}), mk(a.out, NB * DM * 2), (b * DM + col) * 2, 0, 0);
    TS(14);
  } else {
    BAR();
  }
}

}

extern "C" void kernel_launch(void* const* d_in, const int* in_sizes, int n_in, void* d_out, int out_size, void* d_ws, size_t ws_size,
                              hipStream_t stream) {
  Args a;
  a.x = (const char*)d_in[0]; a.k = (const char*)d_in[1]; a.v = (const char*)d_in[2]; a.wq = (const char*)d_in[3]; a.wo = (const char*)d_in[4];
  a.w1 = (const char*)d_in[6]; a.w3 = (const char*)d_in[7]; a.w2 = (const char*)d_in[8];
  a.out = (char*)d_out; a.ws = (char*)d_ws;
  hipLaunchKernelGGL(fused_decode, dim3(NB * NH), dim3(1024), 0, stream, a);
}
```

```cpp
#include <hip/hip_runtime.h>
#include <hip/hip_bf16.h>
#include <cstdint>
#include <cstdio>
#include <vector>
#include <string>

#define PF 5
#define DEEP_MLP 0x11111111u
#define DEEP_ALL 0u
#define INIT 8
#define VEXT 8
#define ALIGN 512
#define ROT 1
#define WOD 1
#define ABL 0
#define KAUX 2
#define VAUX 2
#define WAUX 2
#define AUX_HO 16

namespace {

typedef unsigned u32;
typedef unsigned long long u64;
typedef unsigned short u16;
typedef u32 u32x4 __attribute__((ext_vector_type(4)));
typedef u32 u32x2 __attribute__((ext_vector_type(2)));
typedef float f32x4 __attribute__((ext_vector_type(4)));
typedef __bf16 bf16x8 __attribute__((ext_vector_type(8)));
typedef __bf16 bf16x2 __attribute__((ext_vector_type(2)));
#define GAS __attribute__((address_space(1)))
#define LAS __attribute__((address_space(3)))

constexpr int NB = 8, DM = 576, NH = 32, SK = 4096, DH = 128, FF = 4608;
constexpr int NTASK = FF / 64;
constexpr int NCONS = DM / 32;
constexpr int NT = 2, SC1 = 16;

constexpr u32 WS_EPOCH = 0, WS_F1 = 256, WS_F2 = WS_F1 + 256 * 8, WS_ATT = 4096;
constexpr u32 WS_MLP = WS_ATT + NB * NH * DM * 4, WS_END = WS_MLP + NTASK * NB * DM * 4;
constexpr u32 WS_DBG = 4u << 20;
constexpr u32 EMPTY = 0xffffffffu;

constexpr int XN_LD = 1168;
constexpr int L_XN = 0, L_QP = L_XN + NB * XN_LD, L_QB = L_QP + 12 * DH * 4, L_PS = L_QB + DH * 2, L_WM = L_PS + SK * 2, L_WS = L_WM + 64,
              L_OP = L_WS + 64, L_OB = L_OP + 4 * DH * 4, L_TG = L_OB + 9 * DH * 2, L_XB = L_TG + 16, L_MO = L_XB + DM * 2, L_END = L_MO + NB * DM * 4;

struct Args { const char* x; const char* k; const char* v; const char* wq; const char* wo; const char* w1; const char* w3; const char* w2; char* out; char* ws; };

typedef __amdgpu_buffer_rsrc_t rsrc_t;
__device__ __forceinline__ rsrc_t mk(const char* p, u32 n) { return __builtin_amdgcn_make_buffer_rsrc((void*)p, 0, n, 0x00020000); }
#define LD128(r, vo, so, aux) __builtin_amdgcn_raw_buffer_load_b128(r, vo, so, aux)
#define LD64(r, vo, so, aux) __builtin_amdgcn_raw_buffer_load_b64(r, vo, so, aux)
#define LD32(r, vo, so, aux) __builtin_amdgcn_raw_buffer_load_b32(r, vo, so, aux)
#define ST128(v, r, vo, so, aux) __builtin_amdgcn_raw_buffer_store_b128(v, r, vo, so, aux)
#define ST64(v, r, vo, so, aux) __builtin_amdgcn_raw_buffer_store_b64(v, r, vo, so, aux)
#define PIN() __builtin_amdgcn_sched_barrier(0)
#define USE(x) asm volatile("" ::"v"(x) : "memory")
#define BAR() asm volatile("s_waitcnt lgkmcnt(0)\n\ts_barrier" ::: "memory")
#define LWAIT() asm volatile("s_waitcnt lgkmcnt(0)" ::: "memory")
#define DRAIN() asm volatile("s_waitcnt vmcnt(0)" ::: "memory")

__device__ __forceinline__ u32 pk(float lo, float hi) { bf16x2 t = {(__bf16)lo, (__bf16)hi}; return __builtin_bit_cast(u32, t); }
__device__ __forceinline__ float rb(float x) { return __uint_as_float(pk(x, x) << 16); }
__device__ __forceinline__ float lo16(u32 v) { return __uint_as_float(v << 16); }
__device__ __forceinline__ float hi16(u32 v) { return __uint_as_float(v & 0xffff0000u); }
__device__ __forceinline__ float dot2(u32 a, u32 b, float c) {
  return __builtin_amdgcn_fdot2_f32_bf16(__builtin_bit_cast(bf16x2, a), __builtin_bit_cast(bf16x2, b), c, false);
}
__device__ __forceinline__ f32x4 mfma(u32x4 a, u32x4 b, f32x4 c) {
  return __builtin_amdgcn_mfma_f32_16x16x32_bf16(__builtin_bit_cast(bf16x8, a), __builtin_bit_cast(bf16x8, b), c, 0, 0, 0);
}
template <int CTRL> __device__ __forceinline__ float dpp(float x) {
  return __builtin_bit_cast(float, __builtin_amdgcn_mov_dpp(__builtin_bit_cast(int, x), CTRL, 0xf, 0xf, true));
}
constexpr int X1 = 0xB1, X2 = 0x4E, HMIR = 0x141, MIR = 0x140, ROR8 = 0x128;
__device__ __forceinline__ float sum16(float x) { x += dpp<X1>(x); x += dpp<X2>(x); x += dpp<HMIR>(x); x += dpp<MIR>(x); return x; }
__device__ __forceinline__ float max16(float x) { x = fmaxf(x, dpp<X1>(x)); x = fmaxf(x, dpp<X2>(x)); x = fmaxf(x, dpp<HMIR>(x)); x = fmaxf(x, dpp<MIR>(x)); return x; }
__device__ __forceinline__ float sumrows(float x) {
  auto s = __builtin_amdgcn_permlane16_swap(__float_as_uint(x), __float_as_uint(x), false, false);
  x = __uint_as_float(s[0]) + __uint_as_float(s[1]);
  auto t = __builtin_amdgcn_permlane32_swap(__float_as_uint(x), __float_as_uint(x), false, false);
  return __uint_as_float(t[0]) + __uint_as_float(t[1]);
}
__device__ __forceinline__ float maxrows(float x) {
  auto s = __builtin_amdgcn_permlane16_swap(__float_as_uint(x), __float_as_uint(x), false, false);
  x = fmaxf(__uint_as_float(s[0]), __uint_as_float(s[1]));
  auto t = __builtin_amdgcn_permlane32_swap(__float_as_uint(x), __float_as_uint(x), false, false);
  return fmaxf(__uint_as_float(t[0]), __uint_as_float(t[1]));
}
__device__ __forceinline__ float fdiv(float a, float d, float rd) { const float q = a * rd; return fmaf(fmaf(-q, d, a), rd, q); }
__device__ __forceinline__ float silu(float x) { const float d = 1.f + __expf(-x); return d < INFINITY ? fdiv(x, d, __builtin_amdgcn_rcpf(d)) : -0.f; }
__device__ __forceinline__ u64 now() { return __builtin_amdgcn_s_memrealtime(); }
constexpr u64 SPIN_CAP = 5000000;

#define TS(i) do {} while (0)
#define TSW(ww, i) do {} while (0)

struct Diag { u32 s[2][4]; };
__device__ __forceinline__ Diag make_diag(int c) {
  Diag d;
#pragma unroll
  for (int par = 0; par < 2; ++par)
#pragma unroll
    for (int w = 0; w < 4; ++w) {
      const u32 b = par ? 0x0302u : 0x0100u;
      d.s[par][w] = (c >> 1) == w ? ((c & 1) ? (b << 16) | 0x0c0cu : 0x0c0c0000u | b) : 0x0c0c0c0cu;
    }
  return d;
}
template <int PAR> __device__ __forceinline__ f32x4 axpy4(u32 val, u32x4 rows, const Diag& d, f32x4 acc) {
  if (ABL & 128) { USE(rows); return acc; }
  u32x4 af;
#pragma unroll
  for (int w = 0; w < 4; ++w) af[w] = __builtin_amdgcn_perm(val, val, d.s[PAR][w]);
  return mfma(af, rows, acc);
}
__device__ __forceinline__ float score4(u32x4 k, u32x4 q) {
  if (ABL & 128) { USE(k); return 1.f; }
  float d = dot2(k[0], q[0], 0.f); d = dot2(k[1], q[1], d); d = dot2(k[2], q[2], d); d = dot2(k[3], q[3], d);
  return sum16(d);
}
__device__ __forceinline__ void tr8(const u32x2 (&raw)[8], u32x4 (&f)[4]) {
#pragma unroll
  for (int e = 0; e < 4; ++e)
#pragma unroll
    for (int d = 0; d < 4; ++d) f[e][d] = __builtin_amdgcn_perm(raw[2 * d + 1][e >> 1], raw[2 * d][e >> 1], (e & 1) ? 0x07060302u : 0x05040100u);
}

__global__ void __launch_bounds__(1024) fused_decode(Args a) {
  __shared__ __attribute__((aligned(16))) char lds_[L_END];
  LAS char* lds = (LAS char*)lds_;
  const int tid = threadIdx.x, lane = tid & 63, w = __builtin_amdgcn_readfirstlane(tid >> 6), r = lane >> 4, c = lane & 15;
  const int blk = blockIdx.x, b = blk >> 5, h = blk & 31;
  const char* kbase = a.k + ((size_t)blk << 20);
  const char* vbase = a.v + ((size_t)blk << 20);
  const rsrc_t rk = mk(kbase, 1u << 20);
  const int sw = w & 3;
  const int vA = lane * 16 + sw * 4096, vB = vA + 16384;
#define KVLD_(rs, beta, i, AUX) LD128(rs, ((i) < 4 ? vA : vB) + ((i) & 3) * 1024, (beta) * 32768, AUX)
#define KVLD(rs, beta, i) KVLD_(rs, beta, i, KAUX)
#define VLD(rs, beta, i) KVLD_(rs, beta, i, VAUX)
  float mx = -INFINITY;
  auto put_scores = [&](float sc, int batch) {
    const float s = rb(rb(sc) * 0.08838834764831845f);
    mx = fmaxf(mx, s);
    if (c < 8) *(LAS u16*)(lds + L_PS + batch * 64 + r * 16 + c * 2) = (u16)(__float_as_uint(s) >> 16);
  };
  auto softmax = [&]() {
    const float wm = maxrows(max16(mx));
    if (lane == 0) *(LAS float*)(lds + L_WM + w * 4) = wm;
    BAR();
    const float M = max16(*(LAS float*)(lds + L_WM + c * 4));
    const u32x2 sv = *(LAS u32x2*)(lds + L_PS + tid * 8);
    const float e0 = __expf(lo16(sv[0]) - M), e1 = __expf(hi16(sv[0]) - M), e2 = __expf(lo16(sv[1]) - M), e3 = __expf(hi16(sv[1]) - M);
    const float ws = sumrows(sum16((e0 + e1) + (e2 + e3)));
    if (lane == 0) *(LAS float*)(lds + L_WS + w * 4) = ws;
    BAR();
    const float inv = 1.f / sum16(*(LAS float*)(lds + L_WS + c * 4));
    *(LAS u32x2*)(lds + L_PS + tid * 8) = u32x2{pk(e0 * inv, e1 * inv), pk(e2 * inv, e3 * inv)};
    BAR();
  };

  if (w >= 12) {
    TSW(12, 0);
    const u32 deep = (blk < NTASK ? DEEP_MLP : 0u) | DEEP_ALL;
    u32x4 ring[8], ext[8];
    BAR();
#pragma unroll
    for (int i = 0; i < (INIT < 8 ? INIT : 8); ++i) { ring[i] = KVLD(rk, 0, i); PIN(); }
    if (INIT > 8) {
#pragma unroll
      for (int i = 0; i < 8; ++i) { ext[i] = KVLD(rk, 1, i); PIN(); }
    }
    __builtin_amdgcn_s_setprio(2);
    BAR(); BAR();
    TSW(12, 1);
    const u32x4 q = *(LAS u32x4*)(lds + L_QB + c * 16);
    float sc = 0.f;
    if (INIT > 8) {
#pragma unroll
      for (int i = 0; i < 8; ++i) { const float d = score4(ring[i], q); sc = (c & 7) == i ? d : sc; PIN(); }
      put_scores(sc, sw);
#pragma unroll
      for (int i = 0; i < 8; ++i) {
        if (i + PF >= 8) { USE(ext[i]); ring[i + PF - 8] = KVLD(rk, 2, i + PF - 8); }
        PIN();
        const float d = score4(ext[i], q); sc = (c & 7) == i ? d : sc;
        PIN();
      }
      put_scores(sc, 4 + sw);
    } else {
#pragma unroll
      for (int i = 0; i < 8; ++i) {
        if (i + PF >= INIT) { USE(ring[i]); if (i + PF < 8) ring[i + PF] = KVLD(rk, 0, i + PF); else ring[i + PF - 8] = KVLD(rk, 1, i + PF - 8); }
        PIN();
        const float d = score4(ring[i], q); sc = (c & 7) == i ? d : sc;
        PIN();
      }
      put_scores(sc, sw);
    }
#pragma unroll 1
    for (int beta = INIT > 8 ? 2 : 1; beta < 32; ++beta) {
      const rsrc_t rn = mk(beta < 31 ? kbase : vbase, 1u << 20);
      const int bn = beta < 31 ? beta + 1 : 0;
      const bool shallow = !((deep >> beta) & 1);
#pragma unroll
      for (int i = 0; i < 8; ++i) {
        if (shallow) USE(ring[i]);
        if (i + PF < 8) ring[i + PF] = KVLD(rk, beta, i + PF); else ring[i + PF - 8] = KVLD(rn, bn, i + PF - 8);
        PIN();
        const float d = score4(ring[i], q); sc = (c & 7) == i ? d : sc;
        PIN();
      }
      put_scores(sc, beta * 4 + sw);
    }
    const rsrc_t rv = mk(vbase, 1u << 20);
#pragma unroll
    for (int i = PF; i < 8; ++i) ring[i] = VLD(rv, 0, i);
#pragma unroll
    for (int i = 0; i < 8; ++i) ext[i] = VLD(rv, 1, i);
    PIN();
    TSW(12, 2);
    softmax();
    TSW(12, 3);
    const Diag dg = make_diag(c);
    f32x4 acc = {0.f, 0.f, 0.f, 0.f};
    const int po = L_PS + sw * 64 + r * 16;
    u32x4 pv = *(LAS u32x4*)(lds + po), pn = *(LAS u32x4*)(lds + po + 256);
#define VSTEP(i, reg) acc = ((i) & 1) ? axpy4<1>(pv[(i) >> 1], reg, dg, acc) : axpy4<0>(pv[(i) >> 1], reg, dg, acc)
#pragma unroll
    for (int i = 0; i < 8; ++i) { VSTEP(i, ring[i]); PIN(); }
    pv = pn; pn = *(LAS u32x4*)(lds + po + 512);
#pragma unroll
    for (int i = 0; i < 8; ++i) {
      if (i + PF >= 8) { USE(ext[i]); ring[i + PF - 8] = VLD(rv, 2, i + PF - 8); }
      PIN();
      VSTEP(i, ext[i]);
      PIN();
    }
#pragma unroll 1
    for (int beta = 2; beta < 32; ++beta) {
      const rsrc_t rn = mk(vbase, beta < 31 ? 1u << 20 : 0u);
      pv = pn; pn = *(LAS u32x4*)(lds + po + (beta < 31 ? beta + 1 : 31) * 256);
      const bool shallow = !((deep >> beta) & 1);
#pragma unroll
      for (int i = 0; i < 8; ++i) {
        if (shallow) USE(ring[i]);
        if (i + PF < 8) ring[i + PF] = VLD(rv, beta, i + PF); else ring[i + PF - 8] = VLD(rn, beta < 31 ? beta + 1 : 0, i + PF - 8);
        PIN();
        VSTEP(i, ring[i]);
        PIN();
      }
    }
    if (r < 2) *(LAS f32x4*)(lds + L_OP + sw * 512 + c * 32 + r * 16) = acc;
    TSW(12, 4);
    BAR();
    return;
  }

  const rsrc_t rws = mk(a.ws, WS_END);
  u32 xr[5];
  if (w == 0) {
    const rsrc_t rx = mk(a.x + b * (DM * 2), DM * 2);
#pragma unroll
    for (int i = 0; i < 5; ++i) xr[i] = LD32(rx, lane * 4 + i * 256, 0, NT);
  }
  PIN();
  const int wr = ROT ? (w + b + (b >> 1)) % 12 : w;
  u32x4 wb[12];
  {
    const rsrc_t rq = mk(a.wq + h * 256, DM * 8192 - h * 256);
    const int vo = (48 * wr + 12 * r) * 8192 + c * 16;
#pragma unroll
    for (int i = 0; i < 12; ++i) wb[i] = (ABL & 32) ? u32x4{1u,1u,1u,1u} : LD128(rq, vo, i * 8192, WAUX);
  }
  PIN();
  u32x2 ep = {0u, 0u};
  if (w == 10) {
    ep = LD64(rws, WS_EPOCH, 0, AUX_HO);
    if (lane < 48 && !(ABL & 16)) {
#pragma unroll
      for (int i = 0; i < 3; ++i) ST128((u32x4{EMPTY, EMPTY, EMPTY, EMPTY}), rws, WS_ATT + blk * (DM * 4) + lane * 16 + i * 768, 0, SC1);
    }
  }
  PIN();
  TSW(0, 16); TSW(11, 17);
  const Diag dgq = make_diag(c);
  if (w == 0) {
    float s = 0.f;
#pragma unroll
    for (int i = 0; i < 5; ++i) s += lo16(xr[i]) + hi16(xr[i]);
    const float mean = sumrows(sum16(s)) * (1.f / DM);
    float ss = 0.f;
#pragma unroll
    for (int i = 0; i < 5; ++i) {
      const float d0 = lo16(xr[i]) - mean, d1 = hi16(xr[i]) - mean;
      ss += (i < 4 || lane < 32) ? d0 * d0 + d1 * d1 : 0.f;
    }
    const float var = sumrows(sum16(ss)) * (1.f / DM);
    const float m = rb(mean), den = rb(__builtin_amdgcn_sqrtf(rb(rb(var) + 1e-5f))), rd = __builtin_amdgcn_rcpf(den);
#pragma unroll
    for (int i = 0; i < 5; ++i)
      if (i < 4 || lane < 32) *(LAS u32*)(lds + L_XB + lane * 4 + i * 256) = pk(fdiv(rb(lo16(xr[i]) - m), den, rd), fdiv(rb(hi16(xr[i]) - m), den, rd));
  }
  TSW(0, 19);
  BAR();
  TSW(0, 21);
  {
    const int xo = L_XB + (48 * wr + 12 * r) * 2;
    const u32x2 x0 = *(LAS u32x2*)(lds + xo), x1 = *(LAS u32x2*)(lds + xo + 8), x2 = *(LAS u32x2*)(lds + xo + 16);
    const u32 xv[6] = {x0[0], x0[1], x1[0], x1[1], x2[0], x2[1]};
    f32x4 acc0 = {0.f, 0.f, 0.f, 0.f}, acc1 = acc0;
#pragma unroll
    for (int i = 0; i < 6; ++i) { acc0 = axpy4<0>(xv[i], wb[2 * i], dgq, acc0); acc1 = axpy4<1>(xv[i], wb[2 * i + 1], dgq, acc1); }
    if (r < 2) *(LAS f32x4*)(lds + L_QP + wr * 512 + c * 32 + r * 16) = acc0 + acc1;
  }
  TSW(0, 22); TSW(11, 23);
  BAR();
  TSW(0, 24);
  if (w < 2) {
    float s = 0.f;
#pragma unroll
    for (int j = 0; j < 12; ++j) s += *(LAS float*)(lds + L_QP + j * 512 + tid * 4);
    *(LAS u16*)(lds + L_QB + tid * 2) = (u16)pk(s, s);
  }
  u64 tag = 0;
  if (w == 10) {
    tag = (((u64)(u32)__builtin_amdgcn_readfirstlane(ep[1]) << 32) | (u32)__builtin_amdgcn_readfirstlane(ep[0])) + 1;
    if (lane == 0) *(LAS u64*)(lds + L_TG) = tag;
  }
  BAR();
  PIN();

  if (w < 9) {
    u32x4 wf[4][4];
    const rsrc_t ro = mk(a.wo + (size_t)h * (DH * DM * 2), DH * DM * 2);
    const int vo = r * 8 * (DM * 2) + w * 128 + c * 8;
    u32x2 raw[4][8];
#pragma unroll
    for (int n = 0; n < 32; ++n) {
      raw[n >> 3][n & 7] = (ABL & 8) ? u32x2{1u,1u} : LD64(ro, vo, ((n >> 3) * 32 + (n & 7)) * (DM * 2), WAUX);
      if (n >= WOD) USE(raw[(n - WOD) >> 3][(n - WOD) & 7]);
      if (n >= WOD + 7 && ((n - WOD) & 7) == 7) tr8(raw[(n - WOD) >> 3], wf[(n - WOD) >> 3]);
      PIN();
    }
    tr8(raw[3], wf[3]);
    TSW(0, 8);
    PIN();
    softmax();
    BAR();
    TSW(0, 10);
    float o0 = 0.f, o1 = 0.f;
#pragma unroll
    for (int j = 0; j < 4; ++j) { const u32x2 t = *(LAS u32x2*)(lds + L_OP + j * 512 + lane * 8); o0 += __uint_as_float(t[0]); o1 += __uint_as_float(t[1]); }
    *(LAS u32*)(lds + L_OB + w * 256 + lane * 4) = pk(o0, o1);
    LWAIT();
    TSW(0, 25);
    f32x4 d[4];
#pragma unroll
    for (int e = 0; e < 4; ++e) d[e] = f32x4{0.f, 0.f, 0.f, 0.f};
#pragma unroll
    for (int s = 0; s < 4; ++s) {
      const u32x4 ob = *(LAS u32x4*)(lds + L_OB + w * 256 + s * 64 + r * 16);
#pragma unroll
      for (int e = 0; e < 4; ++e) d[e] = mfma(wf[s][e], ob, d[e]);
    }
    u32x4 o;
#pragma unroll
    for (int e = 0; e < 4; ++e) {
      const float t = (c & 2) ? ((c & 1) ? d[e][3] : d[e][2]) : ((c & 1) ? d[e][1] : d[e][0]);
      o[e] = min(__float_as_uint(t), EMPTY - 1);
    }
    if (c < 4) ST128(o, rws, WS_ATT + blk * (DM * 4) + w * 256 + r * 64 + c * 16, 0, SC1);
    TSW(0, 11); TSW(8, 26);
    return;
  }
  if (w == 9) {
    u32x4 hf[2] = {};
    if (blk < NTASK && !(ABL & 2)) {
      {
        const rsrc_t rx = mk(a.x, NB * DM * 2);
        u32x4 xa[9];
#pragma unroll
        for (int i = 0; i < 9; ++i) xa[i] = LD128(rx, (lane >> 3) * (DM * 2) + (lane & 7) * 16, i * 128, NT);
        float s = 0.f;
#pragma unroll
        for (int i = 0; i < 9; ++i)
#pragma unroll
          for (int j = 0; j < 4; ++j) s += lo16(xa[i][j]) + hi16(xa[i][j]);
        s += dpp<X1>(s); s += dpp<X2>(s); s += dpp<HMIR>(s);
        const float mean = s * (1.f / DM);
        float ss = 0.f;
#pragma unroll
        for (int i = 0; i < 9; ++i)
#pragma unroll
          for (int j = 0; j < 4; ++j) { const float d0 = lo16(xa[i][j]) - mean, d1 = hi16(xa[i][j]) - mean; ss += d0 * d0 + d1 * d1; }
        ss += dpp<X1>(ss); ss += dpp<X2>(ss); ss += dpp<HMIR>(ss);
        const float m = rb(mean), den = rb(__builtin_amdgcn_sqrtf(rb(rb(ss * (1.f / DM)) + 1e-5f))), rd = __builtin_amdgcn_rcpf(den);
#pragma unroll
        for (int i = 0; i < 9; ++i) {
          u32x4 o;
#pragma unroll
          for (int j = 0; j < 4; ++j) o[j] = pk(fdiv(rb(lo16(xa[i][j]) - m), den, rd), fdiv(rb(hi16(xa[i][j]) - m), den, rd));
          *(LAS u32x4*)(lds + L_XN + (lane >> 3) * XN_LD + (lane & 7) * 16 + i * 128) = o;
        }
        LWAIT();
        PIN();
      }
      const rsrc_t r1 = mk(a.w1 + blk * 128, DM * FF * 2 - blk * 128), r3 = mk(a.w3 + blk * 128, DM * FF * 2 - blk * 128);
      const int vo = r * 8 * (FF * 2) + c * 8;
      const int xo = L_XN + (c & 7) * XN_LD + r * 16;
      u32x2 ra[8], rc[8];
      f32x4 g[4], u[4];
#pragma unroll
      for (int e = 0; e < 4; ++e) { g[e] = f32x4{0.f, 0.f, 0.f, 0.f}; u[e] = g[e]; }
#pragma unroll
      for (int i = 0; i < 8; ++i) ra[i] = LD64(r1, vo, i * (FF * 2), WAUX);
#pragma unroll 1
      for (int s = 0; s < 18; ++s) {
        const int sc_ = s * 32 * (FF * 2), sn = (s < 17 ? s + 1 : 17) * 32 * (FF * 2);
        const rsrc_t n1 = mk(a.w1 + blk * 128, s < 17 ? DM * FF * 2 - blk * 128 : 0);
        const u32x4 xb = *(LAS u32x4*)(lds + xo + s * 64);
        u32x4 f[4];
#pragma unroll
        for (int i = 0; i < 8; ++i) { USE(ra[i]); rc[i] = LD64(r3, vo, sc_ + i * (FF * 2), WAUX); PIN(); }
        tr8(ra, f);
#pragma unroll
        for (int e = 0; e < 4; ++e) g[e] = mfma(f[e], xb, g[e]);
        PIN();
#pragma unroll
        for (int i = 0; i < 8; ++i) { USE(rc[i]); ra[i] = LD64(n1, vo, sn + i * (FF * 2), WAUX); PIN(); }
        tr8(rc, f);
#pragma unroll
        for (int e = 0; e < 4; ++e) u[e] = mfma(f[e], xb, u[e]);
        PIN();
      }
      float hv[4][4];
#pragma unroll
      for (int p = 0; p < 4; ++p)
#pragma unroll
        for (int e = 0; e < 4; ++e) hv[p][e] = rb(silu(rb(g[e][p]))) * rb(u[e][p]);
#pragma unroll
      for (int s = 0; s < 2; ++s) hf[s] = u32x4{pk(hv[2 * s][0], hv[2 * s][1]), pk(hv[2 * s][2], hv[2 * s][3]), pk(hv[2 * s + 1][0], hv[2 * s + 1][1]), pk(hv[2 * s + 1][2], hv[2 * s + 1][3])};
      TS(9);
    }
    PIN();
    softmax();
    if (blk < NTASK && !(ABL & 2)) {
      const rsrc_t r2 = mk(a.w2 + (size_t)blk * (64 * DM * 2), 64 * DM * 2);
      const int vo = r * 16 * (DM * 2) + c * 8;
      u32x2 ra[8], rc[8];
#pragma unroll
      for (int i = 0; i < 8; ++i) ra[i] = LD64(r2, vo, i * (DM * 2), WAUX);
      const int so = L_MO + (c & 7) * (DM * 4) + r * 64 + (c >> 3) * 32;
#pragma unroll 1
      for (int W = 0; W < 9; ++W) {
        const rsrc_t n2 = mk(a.w2 + (size_t)blk * (64 * DM * 2), W < 8 ? 64 * DM * 2 : 0);
        const int sn = (W < 8 ? W + 1 : 8) * 128;
        u32x4 f[4];
        f32x4 d[4];
#pragma unroll
        for (int i = 0; i < 8; ++i) { USE(ra[i]); rc[i] = LD64(r2, vo, W * 128 + (8 + i) * (DM * 2), WAUX); PIN(); }
        tr8(ra, f);
#pragma unroll
        for (int e = 0; e < 4; ++e) d[e] = mfma(f[e], hf[0], f32x4{0.f, 0.f, 0.f, 0.f});
        PIN();
#pragma unroll
        for (int i = 0; i < 8; ++i) { USE(rc[i]); ra[i] = LD64(n2, vo, sn + i * (DM * 2), WAUX); PIN(); }
        tr8(rc, f);
#pragma unroll
        for (int e = 0; e < 4; ++e) d[e] = mfma(f[e], hf[1], d[e]);
#pragma unroll
        for (int p = 0; p < 2; ++p) {
          u32x4 o;
#pragma unroll
          for (int e = 0; e < 4; ++e) o[e] = __float_as_uint((c & 8) ? d[e][2 + p] : d[e][p]);
          *(LAS u32x4*)(lds + so + p * 16 + W * 256) = o;
        }
        PIN();
      }
      LWAIT();
      TS(15);
      if (ALIGN) { const u64 t = now() | (ALIGN - 1); while (now() <= t) __builtin_amdgcn_s_sleep(2); }
      if (!(ABL & 4)) {
#pragma unroll
        for (int i = 0; i < 18; ++i) ST128(*(LAS u32x4*)(lds + L_MO + lane * 16 + i * 1024), rws, WS_MLP + blk * (NB * DM * 4) + lane * 16, i * 1024, SC1);
      }
      DRAIN();
      const u32x2 tg = *(LAS u32x2*)(lds + L_TG);
      if (lane == 0) ST64(tg, rws, WS_F2 + blk * 8, 0, SC1);
      TS(12);
    }
    BAR();
    return;
  }
  if (w == 10) {
    DRAIN();
    if (lane == 0) ST64((u32x2{(u32)tag, (u32)(tag >> 32)}), rws, WS_F1 + blk * 8, 0, SC1);
  }
  PIN();
  softmax();
  if (w == 10 && h < NCONS) {
    bool ok = true;
    const u32 t0 = (u32)tag, t1 = (u32)(tag >> 32);
    const int col = h * 32 + (lane & 7) * 4;
    const u32x2 xv = LD64(mk(a.x, NB * DM * 2), (b * DM + col) * 2, 0, NT);
    USE(xv);
    BAR();
    const u64 start = now();
    if (ABL & 1) while (now() - start < 2600) __builtin_amdgcn_s_sleep(32);
    for (;;) {
      if (ABL & 22) break;
      bool all = true;
#pragma unroll
      for (int i = 0; i < 2; ++i) { const u32x4 f = LD128(rws, WS_F1 + lane * 16 + i * 1024, 0, AUX_HO); all &= f[0] == t0 && f[1] == t1 && f[2] == t0 && f[3] == t1; }
      { const u32x4 f = LD128(rws, WS_F2 + (lane < 36 ? lane : 0) * 16, 0, AUX_HO); all &= f[0] == t0 && f[1] == t1 && f[2] == t0 && f[3] == t1; }
      if (__all(all)) break;
      if (now() - start > SPIN_CAP) { ok = false; break; }
    }
    asm volatile("" ::: "memory");
    if (blk == 0 && lane == 0) ST64((u32x2{t0, t1}), rws, WS_EPOCH, 0, SC1);
    float f[4] = {0.f, 0.f, 0.f, 0.f};
    {
      u32x4 t[9];
#pragma unroll
      for (int i = 0; i < 9; ++i) t[i] = (ABL & 6) ? u32x4{0u,0u,0u,0u} : LD128(rws, WS_MLP + ((lane >> 3) * NB + b) * (DM * 4) + col * 4, i * 8 * NB * DM * 4, AUX_HO);
#pragma unroll
      for (int i = 0; i < 9; ++i)
#pragma unroll
        for (int e = 0; e < 4; ++e) f[e] += __uint_as_float(t[i][e]);
    }
    float g[4];
#pragma unroll
    for (int e = 0; e < 4; ++e) { float t = f[e]; t += dpp<ROR8>(t); g[e] = rb(silu(rb(sumrows(t)))); }
    TS(13);
    u32x4 t[4];
    const u64 start2 = now();
    while (ok) {
      bool all = true;
#pragma unroll
      for (int i = 0; i < 4; ++i) t[i] = LD128(rws, WS_ATT + (b * NH + (lane >> 3)) * (DM * 4) + col * 4, i * 8 * DM * 4, AUX_HO);
#pragma unroll
      for (int i = 0; i < 4; ++i) all &= t[i][0] != EMPTY && t[i][1] != EMPTY && t[i][2] != EMPTY && t[i][3] != EMPTY;
      if (__all(all)) break;
      if (now() - start2 > SPIN_CAP) ok = false;
      asm volatile("" ::: "memory");
    }
    float y[4];
#pragma unroll
    for (int e = 0; e < 4; ++e) {
      float s = (__uint_as_float(t[0][e]) + __uint_as_float(t[1][e])) + (__uint_as_float(t[2][e]) + __uint_as_float(t[3][e]));
      s += dpp<ROR8>(s);
      s = rb(sumrows(s));
      const float xe = (e & 1) ? hi16(xv[e >> 1]) : lo16(xv[e >> 1]);
      y[e] = ok ? xe + rb(s * g[e]) : __uint_as_float(0x7fc00000u);
    }
    if (lane < 8) ST64((u32x2{pk(y[0], y[1]), pk(y[2], y[3])}), mk(a.out, NB * DM * 2), (b * DM + col) * 2, 0, 0);
    TS(14);
  } else {
    BAR();
  }
}

__global__ void scan(const char* base, long stride, int n, unsigned* out) {
  unsigned x = (unsigned)__builtin_amdgcn_s_getreg(20 | (3 << 11)) & 0xfu;
  if (x != 0 || blockIdx.x >= 8) return;
  for (int i = 0; i < n; ++i) {
    unsigned best = ~0u;
    for (int k = 0; k < 3; ++k) {
      const unsigned* p = (const unsigned*)(base + (size_t)i * stride + k * 512 + 128);
      unsigned long long t0, t1; unsigned v;
      asm volatile("s_memtime %0\n\ts_waitcnt lgkmcnt(0)\n\tglobal_load_dword %2, %3, off nt\n\ts_waitcnt vmcnt(0)\n\ts_memtime %1\n\ts_waitcnt lgkmcnt(0)" : "=&s"(t0), "=&s"(t1), "=&v"(v) : "v"(p) : "memory");
      unsigned d = (unsigned)(t1 - t0) + (v == 0x12345678u);
      if (k == 0) out[n + i] = d;
      best = min(best, d);
    }
    out[i] = best;
  }
}
}

extern "C" void kernel_launch(void* const* d_in, const int* in_sizes, int n_in, void* d_out, int out_size, void* d_ws, size_t ws_size,
                              hipStream_t stream) {
  hipStreamCaptureStatus cs; hipStreamIsCapturing(stream, &cs);
  for (int i = 0; i < n_in; ++i) { void* b = nullptr; size_t sz = 0; if (cs == hipStreamCaptureStatusNone) hipMemGetAddressRange(&b, &sz, d_in[i]); fprintf(stderr, "in%d %p base %p size %zu\n", i, d_in[i], b, sz); }
  { void* b = nullptr; size_t sz = 0; if (cs == hipStreamCaptureStatusNone) hipMemGetAddressRange(&b, &sz, d_ws); fprintf(stderr, "out %p ws %p base %p size %zu capturing %d\n", d_out, d_ws, b, sz, (int)cs); }
  if (cs == hipStreamCaptureStatusNone) {
    struct { int t; long stride; int n; } cfg[] = {{1, 4096, 64}, {1, 65536, 128}, {1, 1 << 20, 256}, {2, 4096, 64}, {2, 65536, 128}, {2, 1 << 20, 256}};
    for (auto c : cfg) {
      unsigned* o = (unsigned*)((char*)d_ws + (16u << 20));
      scan<<<8, 1, 0, stream>>>((const char*)d_in[c.t], c.stride, c.n, o);
      hipStreamSynchronize(stream);
      std::vector<unsigned> h(2 * c.n); hipMemcpy(h.data(), o, 8 * c.n, hipMemcpyDeviceToHost);
      std::string l; for (int i = 0; i < c.n; ++i) l += std::to_string(h[i] / 10) + " ";
      fprintf(stderr, "t%d stride %ld best/10: %s\n", c.t, c.stride, l.c_str());
      l.clear(); for (int i = 0; i < c.n; ++i) l += std::to_string(h[c.n + i] / 10) + " ";
      fprintf(stderr, "t%d stride %ld first/10: %s\n", c.t, c.stride, l.c_str());
    }
  }
  Args a;
  a.x = (const char*)d_in[0]; a.k = (const char*)d_in[1]; a.v = (const char*)d_in[2]; a.wq = (const char*)d_in[3]; a.wo = (const char*)d_in[4];
  a.w1 = (const char*)d_in[6]; a.w3 = (const char*)d_in[7]; a.w2 = (const char*)d_in[8];
  a.out = (char*)d_out; a.ws = (char*)d_ws;
  hipLaunchKernelGGL(fused_decode, dim3(NB * NH), dim3(1024), 0, stream, a);
}
```

```cpp
#include <hip/hip_runtime.h>
#include <hip/hip_bf16.h>
#include <cstdint>

#define WIN0 41
#define WIN_MLP 2
#define PACE 0
#define GAIN 0.04f
#define LAG0 0.f
#define WLO 34
#define WHI 52
#define ALIGN 512
#define WOD 1
#define PRE_AT 58
#define AUX_MS 18
#define AUX_CL 16
#define AUX_AS 16
#define HINT 0
#define DLY 3
#define CODE_KB 27
#define ABL 0
#define KAUX 2
#define VAUX 2
#define WAUX 2
#define AUX_HO 16
#define WARM 1
#define ROT 1

namespace {

typedef unsigned u32;
typedef unsigned long long u64;
typedef unsigned short u16;
typedef u32 u32x4 __attribute__((ext_vector_type(4)));
typedef u32 u32x2 __attribute__((ext_vector_type(2)));
typedef float f32x4 __attribute__((ext_vector_type(4)));
typedef __bf16 bf16x8 __attribute__((ext_vector_type(8)));
typedef __bf16 bf16x2 __attribute__((ext_vector_type(2)));
#define GAS __attribute__((address_space(1)))
#define LAS __attribute__((address_space(3)))

constexpr int NB = 8, DM = 576, NH = 32, SK = 4096, DH = 128, FF = 4608;
constexpr int NTASK = FF / 64;
constexpr int NCONS = DM / 32;
constexpr int NT = 2, SC1 = 16;

constexpr u32 WS_EPOCH = 0, WS_F1 = 256, WS_F2 = WS_F1 + 256 * 8, WS_PACE = 4096, WS_ATT = 8192;
constexpr u32 WS_MLP = WS_ATT + NB * NH * DM * 4, WS_END = WS_MLP + NTASK * NB * DM * 4;
constexpr u32 WS_DBG = 4u << 20;
constexpr u32 EMPTY = 0xffffffffu;

constexpr int XN_LD = 1168;
constexpr int L_XN = 0, L_QP = L_XN + NB * XN_LD, L_QB = L_QP + 12 * DH * 4, L_PS = L_QB + DH * 2, L_WM = L_PS + SK * 2, L_WS = L_WM + 64,
              L_OP = L_WS + 64, L_OB = L_OP + 4 * DH * 4, L_TG = L_OB + 9 * DH * 2, L_PG = L_TG + 8, L_WN = L_PG + 4, L_XB = L_TG + 16, L_MO = L_XB + DM * 2, L_END = L_MO + NB * DM * 4;

struct Args { const char* x; const char* k; const char* v; const char* wq; const char* wo; const char* w1; const char* w3; const char* w2; char* out; char* ws; };

typedef __amdgpu_buffer_rsrc_t rsrc_t;
__device__ __forceinline__ rsrc_t mk(const char* p, u32 n) { return __builtin_amdgcn_make_buffer_rsrc((void*)p, 0, n, 0x00020000); }
#define LD128(r, vo, so, aux) __builtin_amdgcn_raw_buffer_load_b128(r, vo, so, aux)
#define LD64(r, vo, so, aux) __builtin_amdgcn_raw_buffer_load_b64(r, vo, so, aux)
#define LD32(r, vo, so, aux) __builtin_amdgcn_raw_buffer_load_b32(r, vo, so, aux)
#define ST128(v, r, vo, so, aux) __builtin_amdgcn_raw_buffer_store_b128(v, r, vo, so, aux)
#define ST64(v, r, vo, so, aux) __builtin_amdgcn_raw_buffer_store_b64(v, r, vo, so, aux)
#define PIN() __builtin_amdgcn_sched_barrier(0)
#define USE(x) asm volatile("" ::"v"(x) : "memory")
#define BAR() asm volatile("s_waitcnt lgkmcnt(0)\n\ts_barrier" ::: "memory")
#define RAWBAR() asm volatile("s_barrier" ::: "memory")
#define TOUCH(d, p) asm volatile("s_load_dword %0, %1, 0x0" : "=&s"(d) : "s"(p) : "memory")
#define GATE(i) asm volatile("s_cmp_gt_i32 %0, %1\n\ts_cbranch_scc1 Lg7_%=\n\ts_cmp_gt_i32 %0, %2\n\ts_cbranch_scc1 Lg5_%=\n\t" \
                             "s_cmp_gt_i32 %0, %3\n\ts_cbranch_scc1 Lg4_%=\n\ts_waitcnt vmcnt(3)\nLg4_%=:\n\ts_waitcnt vmcnt(4)\n" \
                             "Lg5_%=:\n\ts_waitcnt vmcnt(5)\nLg7_%=:" \
                             ::"s"(win), "i"(48 + DITH(i)), "i"(40 + DITH(i)), "i"(32 + DITH(i)) : "scc", "memory")
#define DITH(i) (((i) & 1) * 4 + ((i) & 2) + ((i) >> 2))
#define LGET(off) __hip_atomic_load((LAS u32*)(lds + (off)), __ATOMIC_RELAXED, __HIP_MEMORY_SCOPE_WORKGROUP)
#define LPUT(off, v) __hip_atomic_store((LAS u32*)(lds + (off)), (u32)(v), __ATOMIC_RELAXED, __HIP_MEMORY_SCOPE_WORKGROUP)
#define LWAIT() asm volatile("s_waitcnt lgkmcnt(0)" ::: "memory")
#define DRAIN() asm volatile("s_waitcnt vmcnt(0)" ::: "memory")

__device__ __forceinline__ u32 pk(float lo, float hi) { bf16x2 t = {(__bf16)lo, (__bf16)hi}; return __builtin_bit_cast(u32, t); }
__device__ __forceinline__ float rb(float x) { return __uint_as_float(pk(x, x) << 16); }
__device__ __forceinline__ float lo16(u32 v) { return __uint_as_float(v << 16); }
__device__ __forceinline__ float hi16(u32 v) { return __uint_as_float(v & 0xffff0000u); }
__device__ __forceinline__ float dot2(u32 a, u32 b, float c) {
  return __builtin_amdgcn_fdot2_f32_bf16(__builtin_bit_cast(bf16x2, a), __builtin_bit_cast(bf16x2, b), c, false);
}
__device__ __forceinline__ f32x4 mfma(u32x4 a, u32x4 b, f32x4 c) {
  return __builtin_amdgcn_mfma_f32_16x16x32_bf16(__builtin_bit_cast(bf16x8, a), __builtin_bit_cast(bf16x8, b), c, 0, 0, 0);
}
template <int CTRL> __device__ __forceinline__ float dpp(float x) {
  return __builtin_bit_cast(float, __builtin_amdgcn_mov_dpp(__builtin_bit_cast(int, x), CTRL, 0xf, 0xf, true));
}
constexpr int X1 = 0xB1, X2 = 0x4E, HMIR = 0x141, MIR = 0x140, ROR8 = 0x128;
__device__ __forceinline__ float sum16(float x) { x += dpp<X1>(x); x += dpp<X2>(x); x += dpp<HMIR>(x); x += dpp<MIR>(x); return x; }
__device__ __forceinline__ float max16(float x) { x = fmaxf(x, dpp<X1>(x)); x = fmaxf(x, dpp<X2>(x)); x = fmaxf(x, dpp<HMIR>(x)); x = fmaxf(x, dpp<MIR>(x)); return x; }
__device__ __forceinline__ float sumrows(float x) {
  auto s = __builtin_amdgcn_permlane16_swap(__float_as_uint(x), __float_as_uint(x), false, false);
  x = __uint_as_float(s[0]) + __uint_as_float(s[1]);
  auto t = __builtin_amdgcn_permlane32_swap(__float_as_uint(x), __float_as_uint(x), false, false);
  return __uint_as_float(t[0]) + __uint_as_float(t[1]);
}
__device__ __forceinline__ float maxrows(float x) {
  auto s = __builtin_amdgcn_permlane16_swap(__float_as_uint(x), __float_as_uint(x), false, false);
  x = fmaxf(__uint_as_float(s[0]), __uint_as_float(s[1]));
  auto t = __builtin_amdgcn_permlane32_swap(__float_as_uint(x), __float_as_uint(x), false, false);
  return fmaxf(__uint_as_float(t[0]), __uint_as_float(t[1]));
}
__device__ __forceinline__ float fdiv(float a, float d, float rd) { const float q = a * rd; return fmaf(fmaf(-q, d, a), rd, q); }
__device__ __forceinline__ float silu(float x) { const float d = 1.f + __expf(-x); return d < INFINITY ? fdiv(x, d, __builtin_amdgcn_rcpf(d)) : -0.f; }
__device__ __forceinline__ u64 now() { return __builtin_amdgcn_s_memrealtime(); }
constexpr u64 SPIN_CAP = 5000000;

#define TS(i) do {} while (0)
#define TSW(ww, i) do {} while (0)

struct Diag { u32 s[2][4]; };
__device__ __forceinline__ Diag make_diag(int c) {
  Diag d;
#pragma unroll
  for (int par = 0; par < 2; ++par)
#pragma unroll
    for (int w = 0; w < 4; ++w) {
      const u32 b = par ? 0x0302u : 0x0100u;
      d.s[par][w] = (c >> 1) == w ? ((c & 1) ? (b << 16) | 0x0c0cu : 0x0c0c0000u | b) : 0x0c0c0c0cu;
    }
  return d;
}
template <int PAR> __device__ __forceinline__ f32x4 axpy4(u32 val, u32x4 rows, const Diag& d, f32x4 acc) {
  if (ABL & 128) { USE(rows); return acc; }
  u32x4 af;
#pragma unroll
  for (int w = 0; w < 4; ++w) af[w] = __builtin_amdgcn_perm(val, val, d.s[PAR][w]);
  return mfma(af, rows, acc);
}
__device__ __forceinline__ float score4(u32x4 k, u32x4 q) {
  if (ABL & 128) { USE(k); return 1.f; }
  float d = dot2(k[0], q[0], 0.f); d = dot2(k[1], q[1], d); d = dot2(k[2], q[2], d); d = dot2(k[3], q[3], d);
  return sum16(d);
}
__device__ __forceinline__ void tr8(const u32x2 (&raw)[8], u32x4 (&f)[4]) {
#pragma unroll
  for (int e = 0; e < 4; ++e)
#pragma unroll
    for (int d = 0; d < 4; ++d) f[e][d] = __builtin_amdgcn_perm(raw[2 * d + 1][e >> 1], raw[2 * d][e >> 1], (e & 1) ? 0x07060302u : 0x05040100u);
}

__global__ void __launch_bounds__(1024) fused_decode(Args a) {
  __shared__ __attribute__((aligned(16))) char lds_[L_END];
  LAS char* lds = (LAS char*)lds_;
  if (CODE_KB && __builtin_amdgcn_readfirstlane(threadIdx.x >> 6) == 11 && (blockIdx.x >> 3) < CODE_KB) {
    u64 p;
    u32 c0, c1, c2, c3, c4, c5, c6, c7;
    asm volatile("s_getpc_b64 %0" : "=s"(p));
    p = (p & ~255ull) + (blockIdx.x >> 3) * 1024;
    asm volatile("s_load_dword %0, %8, 0x0\n\ts_load_dword %1, %8, 0x80\n\ts_load_dword %2, %8, 0x100\n\ts_load_dword %3, %8, 0x180\n\t"
                 "s_load_dword %4, %8, 0x200\n\ts_load_dword %5, %8, 0x280\n\ts_load_dword %6, %8, 0x300\n\ts_load_dword %7, %8, 0x380\n\ts_waitcnt lgkmcnt(0)"
                 : "=&s"(c0), "=&s"(c1), "=&s"(c2), "=&s"(c3), "=&s"(c4), "=&s"(c5), "=&s"(c6), "=&s"(c7) : "s"(p) : "memory");
  }
  asm volatile("" ::"s"(a.x), "s"(a.ws));
  const int tid = threadIdx.x, lane = tid & 63, w = __builtin_amdgcn_readfirstlane(tid >> 6), r = lane >> 4, c = lane & 15;
  const int blk = blockIdx.x, b = blk >> 5, h = blk & 31;
  const char* kbase = a.k + ((size_t)blk << 20);
  const char* vbase = a.v + ((size_t)blk << 20);
  const rsrc_t rk = mk(kbase, 1u << 20);
  const int sw = w & 3;
  const int vA = lane * 16 + sw * 4096, vB = vA + 16384;
#define KVLD_(rs, beta, i, AUX) LD128(rs, ((i) < 4 ? vA : vB) + ((i) & 3) * 1024, (beta) * 32768, AUX)
#define KVLD(rs, beta, i) KVLD_(rs, beta, i, KAUX)
#define VLD(rs, beta, i) KVLD_(rs, beta, i, VAUX)
  float mx = -INFINITY;
  auto put_scores = [&](float sc, int batch) {
    const float s = rb(rb(sc) * 0.08838834764831845f);
    mx = fmaxf(mx, s);
    if (c < 8) *(LAS u16*)(lds + L_PS + batch * 64 + r * 16 + c * 2) = (u16)(__float_as_uint(s) >> 16);
  };
  auto softmax = [&]() {
    const float wm = maxrows(max16(mx));
    if (lane == 0) *(LAS float*)(lds + L_WM + w * 4) = wm;
    BAR();
    const float M = max16(*(LAS float*)(lds + L_WM + c * 4));
    const u32x2 sv = *(LAS u32x2*)(lds + L_PS + tid * 8);
    const float e0 = __expf(lo16(sv[0]) - M), e1 = __expf(hi16(sv[0]) - M), e2 = __expf(lo16(sv[1]) - M), e3 = __expf(hi16(sv[1]) - M);
    const float ws = sumrows(sum16((e0 + e1) + (e2 + e3)));
    if (lane == 0) *(LAS float*)(lds + L_WS + w * 4) = ws;
    BAR();
    const float inv = 1.f / sum16(*(LAS float*)(lds + L_WS + c * 4));
    *(LAS u32x2*)(lds + L_PS + tid * 8) = u32x2{pk(e0 * inv, e1 * inv), pk(e2 * inv, e3 * inv)};
    BAR();
  };

  if (w >= 12) {
    TSW(12, 0);
    u32x4 ring[8];
    RAWBAR();
#pragma unroll
    for (int i = 0; i < 7; ++i) { ring[i] = KVLD(rk, 0, i); PIN(); }
    __builtin_amdgcn_s_setprio(2);
    RAWBAR(); RAWBAR();
    TSW(12, 1);
    const u32x4 q = *(LAS u32x4*)(lds + L_QB + c * 16);
    float sc = 0.f;
    u32 wn = LGET(L_WN);
#pragma unroll 1
    for (int beta = 0; beta < 32; ++beta) {
      const rsrc_t rn = mk(beta < 31 ? kbase : vbase, 1u << 20);
      const int bn = beta < 31 ? beta + 1 : 0;
      const int win = beta < 31 ? __builtin_amdgcn_readfirstlane(wn) : 64;
      wn = LGET(L_WN);
      if (tid == 768) LPUT(L_PG, beta);
#define KSTEP(i) { GATE(i); if (i == 0) ring[7] = KVLD(rk, beta, 7); else ring[(i + 7) & 7] = KVLD(rn, bn, (i + 7) & 7); PIN(); \
        const float d = score4(ring[i], q); sc = (c & 7) == i ? d : sc; PIN(); }
      KSTEP(0) KSTEP(1) KSTEP(2) KSTEP(3) KSTEP(4) KSTEP(5) KSTEP(6) KSTEP(7)
      put_scores(sc, beta * 4 + sw);
    }
    const rsrc_t rv = mk(vbase, 1u << 20);
    u32x4 ext[8];
    ring[7] = VLD(rv, 0, 7);
#pragma unroll
    for (int i = 0; i < 8; ++i) { ext[i] = VLD(rv, 1, i); PIN(); }
    TSW(12, 2);
    softmax();
    TSW(12, 3);
    const Diag dg = make_diag(c);
    f32x4 acc = {0.f, 0.f, 0.f, 0.f};
    const int po = L_PS + sw * 64 + r * 16;
    u32x4 pn = *(LAS u32x4*)(lds + po + 512);
    {
      const u32x4 p0 = *(LAS u32x4*)(lds + po), p1 = *(LAS u32x4*)(lds + po + 256);
      const int win = __builtin_amdgcn_readfirstlane(wn);
#pragma unroll
      for (int i = 0; i < 8; ++i) { acc = (i & 1) ? axpy4<1>(p0[i >> 1], ring[i], dg, acc) : axpy4<0>(p0[i >> 1], ring[i], dg, acc); PIN(); }
#define ESTEP(i) { if (i > 0) { GATE(i); ring[(i + 7) & 7] = VLD(rv, 2, (i + 7) & 7); } PIN(); acc = axpy4<i & 1>(p1[i >> 1], ext[i], dg, acc); PIN(); }
      ESTEP(0) ESTEP(1) ESTEP(2) ESTEP(3) ESTEP(4) ESTEP(5) ESTEP(6) ESTEP(7)
    }
#pragma unroll 1
    for (int beta = 2; beta < 32; ++beta) {
      const rsrc_t rn = mk(vbase, beta < 31 ? 1u << 20 : 0u);
      const int bn = beta < 31 ? beta + 1 : 0;
      const int win = beta < 31 ? __builtin_amdgcn_readfirstlane(wn) : 64;
      const u32x4 pv = pn;
      pn = *(LAS u32x4*)(lds + po + bn * 256);
      wn = LGET(L_WN);
      if (tid == 768) LPUT(L_PG, 32 + beta);
#define VSTEP(i) { GATE(i); if (i == 0) ring[7] = VLD(rv, beta, 7); else ring[(i + 7) & 7] = VLD(rn, bn, (i + 7) & 7); PIN(); \
        acc = axpy4<i & 1>(pv[i >> 1], ring[i], dg, acc); PIN(); }
      VSTEP(0) VSTEP(1) VSTEP(2) VSTEP(3) VSTEP(4) VSTEP(5) VSTEP(6) VSTEP(7)
    }
    if (r < 2) *(LAS f32x4*)(lds + L_OP + sw * 512 + c * 32 + r * 16) = acc;
    TSW(12, 4);
    BAR();
    return;
  }

  const rsrc_t rws = mk(a.ws, WS_END);
  auto wait_batch = [&](u32 n) { while (LGET(L_PG) < n) __builtin_amdgcn_s_sleep(4); };
  if (w == 11) {
    u32 t0, t1, t2, t3, t4, t5, t6, t7;
    asm volatile("" ::"s"(a.ws), "s"(a.out), "s"(kbase), "s"(vbase), "s"(a.w2));
    TOUCH(t0, kbase); TOUCH(t1, a.ws);
    RAWBAR();
    TOUCH(t2, a.wo + (size_t)h * (DH * DM * 2)); TOUCH(t3, a.w1); TOUCH(t4, a.w3); TOUCH(t5, vbase); TOUCH(t6, a.w2); TOUCH(t7, a.out);
    const u32x2 g0 = LD64(rws, WS_PACE + b * 128 + (lane & 15) * 8, 0, AUX_HO);
    RAWBAR(); RAWBAR();
    asm volatile("s_waitcnt lgkmcnt(0)" : "+s"(t0), "+s"(t1), "+s"(t2), "+s"(t3), "+s"(t4), "+s"(t5), "+s"(t6), "+s"(t7)::"memory");
    TS(17);
    const int base = WIN0 + (blk < NTASK ? WIN_MLP : 0);
    GAS u64* const arr = (GAS u64*)(a.ws + WS_PACE + b * 128);
    auto pace = [&](int k, u32 at, float gain) {
      wait_batch(at);
      const u32 t = (u32)now();
      if (lane == 0) __hip_atomic_fetch_add(arr + k, (1ull << 40) + t, __ATOMIC_RELAXED, __HIP_MEMORY_SCOPE_AGENT);
      wait_batch(at + DLY);
      const u32x2 v = LD64(rws, WS_PACE + b * 128 + k * 8, 0, AUX_HO);
      const u64 sum = (((u64)v[1] << 32) | v[0]) - (((u64)(u32)__builtin_amdgcn_readlane(g0[1], k) << 32) | (u32)__builtin_amdgcn_readlane(g0[0], k));
      const u32 n = (u32)(sum >> 40);
      const float lag = n ? (float)(int)(n * t - (u32)sum) * __builtin_amdgcn_rcpf((float)n) : 0.f;
      const int wnew = min(max(base + (int)((lag - LAG0) * gain), WLO), WHI);
      if (lane == 0) LPUT(L_WN, wnew);
    };
    if (PACE) {
#pragma unroll 1
      for (int k = 0; k < 4; ++k) pace(k, 2 + 8 * k, GAIN);
    }
    softmax();
    if (PACE) {
#pragma unroll 1
      for (int k = 0; k < 4; ++k) pace(4 + k, (0x36302922u >> (8 * k)) & 255u, GAIN * (1.f + 0.33f * k));
    }
    BAR();
    if (HINT && lane == 0) __hip_atomic_fetch_add(arr + 8, 1ull, __ATOMIC_RELAXED, __HIP_MEMORY_SCOPE_AGENT);
    return;
  }
  u64 tag = 0;
  u32 g8 = 0;
  if (w < 9) {
    const int wr = ROT ? (w + b) % 9 : w;
    u32x4 wb[16];
    {
      const rsrc_t rq = mk(a.wq + h * 256, DM * 8192 - h * 256);
      const int vo = (64 * wr + 16 * r) * 8192 + c * 16;
#pragma unroll
      for (int i = 0; i < 16; ++i) wb[i] = (ABL & 32) ? u32x4{1u,1u,1u,1u} : LD128(rq, vo, i * 8192, WAUX);
    }
    PIN();
    TSW(0, 16);
    const Diag dgq = make_diag(c);
    RAWBAR();
    TSW(0, 21);
    {
      const int xo = L_XB + (64 * wr + 16 * r) * 2;
      const u32x4 x0 = *(LAS u32x4*)(lds + xo), x1 = *(LAS u32x4*)(lds + xo + 16);
      f32x4 acc0 = {0.f, 0.f, 0.f, 0.f}, acc1 = acc0;
#pragma unroll
      for (int i = 0; i < 8; ++i) {
        const u32 xv = i < 4 ? x0[i & 3] : x1[i & 3];
        acc0 = axpy4<0>(xv, wb[2 * i], dgq, acc0); acc1 = axpy4<1>(xv, wb[2 * i + 1], dgq, acc1);
      }
      if (r < 2) *(LAS f32x4*)(lds + L_QP + wr * 512 + c * 32 + r * 16) = acc0 + acc1;
    }
    TSW(0, 22); TSW(8, 23);
    BAR();
    TSW(0, 24);
    if (w < 2) {
      float s = 0.f;
#pragma unroll
      for (int j = 0; j < 9; ++j) s += *(LAS float*)(lds + L_QP + j * 512 + tid * 4);
      *(LAS u16*)(lds + L_QB + tid * 2) = (u16)pk(s, s);
    }
    BAR();
  } else {
    __builtin_amdgcn_s_setprio(3);
    if (w == 10) {
      const rsrc_t rx = mk(a.x + b * (DM * 2), DM * 2);
      u32 xr[5];
#pragma unroll
      for (int i = 0; i < 5; ++i) xr[i] = LD32(rx, lane * 4 + i * 256, 0, NT);
      if (lane == 0) { LPUT(L_PG, 0); LPUT(L_WN, WIN0 + (blk < NTASK ? WIN_MLP : 0)); }
      float s = 0.f;
#pragma unroll
      for (int i = 0; i < 5; ++i) s += lo16(xr[i]) + hi16(xr[i]);
      const float mean = sumrows(sum16(s)) * (1.f / DM);
      float ss = 0.f;
#pragma unroll
      for (int i = 0; i < 5; ++i) {
        const float d0 = lo16(xr[i]) - mean, d1 = hi16(xr[i]) - mean;
        ss += (i < 4 || lane < 32) ? d0 * d0 + d1 * d1 : 0.f;
      }
      const float var = sumrows(sum16(ss)) * (1.f / DM);
      const float m = rb(mean), den = rb(__builtin_amdgcn_sqrtf(rb(rb(var) + 1e-5f))), rd = __builtin_amdgcn_rcpf(den);
#pragma unroll
      for (int i = 0; i < 5; ++i)
        if (i < 4 || lane < 32) *(LAS u32*)(lds + L_XB + lane * 4 + i * 256) = pk(fdiv(rb(lo16(xr[i]) - m), den, rd), fdiv(rb(hi16(xr[i]) - m), den, rd));
    }
    TSW(10, 19);
    BAR();
    __builtin_amdgcn_s_setprio(0);
    u32x2 ep = {0u, 0u};
    if (w == 10) {
      ep = LD64(rws, WS_EPOCH, 0, AUX_HO);
      g8 = LD32(rws, WS_PACE + b * 128 + 64, 0, AUX_HO);
      if (lane < 48 && !(ABL & 16)) {
#pragma unroll
        for (int i = 0; i < 3; ++i) ST128((u32x4{EMPTY, EMPTY, EMPTY, EMPTY}), rws, WS_ATT + blk * (DM * 4) + lane * 16 + i * 768, 0, AUX_CL);
      }
    }
    RAWBAR(); RAWBAR();
    if (w == 10) {
      tag = (((u64)(u32)__builtin_amdgcn_readfirstlane(ep[1]) << 32) | (u32)__builtin_amdgcn_readfirstlane(ep[0])) + 1;
      if (lane == 0) *(LAS u64*)(lds + L_TG) = tag;
    }
  }
  PIN();

  if (w < 9) {
    u32x4 wf[4][4];
    const rsrc_t ro = mk(a.wo + (size_t)h * (DH * DM * 2), DH * DM * 2);
    const int vo = r * 8 * (DM * 2) + w * 128 + c * 8;
    u32x2 raw[4][8];
#pragma unroll
    for (int n = 0; n < 32; ++n) {
      raw[n >> 3][n & 7] = (ABL & 8) ? u32x2{1u,1u} : LD64(ro, vo, ((n >> 3) * 32 + (n & 7)) * (DM * 2), WAUX);
      if (n >= WOD) USE(raw[(n - WOD) >> 3][(n - WOD) & 7]);
      if (n >= WOD + 7 && ((n - WOD) & 7) == 7) tr8(raw[(n - WOD) >> 3], wf[(n - WOD) >> 3]);
      PIN();
    }
    tr8(raw[3], wf[3]);
    TSW(0, 8);
    PIN();
    softmax();
    BAR();
    TSW(0, 10);
    float o0 = 0.f, o1 = 0.f;
#pragma unroll
    for (int j = 0; j < 4; ++j) { const u32x2 t = *(LAS u32x2*)(lds + L_OP + j * 512 + lane * 8); o0 += __uint_as_float(t[0]); o1 += __uint_as_float(t[1]); }
    *(LAS u32*)(lds + L_OB + w * 256 + lane * 4) = pk(o0, o1);
    LWAIT();
    TSW(0, 25);
    f32x4 d[4];
#pragma unroll
    for (int e = 0; e < 4; ++e) d[e] = f32x4{0.f, 0.f, 0.f, 0.f};
#pragma unroll
    for (int s = 0; s < 4; ++s) {
      const u32x4 ob = *(LAS u32x4*)(lds + L_OB + w * 256 + s * 64 + r * 16);
#pragma unroll
      for (int e = 0; e < 4; ++e) d[e] = mfma(wf[s][e], ob, d[e]);
    }
    u32x4 o;
#pragma unroll
    for (int e = 0; e < 4; ++e) {
      const float t = (c & 2) ? ((c & 1) ? d[e][3] : d[e][2]) : ((c & 1) ? d[e][1] : d[e][0]);
      o[e] = min(__float_as_uint(t), EMPTY - 1);
    }
    if (c < 4) ST128(o, rws, WS_ATT + blk * (DM * 4) + w * 256 + r * 64 + c * 16, 0, AUX_AS);
    TSW(0, 11); TSW(8, 26);
    return;
  }
  if (w == 9) {
    u32x4 hf[2] = {};
    if (blk < NTASK && !(ABL & 2)) {
      {
        const rsrc_t rx = mk(a.x, NB * DM * 2);
        u32x4 xa[9];
#pragma unroll
        for (int i = 0; i < 9; ++i) xa[i] = LD128(rx, (lane >> 3) * (DM * 2) + (lane & 7) * 16, i * 128, NT);
        float s = 0.f;
#pragma unroll
        for (int i = 0; i < 9; ++i)
#pragma unroll
          for (int j = 0; j < 4; ++j) s += lo16(xa[i][j]) + hi16(xa[i][j]);
        s += dpp<X1>(s); s += dpp<X2>(s); s += dpp<HMIR>(s);
        const float mean = s * (1.f / DM);
        float ss = 0.f;
#pragma unroll
        for (int i = 0; i < 9; ++i)
#pragma unroll
          for (int j = 0; j < 4; ++j) { const float d0 = lo16(xa[i][j]) - mean, d1 = hi16(xa[i][j]) - mean; ss += d0 * d0 + d1 * d1; }
        ss += dpp<X1>(ss); ss += dpp<X2>(ss); ss += dpp<HMIR>(ss);
        const float m = rb(mean), den = rb(__builtin_amdgcn_sqrtf(rb(rb(ss * (1.f / DM)) + 1e-5f))), rd = __builtin_amdgcn_rcpf(den);
#pragma unroll
        for (int i = 0; i < 9; ++i) {
          u32x4 o;
#pragma unroll
          for (int j = 0; j < 4; ++j) o[j] = pk(fdiv(rb(lo16(xa[i][j]) - m), den, rd), fdiv(rb(hi16(xa[i][j]) - m), den, rd));
          *(LAS u32x4*)(lds + L_XN + (lane >> 3) * XN_LD + (lane & 7) * 16 + i * 128) = o;
        }
        LWAIT();
        PIN();
      }
      const rsrc_t r1 = mk(a.w1 + blk * 128, DM * FF * 2 - blk * 128), r3 = mk(a.w3 + blk * 128, DM * FF * 2 - blk * 128);
      const int vo = r * 8 * (FF * 2) + c * 8;
      const int xo = L_XN + (c & 7) * XN_LD + r * 16;
      u32x2 ra[8], rc[8];
      f32x4 g[4], u[4];
#pragma unroll
      for (int e = 0; e < 4; ++e) { g[e] = f32x4{0.f, 0.f, 0.f, 0.f}; u[e] = g[e]; }
#pragma unroll
      for (int i = 0; i < 8; ++i) ra[i] = LD64(r1, vo, i * (FF * 2), WAUX);
#pragma unroll 1
      for (int s = 0; s < 18; ++s) {
        const int sc_ = s * 32 * (FF * 2), sn = (s < 17 ? s + 1 : 17) * 32 * (FF * 2);
        const rsrc_t n1 = mk(a.w1 + blk * 128, s < 17 ? DM * FF * 2 - blk * 128 : 0);
        const u32x4 xb = *(LAS u32x4*)(lds + xo + s * 64);
        u32x4 f[4];
#pragma unroll
        for (int i = 0; i < 8; ++i) { USE(ra[i]); rc[i] = LD64(r3, vo, sc_ + i * (FF * 2), WAUX); PIN(); }
        tr8(ra, f);
#pragma unroll
        for (int e = 0; e < 4; ++e) g[e] = mfma(f[e], xb, g[e]);
        PIN();
#pragma unroll
        for (int i = 0; i < 8; ++i) { USE(rc[i]); ra[i] = LD64(n1, vo, sn + i * (FF * 2), WAUX); PIN(); }
        tr8(rc, f);
#pragma unroll
        for (int e = 0; e < 4; ++e) u[e] = mfma(f[e], xb, u[e]);
        PIN();
      }
      float hv[4][4];
#pragma unroll
      for (int p = 0; p < 4; ++p)
#pragma unroll
        for (int e = 0; e < 4; ++e) hv[p][e] = rb(silu(rb(g[e][p]))) * rb(u[e][p]);
#pragma unroll
      for (int s = 0; s < 2; ++s) hf[s] = u32x4{pk(hv[2 * s][0], hv[2 * s][1]), pk(hv[2 * s][2], hv[2 * s][3]), pk(hv[2 * s + 1][0], hv[2 * s + 1][1]), pk(hv[2 * s + 1][2], hv[2 * s + 1][3])};
      TS(9);
    }
    PIN();
    softmax();
    if (blk < NTASK && !(ABL & 2)) {
      const rsrc_t r2 = mk(a.w2 + (size_t)blk * (64 * DM * 2), 64 * DM * 2);
      const int vo = r * 16 * (DM * 2) + c * 8;
      u32x2 ra[8], rc[8];
#pragma unroll
      for (int i = 0; i < 8; ++i) ra[i] = LD64(r2, vo, i * (DM * 2), WAUX);
      const int so = L_MO + (c & 7) * (DM * 4) + r * 64 + (c >> 3) * 32;
#pragma unroll 1
      for (int W = 0; W < 9; ++W) {
        const rsrc_t n2 = mk(a.w2 + (size_t)blk * (64 * DM * 2), W < 8 ? 64 * DM * 2 : 0);
        const int sn = (W < 8 ? W + 1 : 8) * 128;
        u32x4 f[4];
        f32x4 d[4];
#pragma unroll
        for (int i = 0; i < 8; ++i) { USE(ra[i]); rc[i] = LD64(r2, vo, W * 128 + (8 + i) * (DM * 2), WAUX); PIN(); }
        tr8(ra, f);
#pragma unroll
        for (int e = 0; e < 4; ++e) d[e] = mfma(f[e], hf[0], f32x4{0.f, 0.f, 0.f, 0.f});
        PIN();
#pragma unroll
        for (int i = 0; i < 8; ++i) { USE(rc[i]); ra[i] = LD64(n2, vo, sn + i * (DM * 2), WAUX); PIN(); }
        tr8(rc, f);
#pragma unroll
        for (int e = 0; e < 4; ++e) d[e] = mfma(f[e], hf[1], d[e]);
#pragma unroll
        for (int p = 0; p < 2; ++p) {
          u32x4 o;
#pragma unroll
          for (int e = 0; e < 4; ++e) o[e] = __float_as_uint((c & 8) ? d[e][2 + p] : d[e][p]);
          *(LAS u32x4*)(lds + so + p * 16 + W * 256) = o;
        }
        PIN();
      }
      LWAIT();
      TS(15);
      if (ALIGN) { const u64 t = now() | (ALIGN - 1); while (now() <= t) __builtin_amdgcn_s_sleep(2); }
      if (!(ABL & 4)) {
#pragma unroll
        for (int i = 0; i < 18; ++i) ST128(*(LAS u32x4*)(lds + L_MO + lane * 16 + i * 1024), rws, WS_MLP + blk * (NB * DM * 4) + lane * 16, i * 1024, AUX_MS);
      }
      DRAIN();
      const u32x2 tg = *(LAS u32x2*)(lds + L_TG);
      if (lane == 0) ST64(tg, rws, WS_F2 + blk * 8, 0, SC1);
      TS(12);
    }
    BAR();
    return;
  }
  {
    DRAIN();
    if (lane == 0) ST64((u32x2{(u32)tag, (u32)(tag >> 32)}), rws, WS_F1 + blk * 8, 0, SC1);
  }
  PIN();
  softmax();
  if (h < NCONS) {
    bool ok = true;
    const u32 t0 = (u32)tag, t1 = (u32)(tag >> 32);
    const int col = h * 32 + (lane & 7) * 4;
    wait_batch(PRE_AT);
    const u32x2 xv = LD64(mk(a.x, NB * DM * 2), (b * DM + col) * 2, 0, NT);
    const u64 start = now();
    for (;;) {
      bool all = true;
#pragma unroll
      for (int i = 0; i < 2; ++i) { const u32x4 f = LD128(rws, WS_F1 + lane * 16 + i * 1024, 0, AUX_HO); all &= f[0] == t0 && f[1] == t1 && f[2] == t0 && f[3] == t1; }
      { const u32x4 f = LD128(rws, WS_F2 + (lane < 36 ? lane : 0) * 16, 0, AUX_HO); all &= f[0] == t0 && f[1] == t1 && f[2] == t0 && f[3] == t1; }
      if (__all(all)) break;
      if (now() - start > SPIN_CAP) { ok = false; break; }
      __builtin_amdgcn_s_sleep(16);
    }
    asm volatile("" ::: "memory");
    if (blk == 0 && lane == 0) ST64((u32x2{t0, t1}), rws, WS_EPOCH, 0, SC1);
    float f[4] = {0.f, 0.f, 0.f, 0.f};
    {
      u32x4 t[9];
#pragma unroll
      for (int i = 0; i < 9; ++i) t[i] = LD128(rws, WS_MLP + ((lane >> 3) * NB + b) * (DM * 4) + col * 4, i * 8 * NB * DM * 4, AUX_HO);
#pragma unroll
      for (int i = 0; i < 9; ++i)
#pragma unroll
        for (int e = 0; e < 4; ++e) f[e] += __uint_as_float(t[i][e]);
    }
    float g[4];
#pragma unroll
    for (int e = 0; e < 4; ++e) { float t = f[e]; t += dpp<ROR8>(t); g[e] = rb(silu(rb(sumrows(t)))); }
    TS(13);
    BAR();
    u32x4 t[4], u[4];
    const int ao = WS_ATT + (b * NH + (lane >> 3)) * (DM * 4) + col * 4;
#define SWEEP(t) _Pragma("unroll") for (int i = 0; i < 4; ++i) t[i] = LD128(rws, ao, i * 8 * DM * 4, AUX_HO)
#define FULL(t) __all(min(min(min(~t[0][0], ~t[0][1]), min(~t[0][2], ~t[0][3])), min(min(min(~t[1][0], ~t[1][1]), min(~t[1][2], ~t[1][3])), \
                min(min(min(~t[2][0], ~t[2][1]), min(~t[2][2], ~t[2][3])), min(min(~t[3][0], ~t[3][1]), min(~t[3][2], ~t[3][3]))))) != 0u)
    const u64 start2 = now();
    if (HINT) {
      while ((u32)__builtin_amdgcn_readfirstlane(LD32(rws, WS_PACE + b * 128 + 64, 0, AUX_HO) - g8) < 32u && now() - start2 < 1000) asm volatile("" ::: "memory");
    }
    TS(7);
    SWEEP(t);
    for (;;) {
      asm volatile("" ::: "memory");
      SWEEP(u);
      PIN();
      if (FULL(t)) break;
      asm volatile("" ::: "memory");
      SWEEP(t);
      PIN();
      if (FULL(u)) {
#pragma unroll
        for (int i = 0; i < 4; ++i) t[i] = u[i];
        break;
      }
      if (!ok || now() - start2 > SPIN_CAP) { ok = false; break; }
    }
    float y[4];
#pragma unroll
    for (int e = 0; e < 4; ++e) {
      float s = (__uint_as_float(t[0][e]) + __uint_as_float(t[1][e])) + (__uint_as_float(t[2][e]) + __uint_as_float(t[3][e]));
      s += dpp<ROR8>(s);
      s = rb(sumrows(s));
      const float xe = (e & 1) ? hi16(xv[e >> 1]) : lo16(xv[e >> 1]);
      y[e] = ok ? xe + rb(s * g[e]) : __uint_as_float(0x7fc00000u);
    }
    if (lane < 8) ST64((u32x2{pk(y[0], y[1]), pk(y[2], y[3])}), mk(a.out, NB * DM * 2), (b * DM + col) * 2, 0, 0);
    TS(14);
  } else {
    BAR();
  }
}

}

extern "C" void kernel_launch(void* const* d_in, const int* in_sizes, int n_in, void* d_out, int out_size, void* d_ws, size_t ws_size,
                              hipStream_t stream) {
  Args a;
  a.x = (const char*)d_in[0]; a.k = (const char*)d_in[1]; a.v = (const char*)d_in[2]; a.wq = (const char*)d_in[3]; a.wo = (const char*)d_in[4];
  a.w1 = (const char*)d_in[6]; a.w3 = (const char*)d_in[7]; a.w2 = (const char*)d_in[8];
  a.out = (char*)d_out; a.ws = (char*)d_ws;
  hipLaunchKernelGGL(fused_decode, dim3(NB * NH), dim3(1024), 0, stream, a);
}
```

```cpp
#include <hip/hip_runtime.h>
#include <hip/hip_bf16.h>
#include <cstdint>
#include <cstdio>
#include <vector>
#include <algorithm>

#define WIN0 40
#define WIN_MLP 2
#define PACE 0
#define GAIN 0.04f
#define LAG0 0.f
#define WLO 34
#define WHI 52
#define ALIGN 512
#define WOD 1
#define PRE_AT 58
#define AUX_MS 18
#define AUX_CL 16
#define AUX_AS 16
#define HINT 0
#define DLY 3
#define CODE_KB 0
#define ABL 0
#define KAUX 2
#define VAUX 2
#define WAUX 2
#define AUX_HO 16
#define WARM 1
#define ROT 1

namespace {

typedef unsigned u32;
typedef unsigned long long u64;
typedef unsigned short u16;
typedef u32 u32x4 __attribute__((ext_vector_type(4)));
typedef u32 u32x2 __attribute__((ext_vector_type(2)));
typedef float f32x4 __attribute__((ext_vector_type(4)));
typedef __bf16 bf16x8 __attribute__((ext_vector_type(8)));
typedef __bf16 bf16x2 __attribute__((ext_vector_type(2)));
#define GAS __attribute__((address_space(1)))
#define LAS __attribute__((address_space(3)))

constexpr int NB = 8, DM = 576, NH = 32, SK = 4096, DH = 128, FF = 4608;
constexpr int NTASK = FF / 64;
constexpr int NCONS = DM / 32;
constexpr int NT = 2, SC1 = 16;

constexpr u32 WS_EPOCH = 0, WS_F1 = 256, WS_F2 = WS_F1 + 256 * 8, WS_PACE = 4096, WS_ATT = 8192;
constexpr u32 WS_MLP = WS_ATT + NB * NH * DM * 4, WS_END = WS_MLP + NTASK * NB * DM * 4;
constexpr u32 WS_DBG = 4u << 20;
constexpr u32 EMPTY = 0xffffffffu;

constexpr int XN_LD = 1168;
constexpr int L_XN = 0, L_QP = L_XN + NB * XN_LD, L_QB = L_QP + 12 * DH * 4, L_PS = L_QB + DH * 2, L_WM = L_PS + SK * 2, L_WS = L_WM + 64,
              L_OP = L_WS + 64, L_OB = L_OP + 4 * DH * 4, L_TG = L_OB + 9 * DH * 2, L_PG = L_TG + 8, L_WN = L_PG + 4, L_XB = L_TG + 16, L_MO = L_XB + DM * 2, L_END = L_MO + NB * DM * 4;

struct Args { const char* x; const char* k; const char* v; const char* wq; const char* wo; const char* w1; const char* w3; const char* w2; char* out; char* ws; };

typedef __amdgpu_buffer_rsrc_t rsrc_t;
__device__ __forceinline__ rsrc_t mk(const char* p, u32 n) { return __builtin_amdgcn_make_buffer_rsrc((void*)p, 0, n, 0x00020000); }
#define LD128(r, vo, so, aux) __builtin_amdgcn_raw_buffer_load_b128(r, vo, so, aux)
#define LD64(r, vo, so, aux) __builtin_amdgcn_raw_buffer_load_b64(r, vo, so, aux)
#define LD32(r, vo, so, aux) __builtin_amdgcn_raw_buffer_load_b32(r, vo, so, aux)
#define ST128(v, r, vo, so, aux) __builtin_amdgcn_raw_buffer_store_b128(v, r, vo, so, aux)
#define ST64(v, r, vo, so, aux) __builtin_amdgcn_raw_buffer_store_b64(v, r, vo, so, aux)
#define PIN() __builtin_amdgcn_sched_barrier(0)
#define USE(x) asm volatile("" ::"v"(x) : "memory")
#define BAR() asm volatile("s_waitcnt lgkmcnt(0)\n\ts_barrier" ::: "memory")
#define RAWBAR() asm volatile("s_barrier" ::: "memory")
#define TOUCH(d, p) asm volatile("s_load_dword %0, %1, 0x0" : "=&s"(d) : "s"(p) : "memory")
#define GATE(i) asm volatile("s_cmp_gt_i32 %0, %1\n\ts_cbranch_scc1 Lg7_%=\n\ts_cmp_gt_i32 %0, %2\n\ts_cbranch_scc1 Lg5_%=\n\t" \
                             "s_cmp_gt_i32 %0, %3\n\ts_cbranch_scc1 Lg4_%=\n\ts_waitcnt vmcnt(3)\nLg4_%=:\n\ts_waitcnt vmcnt(4)\n" \
                             "Lg5_%=:\n\ts_waitcnt vmcnt(5)\nLg7_%=:" \
                             ::"s"(win), "i"(48 + DITH(i)), "i"(40 + DITH(i)), "i"(32 + DITH(i)) : "scc", "memory")
#define DITH(i) (((i) & 1) * 4 + ((i) & 2) + ((i) >> 2))
#define LGET(off) __hip_atomic_load((LAS u32*)(lds + (off)), __ATOMIC_RELAXED, __HIP_MEMORY_SCOPE_WORKGROUP)
#define LPUT(off, v) __hip_atomic_store((LAS u32*)(lds + (off)), (u32)(v), __ATOMIC_RELAXED, __HIP_MEMORY_SCOPE_WORKGROUP)
#define LWAIT() asm volatile("s_waitcnt lgkmcnt(0)" ::: "memory")
#define DRAIN() asm volatile("s_waitcnt vmcnt(0)" ::: "memory")

__device__ __forceinline__ u32 pk(float lo, float hi) { bf16x2 t = {(__bf16)lo, (__bf16)hi}; return __builtin_bit_cast(u32, t); }
__device__ __forceinline__ float rb(float x) { return __uint_as_float(pk(x, x) << 16); }
__device__ __forceinline__ float lo16(u32 v) { return __uint_as_float(v << 16); }
__device__ __forceinline__ float hi16(u32 v) { return __uint_as_float(v & 0xffff0000u); }
__device__ __forceinline__ float dot2(u32 a, u32 b, float c) {
  return __builtin_amdgcn_fdot2_f32_bf16(__builtin_bit_cast(bf16x2, a), __builtin_bit_cast(bf16x2, b), c, false);
}
__device__ __forceinline__ f32x4 mfma(u32x4 a, u32x4 b, f32x4 c) {
  return __builtin_amdgcn_mfma_f32_16x16x32_bf16(__builtin_bit_cast(bf16x8, a), __builtin_bit_cast(bf16x8, b), c, 0, 0, 0);
}
template <int CTRL> __device__ __forceinline__ float dpp(float x) {
  return __builtin_bit_cast(float, __builtin_amdgcn_mov_dpp(__builtin_bit_cast(int, x), CTRL, 0xf, 0xf, true));
}
constexpr int X1 = 0xB1, X2 = 0x4E, HMIR = 0x141, MIR = 0x140, ROR8 = 0x128;
__device__ __forceinline__ float sum16(float x) { x += dpp<X1>(x); x += dpp<X2>(x); x += dpp<HMIR>(x); x += dpp<MIR>(x); return x; }
__device__ __forceinline__ float max16(float x) { x = fmaxf(x, dpp<X1>(x)); x = fmaxf(x, dpp<X2>(x)); x = fmaxf(x, dpp<HMIR>(x)); x = fmaxf(x, dpp<MIR>(x)); return x; }
__device__ __forceinline__ float sumrows(float x) {
  auto s = __builtin_amdgcn_permlane16_swap(__float_as_uint(x), __float_as_uint(x), false, false);
  x = __uint_as_float(s[0]) + __uint_as_float(s[1]);
  auto t = __builtin_amdgcn_permlane32_swap(__float_as_uint(x), __float_as_uint(x), false, false);
  return __uint_as_float(t[0]) + __uint_as_float(t[1]);
}
__device__ __forceinline__ float maxrows(float x) {
  auto s = __builtin_amdgcn_permlane16_swap(__float_as_uint(x), __float_as_uint(x), false, false);
  x = fmaxf(__uint_as_float(s[0]), __uint_as_float(s[1]));
  auto t = __builtin_amdgcn_permlane32_swap(__float_as_uint(x), __float_as_uint(x), false, false);
  return fmaxf(__uint_as_float(t[0]), __uint_as_float(t[1]));
}
__device__ __forceinline__ float fdiv(float a, float d, float rd) { const float q = a * rd; return fmaf(fmaf(-q, d, a), rd, q); }
__device__ __forceinline__ float silu(float x) { const float d = 1.f + __expf(-x); return d < INFINITY ? fdiv(x, d, __builtin_amdgcn_rcpf(d)) : -0.f; }
__device__ __forceinline__ u64 now() { return __builtin_amdgcn_s_memrealtime(); }
constexpr u64 SPIN_CAP = 5000000;

#define TS(i) do {} while (0)
#define TSW(ww, i) do {} while (0)

struct Diag { u32 s[2][4]; };
__device__ __forceinline__ Diag make_diag(int c) {
  Diag d;
#pragma unroll
  for (int par = 0; par < 2; ++par)
#pragma unroll
    for (int w = 0; w < 4; ++w) {
      const u32 b = par ? 0x0302u : 0x0100u;
      d.s[par][w] = (c >> 1) == w ? ((c & 1) ? (b << 16) | 0x0c0cu : 0x0c0c0000u | b) : 0x0c0c0c0cu;
    }
  return d;
}
template <int PAR> __device__ __forceinline__ f32x4 axpy4(u32 val, u32x4 rows, const Diag& d, f32x4 acc) {
  if (ABL & 128) { USE(rows); return acc; }
  u32x4 af;
#pragma unroll
  for (int w = 0; w < 4; ++w) af[w] = __builtin_amdgcn_perm(val, val, d.s[PAR][w]);
  return mfma(af, rows, acc);
}
__device__ __forceinline__ float score4(u32x4 k, u32x4 q) {
  if (ABL & 128) { USE(k); return 1.f; }
  float d = dot2(k[0], q[0], 0.f); d = dot2(k[1], q[1], d); d = dot2(k[2], q[2], d); d = dot2(k[3], q[3], d);
  return sum16(d);
}
__device__ __forceinline__ void tr8(const u32x2 (&raw)[8], u32x4 (&f)[4]) {
#pragma unroll
  for (int e = 0; e < 4; ++e)
#pragma unroll
    for (int d = 0; d < 4; ++d) f[e][d] = __builtin_amdgcn_perm(raw[2 * d + 1][e >> 1], raw[2 * d][e >> 1], (e & 1) ? 0x07060302u : 0x05040100u);
}

__global__ void __launch_bounds__(1024) fused_decode(Args a) {
  __shared__ __attribute__((aligned(16))) char lds_[L_END];
  LAS char* lds = (LAS char*)lds_;
  if (CODE_KB && __builtin_amdgcn_readfirstlane(threadIdx.x >> 6) == 11 && (blockIdx.x >> 3) < CODE_KB) {
    u64 p;
    u32 c0, c1, c2, c3, c4, c5, c6, c7;
    asm volatile("s_getpc_b64 %0" : "=s"(p));
    p = (p & ~255ull) + (blockIdx.x >> 3) * 1024;
    asm volatile("s_load_dword %0, %8, 0x0\n\ts_load_dword %1, %8, 0x80\n\ts_load_dword %2, %8, 0x100\n\ts_load_dword %3, %8, 0x180\n\t"
                 "s_load_dword %4, %8, 0x200\n\ts_load_dword %5, %8, 0x280\n\ts_load_dword %6, %8, 0x300\n\ts_load_dword %7, %8, 0x380\n\ts_waitcnt lgkmcnt(0)"
                 : "=&s"(c0), "=&s"(c1), "=&s"(c2), "=&s"(c3), "=&s"(c4), "=&s"(c5), "=&s"(c6), "=&s"(c7) : "s"(p) : "memory");
  }
  asm volatile("" ::"s"(a.x), "s"(a.ws));
  const int tid = threadIdx.x, lane = tid & 63, w = __builtin_amdgcn_readfirstlane(tid >> 6), r = lane >> 4, c = lane & 15;
  const int blk = blockIdx.x, b = blk >> 5, h = blk & 31;
  const char* kbase = a.k + ((size_t)blk << 20);
  const char* vbase = a.v + ((size_t)blk << 20);
  const rsrc_t rk = mk(kbase, 1u << 20);
  const int sw = w & 3;
  const int vA = lane * 16 + sw * 4096, vB = vA + 16384;
#define KVLD_(rs, beta, i, AUX) LD128(rs, ((i) < 4 ? vA : vB) + ((i) & 3) * 1024, (beta) * 32768, AUX)
#define KVLD(rs, beta, i) KVLD_(rs, beta, i, KAUX)
#define VLD(rs, beta, i) KVLD_(rs, beta, i, VAUX)
  float mx = -INFINITY;
  auto put_scores = [&](float sc, int batch) {
    const float s = rb(rb(sc) * 0.08838834764831845f);
    mx = fmaxf(mx, s);
    if (c < 8) *(LAS u16*)(lds + L_PS + batch * 64 + r * 16 + c * 2) = (u16)(__float_as_uint(s) >> 16);
  };
  auto softmax = [&]() {
    const float wm = maxrows(max16(mx));
    if (lane == 0) *(LAS float*)(lds + L_WM + w * 4) = wm;
    BAR();
    const float M = max16(*(LAS float*)(lds + L_WM + c * 4));
    const u32x2 sv = *(LAS u32x2*)(lds + L_PS + tid * 8);
    const float e0 = __expf(lo16(sv[0]) - M), e1 = __expf(hi16(sv[0]) - M), e2 = __expf(lo16(sv[1]) - M), e3 = __expf(hi16(sv[1]) - M);
    const float ws = sumrows(sum16((e0 + e1) + (e2 + e3)));
    if (lane == 0) *(LAS float*)(lds + L_WS + w * 4) = ws;
    BAR();
    const float inv = 1.f / sum16(*(LAS float*)(lds + L_WS + c * 4));
    *(LAS u32x2*)(lds + L_PS + tid * 8) = u32x2{pk(e0 * inv, e1 * inv), pk(e2 * inv, e3 * inv)};
    BAR();
  };

  if (w >= 12) {
    TSW(12, 0);
    u32x4 ring[8];
    RAWBAR();
#pragma unroll
    for (int i = 0; i < 7; ++i) { ring[i] = KVLD(rk, 0, i); PIN(); }
    __builtin_amdgcn_s_setprio(2);
    RAWBAR(); RAWBAR();
    TSW(12, 1);
    const u32x4 q = *(LAS u32x4*)(lds + L_QB + c * 16);
    float sc = 0.f;
    u32 wn = LGET(L_WN);
#pragma unroll 1
    for (int beta = 0; beta < 32; ++beta) {
      const rsrc_t rn = mk(beta < 31 ? kbase : vbase, 1u << 20);
      const int bn = beta < 31 ? beta + 1 : 0;
      const int win = beta < 31 ? __builtin_amdgcn_readfirstlane(wn) : 64;
      wn = LGET(L_WN);
      if (tid == 768) LPUT(L_PG, beta);
#define KSTEP(i) { GATE(i); if (i == 0) ring[7] = KVLD(rk, beta, 7); else ring[(i + 7) & 7] = KVLD(rn, bn, (i + 7) & 7); PIN(); \
        const float d = score4(ring[i], q); sc = (c & 7) == i ? d : sc; PIN(); }
      KSTEP(0) KSTEP(1) KSTEP(2) KSTEP(3) KSTEP(4) KSTEP(5) KSTEP(6) KSTEP(7)
      put_scores(sc, beta * 4 + sw);
    }
    const rsrc_t rv = mk(vbase, 1u << 20);
    u32x4 ext[8];
    ring[7] = VLD(rv, 0, 7);
#pragma unroll
    for (int i = 0; i < 8; ++i) { ext[i] = VLD(rv, 1, i); PIN(); }
    TSW(12, 2);
    softmax();
    TSW(12, 3);
    const Diag dg = make_diag(c);
    f32x4 acc = {0.f, 0.f, 0.f, 0.f};
    const int po = L_PS + sw * 64 + r * 16;
    u32x4 pn = *(LAS u32x4*)(lds + po + 512);
    {
      const u32x4 p0 = *(LAS u32x4*)(lds + po), p1 = *(LAS u32x4*)(lds + po + 256);
      const int win = __builtin_amdgcn_readfirstlane(wn);
#pragma unroll
      for (int i = 0; i < 8; ++i) { acc = (i & 1) ? axpy4<1>(p0[i >> 1], ring[i], dg, acc) : axpy4<0>(p0[i >> 1], ring[i], dg, acc); PIN(); }
#define ESTEP(i) { if (i > 0) { GATE(i); ring[(i + 7) & 7] = VLD(rv, 2, (i + 7) & 7); } PIN(); acc = axpy4<i & 1>(p1[i >> 1], ext[i], dg, acc); PIN(); }
      ESTEP(0) ESTEP(1) ESTEP(2) ESTEP(3) ESTEP(4) ESTEP(5) ESTEP(6) ESTEP(7)
    }
#pragma unroll 1
    for (int beta = 2; beta < 32; ++beta) {
      const rsrc_t rn = mk(vbase, beta < 31 ? 1u << 20 : 0u);
      const int bn = beta < 31 ? beta + 1 : 0;
      const int win = beta < 31 ? __builtin_amdgcn_readfirstlane(wn) : 64;
      const u32x4 pv = pn;
      pn = *(LAS u32x4*)(lds + po + bn * 256);
      wn = LGET(L_WN);
      if (tid == 768) LPUT(L_PG, 32 + beta);
#define VSTEP(i) { GATE(i); if (i == 0) ring[7] = VLD(rv, beta, 7); else ring[(i + 7) & 7] = VLD(rn, bn, (i + 7) & 7); PIN(); \
        acc = axpy4<i & 1>(pv[i >> 1], ring[i], dg, acc); PIN(); }
      VSTEP(0) VSTEP(1) VSTEP(2) VSTEP(3) VSTEP(4) VSTEP(5) VSTEP(6) VSTEP(7)
    }
    if (r < 2) *(LAS f32x4*)(lds + L_OP + sw * 512 + c * 32 + r * 16) = acc;
    TSW(12, 4);
    BAR();
    return;
  }

  const rsrc_t rws = mk(a.ws, WS_END);
  auto wait_batch = [&](u32 n) { while (LGET(L_PG) < n) __builtin_amdgcn_s_sleep(4); };
  if (w == 11) {
    u32 t0, t1, t2, t3, t4, t5, t6, t7;
    asm volatile("" ::"s"(a.ws), "s"(a.out), "s"(kbase), "s"(vbase), "s"(a.w2));
    TOUCH(t0, kbase); TOUCH(t1, a.ws);
    RAWBAR();
    TOUCH(t2, a.wo + (size_t)h * (DH * DM * 2)); TOUCH(t3, a.w1); TOUCH(t4, a.w3); TOUCH(t5, vbase); TOUCH(t6, a.w2); TOUCH(t7, a.out);
    const u32x2 g0 = LD64(rws, WS_PACE + b * 128 + (lane & 15) * 8, 0, AUX_HO);
    RAWBAR(); RAWBAR();
    asm volatile("s_waitcnt lgkmcnt(0)" : "+s"(t0), "+s"(t1), "+s"(t2), "+s"(t3), "+s"(t4), "+s"(t5), "+s"(t6), "+s"(t7)::"memory");
    TS(17);
    const int base = WIN0 + (blk < NTASK ? WIN_MLP : 0);
    GAS u64* const arr = (GAS u64*)(a.ws + WS_PACE + b * 128);
    auto pace = [&](int k, u32 at, float gain) {
      wait_batch(at);
      const u32 t = (u32)now();
      if (lane == 0) __hip_atomic_fetch_add(arr + k, (1ull << 40) + t, __ATOMIC_RELAXED, __HIP_MEMORY_SCOPE_AGENT);
      wait_batch(at + DLY);
      const u32x2 v = LD64(rws, WS_PACE + b * 128 + k * 8, 0, AUX_HO);
      const u64 sum = (((u64)v[1] << 32) | v[0]) - (((u64)(u32)__builtin_amdgcn_readlane(g0[1], k) << 32) | (u32)__builtin_amdgcn_readlane(g0[0], k));
      const u32 n = (u32)(sum >> 40);
      const float lag = n ? (float)(int)(n * t - (u32)sum) * __builtin_amdgcn_rcpf((float)n) : 0.f;
      const int wnew = min(max(base + (int)((lag - LAG0) * gain), WLO), WHI);
      if (lane == 0) LPUT(L_WN, wnew);
    };
    if (PACE) {
#pragma unroll 1
      for (int k = 0; k < 4; ++k) pace(k, 2 + 8 * k, GAIN);
    }
    softmax();
    if (PACE) {
#pragma unroll 1
      for (int k = 0; k < 4; ++k) pace(4 + k, (0x36302922u >> (8 * k)) & 255u, GAIN * (1.f + 0.33f * k));
    }
    BAR();
    if (HINT && lane == 0) __hip_atomic_fetch_add(arr + 8, 1ull, __ATOMIC_RELAXED, __HIP_MEMORY_SCOPE_AGENT);
    return;
  }
  u64 tag = 0;
  u32 g8 = 0;
  if (w < 9) {
    const int wr = ROT ? (w + b) % 9 : w;
    u32x4 wb[16];
    {
      const rsrc_t rq = mk(a.wq + h * 256, DM * 8192 - h * 256);
      const int vo = (64 * wr + 16 * r) * 8192 + c * 16;
#pragma unroll
      for (int i = 0; i < 16; ++i) wb[i] = (ABL & 32) ? u32x4{1u,1u,1u,1u} : LD128(rq, vo, i * 8192, WAUX);
    }
    PIN();
    TSW(0, 16);
    const Diag dgq = make_diag(c);
    RAWBAR();
    TSW(0, 21);
    {
      const int xo = L_XB + (64 * wr + 16 * r) * 2;
      const u32x4 x0 = *(LAS u32x4*)(lds + xo), x1 = *(LAS u32x4*)(lds + xo + 16);
      f32x4 acc0 = {0.f, 0.f, 0.f, 0.f}, acc1 = acc0;
#pragma unroll
      for (int i = 0; i < 8; ++i) {
        const u32 xv = i < 4 ? x0[i & 3] : x1[i & 3];
        acc0 = axpy4<0>(xv, wb[2 * i], dgq, acc0); acc1 = axpy4<1>(xv, wb[2 * i + 1], dgq, acc1);
      }
      if (r < 2) *(LAS f32x4*)(lds + L_QP + wr * 512 + c * 32 + r * 16) = acc0 + acc1;
    }
    TSW(0, 22); TSW(8, 23);
    BAR();
    TSW(0, 24);
    if (w < 2) {
      float s = 0.f;
#pragma unroll
      for (int j = 0; j < 9; ++j) s += *(LAS float*)(lds + L_QP + j * 512 + tid * 4);
      *(LAS u16*)(lds + L_QB + tid * 2) = (u16)pk(s, s);
    }
    BAR();
  } else {
    __builtin_amdgcn_s_setprio(3);
    if (w == 10) {
      const rsrc_t rx = mk(a.x + b * (DM * 2), DM * 2);
      u32 xr[5];
#pragma unroll
      for (int i = 0; i < 5; ++i) xr[i] = LD32(rx, lane * 4 + i * 256, 0, NT);
      if (lane == 0) { LPUT(L_PG, 0); LPUT(L_WN, WIN0 + (blk < NTASK ? WIN_MLP : 0)); }
      float s = 0.f;
#pragma unroll
      for (int i = 0; i < 5; ++i) s += lo16(xr[i]) + hi16(xr[i]);
      const float mean = sumrows(sum16(s)) * (1.f / DM);
      float ss = 0.f;
#pragma unroll
      for (int i = 0; i < 5; ++i) {
        const float d0 = lo16(xr[i]) - mean, d1 = hi16(xr[i]) - mean;
        ss += (i < 4 || lane < 32) ? d0 * d0 + d1 * d1 : 0.f;
      }
      const float var = sumrows(sum16(ss)) * (1.f / DM);
      const float m = rb(mean), den = rb(__builtin_amdgcn_sqrtf(rb(rb(var) + 1e-5f))), rd = __builtin_amdgcn_rcpf(den);
#pragma unroll
      for (int i = 0; i < 5; ++i)
        if (i < 4 || lane < 32) *(LAS u32*)(lds + L_XB + lane * 4 + i * 256) = pk(fdiv(rb(lo16(xr[i]) - m), den, rd), fdiv(rb(hi16(xr[i]) - m), den, rd));
    }
    TSW(10, 19);
    BAR();
    __builtin_amdgcn_s_setprio(0);
    u32x2 ep = {0u, 0u};
    if (w == 10) {
      ep = LD64(rws, WS_EPOCH, 0, AUX_HO);
      g8 = LD32(rws, WS_PACE + b * 128 + 64, 0, AUX_HO);
      if (lane < 48 && !(ABL & 16)) {
#pragma unroll
        for (int i = 0; i < 3; ++i) ST128((u32x4{EMPTY, EMPTY, EMPTY, EMPTY}), rws, WS_ATT + blk * (DM * 4) + lane * 16 + i * 768, 0, AUX_CL);
      }
    }
    RAWBAR(); RAWBAR();
    if (w == 10) {
      tag = (((u64)(u32)__builtin_amdgcn_readfirstlane(ep[1]) << 32) | (u32)__builtin_amdgcn_readfirstlane(ep[0])) + 1;
      if (lane == 0) *(LAS u64*)(lds + L_TG) = tag;
    }
  }
  PIN();

  if (w < 9) {
    u32x4 wf[4][4];
    const rsrc_t ro = mk(a.wo + (size_t)h * (DH * DM * 2), DH * DM * 2);
    const int vo = r * 8 * (DM * 2) + w * 128 + c * 8;
    u32x2 raw[4][8];
#pragma unroll
    for (int n = 0; n < 32; ++n) {
      raw[n >> 3][n & 7] = (ABL & 8) ? u32x2{1u,1u} : LD64(ro, vo, ((n >> 3) * 32 + (n & 7)) * (DM * 2), WAUX);
      if (n >= WOD) USE(raw[(n - WOD) >> 3][(n - WOD) & 7]);
      if (n >= WOD + 7 && ((n - WOD) & 7) == 7) tr8(raw[(n - WOD) >> 3], wf[(n - WOD) >> 3]);
      PIN();
    }
    tr8(raw[3], wf[3]);
    TSW(0, 8);
    PIN();
    softmax();
    BAR();
    TSW(0, 10);
    float o0 = 0.f, o1 = 0.f;
#pragma unroll
    for (int j = 0; j < 4; ++j) { const u32x2 t = *(LAS u32x2*)(lds + L_OP + j * 512 + lane * 8); o0 += __uint_as_float(t[0]); o1 += __uint_as_float(t[1]); }
    *(LAS u32*)(lds + L_OB + w * 256 + lane * 4) = pk(o0, o1);
    LWAIT();
    TSW(0, 25);
    f32x4 d[4];
#pragma unroll
    for (int e = 0; e < 4; ++e) d[e] = f32x4{0.f, 0.f, 0.f, 0.f};
#pragma unroll
    for (int s = 0; s < 4; ++s) {
      const u32x4 ob = *(LAS u32x4*)(lds + L_OB + w * 256 + s * 64 + r * 16);
#pragma unroll
      for (int e = 0; e < 4; ++e) d[e] = mfma(wf[s][e], ob, d[e]);
    }
    u32x4 o;
#pragma unroll
    for (int e = 0; e < 4; ++e) {
      const float t = (c & 2) ? ((c & 1) ? d[e][3] : d[e][2]) : ((c & 1) ? d[e][1] : d[e][0]);
      o[e] = min(__float_as_uint(t), EMPTY - 1);
    }
    if (c < 4) ST128(o, rws, WS_ATT + blk * (DM * 4) + w * 256 + r * 64 + c * 16, 0, AUX_AS);
    TSW(0, 11); TSW(8, 26);
    return;
  }
  if (w == 9) {
    u32x4 hf[2] = {};
    if (blk < NTASK && !(ABL & 2)) {
      {
        const rsrc_t rx = mk(a.x, NB * DM * 2);
        u32x4 xa[9];
#pragma unroll
        for (int i = 0; i < 9; ++i) xa[i] = LD128(rx, (lane >> 3) * (DM * 2) + (lane & 7) * 16, i * 128, NT);
        float s = 0.f;
#pragma unroll
        for (int i = 0; i < 9; ++i)
#pragma unroll
          for (int j = 0; j < 4; ++j) s += lo16(xa[i][j]) + hi16(xa[i][j]);
        s += dpp<X1>(s); s += dpp<X2>(s); s += dpp<HMIR>(s);
        const float mean = s * (1.f / DM);
        float ss = 0.f;
#pragma unroll
        for (int i = 0; i < 9; ++i)
#pragma unroll
          for (int j = 0; j < 4; ++j) { const float d0 = lo16(xa[i][j]) - mean, d1 = hi16(xa[i][j]) - mean; ss += d0 * d0 + d1 * d1; }
        ss += dpp<X1>(ss); ss += dpp<X2>(ss); ss += dpp<HMIR>(ss);
        const float m = rb(mean), den = rb(__builtin_amdgcn_sqrtf(rb(rb(ss * (1.f / DM)) + 1e-5f))), rd = __builtin_amdgcn_rcpf(den);
#pragma unroll
        for (int i = 0; i < 9; ++i) {
          u32x4 o;
#pragma unroll
          for (int j = 0; j < 4; ++j) o[j] = pk(fdiv(rb(lo16(xa[i][j]) - m), den, rd), fdiv(rb(hi16(xa[i][j]) - m), den, rd));
          *(LAS u32x4*)(lds + L_XN + (lane >> 3) * XN_LD + (lane & 7) * 16 + i * 128) = o;
        }
        LWAIT();
        PIN();
      }
      const rsrc_t r1 = mk(a.w1 + blk * 128, DM * FF * 2 - blk * 128), r3 = mk(a.w3 + blk * 128, DM * FF * 2 - blk * 128);
      const int vo = r * 8 * (FF * 2) + c * 8;
      const int xo = L_XN + (c & 7) * XN_LD + r * 16;
      u32x2 ra[8], rc[8];
      f32x4 g[4], u[4];
#pragma unroll
      for (int e = 0; e < 4; ++e) { g[e] = f32x4{0.f, 0.f, 0.f, 0.f}; u[e] = g[e]; }
#pragma unroll
      for (int i = 0; i < 8; ++i) ra[i] = LD64(r1, vo, i * (FF * 2), WAUX);
#pragma unroll 1
      for (int s = 0; s < 18; ++s) {
        const int sc_ = s * 32 * (FF * 2), sn = (s < 17 ? s + 1 : 17) * 32 * (FF * 2);
        const rsrc_t n1 = mk(a.w1 + blk * 128, s < 17 ? DM * FF * 2 - blk * 128 : 0);
        const u32x4 xb = *(LAS u32x4*)(lds + xo + s * 64);
        u32x4 f[4];
#pragma unroll
        for (int i = 0; i < 8; ++i) { USE(ra[i]); rc[i] = LD64(r3, vo, sc_ + i * (FF * 2), WAUX); PIN(); }
        tr8(ra, f);
#pragma unroll
        for (int e = 0; e < 4; ++e) g[e] = mfma(f[e], xb, g[e]);
        PIN();
#pragma unroll
        for (int i = 0; i < 8; ++i) { USE(rc[i]); ra[i] = LD64(n1, vo, sn + i * (FF * 2), WAUX); PIN(); }
        tr8(rc, f);
#pragma unroll
        for (int e = 0; e < 4; ++e) u[e] = mfma(f[e], xb, u[e]);
        PIN();
      }
      float hv[4][4];
#pragma unroll
      for (int p = 0; p < 4; ++p)
#pragma unroll
        for (int e = 0; e < 4; ++e) hv[p][e] = rb(silu(rb(g[e][p]))) * rb(u[e][p]);
#pragma unroll
      for (int s = 0; s < 2; ++s) hf[s] = u32x4{pk(hv[2 * s][0], hv[2 * s][1]), pk(hv[2 * s][2], hv[2 * s][3]), pk(hv[2 * s + 1][0], hv[2 * s + 1][1]), pk(hv[2 * s + 1][2], hv[2 * s + 1][3])};
      TS(9);
    }
    PIN();
    softmax();
    if (blk < NTASK && !(ABL & 2)) {
      const rsrc_t r2 = mk(a.w2 + (size_t)blk * (64 * DM * 2), 64 * DM * 2);
      const int vo = r * 16 * (DM * 2) + c * 8;
      u32x2 ra[8], rc[8];
#pragma unroll
      for (int i = 0; i < 8; ++i) ra[i] = LD64(r2, vo, i * (DM * 2), WAUX);
      const int so = L_MO + (c & 7) * (DM * 4) + r * 64 + (c >> 3) * 32;
#pragma unroll 1
      for (int W = 0; W < 9; ++W) {
        const rsrc_t n2 = mk(a.w2 + (size_t)blk * (64 * DM * 2), W < 8 ? 64 * DM * 2 : 0);
        const int sn = (W < 8 ? W + 1 : 8) * 128;
        u32x4 f[4];
        f32x4 d[4];
#pragma unroll
        for (int i = 0; i < 8; ++i) { USE(ra[i]); rc[i] = LD64(r2, vo, W * 128 + (8 + i) * (DM * 2), WAUX); PIN(); }
        tr8(ra, f);
#pragma unroll
        for (int e = 0; e < 4; ++e) d[e] = mfma(f[e], hf[0], f32x4{0.f, 0.f, 0.f, 0.f});
        PIN();
#pragma unroll
        for (int i = 0; i < 8; ++i) { USE(rc[i]); ra[i] = LD64(n2, vo, sn + i * (DM * 2), WAUX); PIN(); }
        tr8(rc, f);
#pragma unroll
        for (int e = 0; e < 4; ++e) d[e] = mfma(f[e], hf[1], d[e]);
#pragma unroll
        for (int p = 0; p < 2; ++p) {
          u32x4 o;
#pragma unroll
          for (int e = 0; e < 4; ++e) o[e] = __float_as_uint((c & 8) ? d[e][2 + p] : d[e][p]);
          *(LAS u32x4*)(lds + so + p * 16 + W * 256) = o;
        }
        PIN();
      }
      LWAIT();
      TS(15);
      if (ALIGN) { const u64 t = now() | (ALIGN - 1); while (now() <= t) __builtin_amdgcn_s_sleep(2); }
      if (!(ABL & 4)) {
#pragma unroll
        for (int i = 0; i < 18; ++i) ST128(*(LAS u32x4*)(lds + L_MO + lane * 16 + i * 1024), rws, WS_MLP + blk * (NB * DM * 4) + lane * 16, i * 1024, AUX_MS);
      }
      DRAIN();
      const u32x2 tg = *(LAS u32x2*)(lds + L_TG);
      if (lane == 0) ST64(tg, rws, WS_F2 + blk * 8, 0, SC1);
      TS(12);
    }
    BAR();
    return;
  }
  {
    DRAIN();
    if (lane == 0) ST64((u32x2{(u32)tag, (u32)(tag >> 32)}), rws, WS_F1 + blk * 8, 0, SC1);
  }
  PIN();
  softmax();
  if (h < NCONS) {
    bool ok = true;
    const u32 t0 = (u32)tag, t1 = (u32)(tag >> 32);
    const int col = h * 32 + (lane & 7) * 4;
    wait_batch(PRE_AT);
    const u32x2 xv = LD64(mk(a.x, NB * DM * 2), (b * DM + col) * 2, 0, NT);
    const u64 start = now();
    for (;;) {
      bool all = true;
#pragma unroll
      for (int i = 0; i < 2; ++i) { const u32x4 f = LD128(rws, WS_F1 + lane * 16 + i * 1024, 0, AUX_HO); all &= f[0] == t0 && f[1] == t1 && f[2] == t0 && f[3] == t1; }
      { const u32x4 f = LD128(rws, WS_F2 + (lane < 36 ? lane : 0) * 16, 0, AUX_HO); all &= f[0] == t0 && f[1] == t1 && f[2] == t0 && f[3] == t1; }
      if (__all(all)) break;
      if (now() - start > SPIN_CAP) { ok = false; break; }
      __builtin_amdgcn_s_sleep(16);
    }
    asm volatile("" ::: "memory");
    if (blk == 0 && lane == 0) ST64((u32x2{t0, t1}), rws, WS_EPOCH, 0, SC1);
    float f[4] = {0.f, 0.f, 0.f, 0.f};
    {
      u32x4 t[9];
#pragma unroll
      for (int i = 0; i < 9; ++i) t[i] = LD128(rws, WS_MLP + ((lane >> 3) * NB + b) * (DM * 4) + col * 4, i * 8 * NB * DM * 4, AUX_HO);
#pragma unroll
      for (int i = 0; i < 9; ++i)
#pragma unroll
        for (int e = 0; e < 4; ++e) f[e] += __uint_as_float(t[i][e]);
    }
    float g[4];
#pragma unroll
    for (int e = 0; e < 4; ++e) { float t = f[e]; t += dpp<ROR8>(t); g[e] = rb(silu(rb(sumrows(t)))); }
    TS(13);
    BAR();
    u32x4 t[4], u[4];
    const int ao = WS_ATT + (b * NH + (lane >> 3)) * (DM * 4) + col * 4;
#define SWEEP(t) _Pragma("unroll") for (int i = 0; i < 4; ++i) t[i] = LD128(rws, ao, i * 8 * DM * 4, AUX_HO)
#define FULL(t) __all(min(min(min(~t[0][0], ~t[0][1]), min(~t[0][2], ~t[0][3])), min(min(min(~t[1][0], ~t[1][1]), min(~t[1][2], ~t[1][3])), \
                min(min(min(~t[2][0], ~t[2][1]), min(~t[2][2], ~t[2][3])), min(min(~t[3][0], ~t[3][1]), min(~t[3][2], ~t[3][3]))))) != 0u)
    const u64 start2 = now();
    if (HINT) {
      while ((u32)__builtin_amdgcn_readfirstlane(LD32(rws, WS_PACE + b * 128 + 64, 0, AUX_HO) - g8) < 32u && now() - start2 < 1000) asm volatile("" ::: "memory");
    }
    TS(7);
    SWEEP(t);
    for (;;) {
      asm volatile("" ::: "memory");
      SWEEP(u);
      PIN();
      if (FULL(t)) break;
      asm volatile("" ::: "memory");
      SWEEP(t);
      PIN();
      if (FULL(u)) {
#pragma unroll
        for (int i = 0; i < 4; ++i) t[i] = u[i];
        break;
      }
      if (!ok || now() - start2 > SPIN_CAP) { ok = false; break; }
    }
    float y[4];
#pragma unroll
    for (int e = 0; e < 4; ++e) {
      float s = (__uint_as_float(t[0][e]) + __uint_as_float(t[1][e])) + (__uint_as_float(t[2][e]) + __uint_as_float(t[3][e]));
      s += dpp<ROR8>(s);
      s = rb(sumrows(s));
      const float xe = (e & 1) ? hi16(xv[e >> 1]) : lo16(xv[e >> 1]);
      y[e] = ok ? xe + rb(s * g[e]) : __uint_as_float(0x7fc00000u);
    }
    if (lane < 8) ST64((u32x2{pk(y[0], y[1]), pk(y[2], y[3])}), mk(a.out, NB * DM * 2), (b * DM + col) * 2, 0, 0);
    TS(14);
  } else {
    BAR();
  }
}

#define RSRC(p, n) __builtin_amdgcn_make_buffer_rsrc((void*)(p), 0, n, 0x00020000)
#define USE_(x) asm volatile("" :: "v"(x) : "memory")
#define LD_(r, vo, so, AUX) __builtin_amdgcn_raw_buffer_load_b128(r, vo, so, AUX)
__global__ void __launch_bounds__(256) strm(const char* K0, const char* V0, char* ws) {
  __shared__ int pad[24000];
  const int tid = threadIdx.x, lane = tid & 63, w = __builtin_amdgcn_readfirstlane(tid >> 6);
  unsigned long long* ts = (unsigned long long*)(ws + (4u << 20)) + blockIdx.x * 32;
  if (tid == 0) ts[0] = __builtin_amdgcn_s_memrealtime();
  const char* K = K0 + ((size_t)blockIdx.x << 20); const char* V = V0 + ((size_t)blockIdx.x << 20);
  constexpr int NW = 4, C = 4, D = 5, UN = 4 * 5, NL = 256, ITERS = (NL + UN - 1) / UN;
  auto off = [&](int n) { int o = (((n / C) * NW + w) * C + (n % C)) * 1024; return o >= 1048576 ? 0x40000000 : o; };
  u32x4 a[D];
  const int vl = lane * 16;
#pragma unroll
  for (int u = 0; u < D; ++u) a[u] = u32x4{0, 0, 0, 0};
  if (false) { u32x4 e[1]; auto r0 = RSRC(K, 1 << 20);
#pragma unroll
    for (int u = 0; u < 0; ++u) e[u] = LD_(r0, vl + off(u), 0, 2);
#pragma unroll
    for (int u = 0; u < 0; ++u) USE_(e[u]);
    if (tid == 0) ts[1] = __builtin_amdgcn_s_memrealtime(); }
#pragma unroll
  for (int s = 0; s < 2; ++s) {
    auto r0 = RSRC(s ? V : K, 1 << 20);
#pragma unroll 1
    for (int it = 0; it < ITERS; ++it) {
#pragma unroll
      for (int u = 0; u < UN; ++u) { USE_(a[u % D]); a[u % D] = LD_(r0, vl + off(it * UN + u + (s ? 0 : 0)), 0, 2); }
    }
    if (s == 0 && tid == 0) ts[2] = __builtin_amdgcn_s_memrealtime();
  }
#pragma unroll
  for (int u = 0; u < D; ++u) USE_(a[u]);
  pad[tid] = 1;
  __syncthreads();
  if (tid == 0) ts[4] = __builtin_amdgcn_s_memrealtime() + pad[5] - 1;
}
}

extern "C" void kernel_launch(void* const* d_in, const int* in_sizes, int n_in, void* d_out, int out_size, void* d_ws, size_t ws_size,
                              hipStream_t stream) {
  Args a;
  a.x = (const char*)d_in[0]; a.k = (const char*)d_in[1]; a.v = (const char*)d_in[2]; a.wq = (const char*)d_in[3]; a.wo = (const char*)d_in[4];
  a.w1 = (const char*)d_in[6]; a.w3 = (const char*)d_in[7]; a.w2 = (const char*)d_in[8];
  a.out = (char*)d_out; a.ws = (char*)d_ws;
  hipStreamCaptureStatus cs; (void)hipStreamIsCapturing(stream, &cs);
  if (cs == hipStreamCaptureStatusNone) {
    hipEvent_t e0, e1; (void)hipEventCreate(&e0); (void)hipEventCreate(&e1);
    for (int which = 0; which < 2; ++which) {
      hipGraph_t gr; hipGraphExec_t ge;
      (void)hipStreamBeginCapture(stream, hipStreamCaptureModeRelaxed);
      for (int i = 0; i < 20; ++i) { if (which) hipLaunchKernelGGL(fused_decode, dim3(NB * NH), dim3(1024), 0, stream, a); else strm<<<256, 256, 0, stream>>>(a.k, a.v, a.ws); }
      (void)hipStreamEndCapture(stream, &gr); (void)hipGraphInstantiate(&ge, gr, nullptr, nullptr, 0);
      std::vector<float> g;
      for (int r = 0; r < 9; ++r) { (void)hipEventRecord(e0, stream); (void)hipGraphLaunch(ge, stream); (void)hipEventRecord(e1, stream); (void)hipStreamSynchronize(stream); float ms; (void)hipEventElapsedTime(&ms, e0, e1); g.push_back(ms * 50); }
      std::sort(g.begin(), g.end());
      fprintf(stderr, "OWN b2b %s: med %.2f min %.2f us\n", which ? "fused" : "strm5", g[4], g[0]);
    }
  }
  hipLaunchKernelGGL(fused_decode, dim3(NB * NH), dim3(1024), 0, stream, a);
}
```

```cpp
#include <hip/hip_runtime.h>
#include <hip/hip_bf16.h>
#include <cstdint>

#define WIN0 40
#define WIN_MLP 2
#define PACE 1
#define GAIN 20
#define LAG0 0.f
#define WLO 34
#define WHI 56
#define ALIGN 512
#define WOD 2
#define PRE_AT 58
#define AUX_MS 18
#define AUX_CL 16
#define AUX_AS 16
#define HINT 0
#define DLY 3
#define CODE_KB 0
#define WMID 45
#define EPS 0.01f
#define PGRP 0
#define PRD 0
#define PSTR 8
#define AUX_PS 18
#define CPK 0x19110903
#define CPV 0x39332b23
#define AUX_FL 16
#define ABL 0
#define KAUX 2
#define VAUX 2
#define QAUX 2
#define OAUX 2
#define WAUX 2
#define AUX_HO 16
#define WARM 1
#define ROT 1

namespace {

typedef unsigned u32;
typedef unsigned long long u64;
typedef unsigned short u16;
typedef u32 u32x4 __attribute__((ext_vector_type(4)));
typedef u32 u32x2 __attribute__((ext_vector_type(2)));
typedef float f32x4 __attribute__((ext_vector_type(4)));
typedef __bf16 bf16x8 __attribute__((ext_vector_type(8)));
typedef __bf16 bf16x2 __attribute__((ext_vector_type(2)));
#define GAS __attribute__((address_space(1)))
#define LAS __attribute__((address_space(3)))

constexpr int NB = 8, DM = 576, NH = 32, SK = 4096, DH = 128, FF = 4608;
constexpr int NTASK = FF / 64;
constexpr int NCONS = DM / 32;
constexpr int NT = 2, SC1 = 16;

constexpr u32 ATT_LD = DM * 4 + 128, MLP_LD = NB * DM * 4 + 128;
constexpr u32 PACE_LD = 4352;
constexpr u32 WS_EPOCH = 0, WS_ATT = 4096, WS_MLP = WS_ATT + NB * NH * ATT_LD, WS_PACE = WS_MLP + NTASK * MLP_LD, WS_END = WS_PACE + 8 * 64 * PACE_LD;
constexpr u32 WS_DBG = 8u << 20;
constexpr u32 EMPTY = 0xffffffffu;

constexpr int XN_LD = 1168;
constexpr int L_XN = 0, L_QP = L_XN + NB * XN_LD, L_QB = L_QP + 12 * DH * 4, L_PS = L_QB + DH * 2, L_WM = L_PS + SK * 2, L_WS = L_WM + 64,
              L_OP = L_WS + 64, L_OB = L_OP + 4 * DH * 4, L_TG = L_OB + 9 * DH * 2, L_PG = L_TG + 8, L_WN = L_TG + 16, L_XB = L_TG + 32, L_MO = L_XB + DM * 2, L_END = L_MO + NB * DM * 4;

struct Args { const char* x; const char* k; const char* v; const char* wq; const char* wo; const char* w1; const char* w3; const char* w2; char* out; char* ws; };

typedef __amdgpu_buffer_rsrc_t rsrc_t;
__device__ __forceinline__ rsrc_t mk(const char* p, u32 n) { return __builtin_amdgcn_make_buffer_rsrc((void*)p, 0, n, 0x00020000); }
#define LD128(r, vo, so, aux) __builtin_amdgcn_raw_buffer_load_b128(r, vo, so, aux)
#define LD64(r, vo, so, aux) __builtin_amdgcn_raw_buffer_load_b64(r, vo, so, aux)
#define LD32(r, vo, so, aux) __builtin_amdgcn_raw_buffer_load_b32(r, vo, so, aux)
#define ST128(v, r, vo, so, aux) __builtin_amdgcn_raw_buffer_store_b128(v, r, vo, so, aux)
#define ST64(v, r, vo, so, aux) __builtin_amdgcn_raw_buffer_store_b64(v, r, vo, so, aux)
#define PIN() __builtin_amdgcn_sched_barrier(0)
#define USE(x) asm volatile("" ::"v"(x) : "memory")
#define BAR() asm volatile("s_waitcnt lgkmcnt(0)\n\ts_barrier" ::: "memory")
#define RAWBAR() asm volatile("s_barrier" ::: "memory")
#define TOUCH(d, p) asm volatile("s_load_dword %0, %1, 0x0" : "=&s"(d) : "s"(p) : "memory")
#define GATE(i) asm volatile("s_cmp_gt_i32 %0, %1\n\ts_cbranch_scc1 Lg7_%=\n\ts_cmp_gt_i32 %0, %2\n\ts_cbranch_scc1 Lg5_%=\n\t" \
                             "s_cmp_gt_i32 %0, %3\n\ts_cbranch_scc1 Lg4_%=\n\ts_waitcnt vmcnt(3)\nLg4_%=:\n\ts_waitcnt vmcnt(4)\n" \
                             "Lg5_%=:\n\ts_waitcnt vmcnt(5)\nLg7_%=:" \
                             ::"s"(win), "i"(48 + DITH(i)), "i"(40 + DITH(i)), "i"(32 + DITH(i)) : "scc", "memory")
#define DITH(i) (((i) & 1) * 4 + ((i) & 2) + ((i) >> 2))
#define LGET(off) __hip_atomic_load((LAS u32*)(lds + (off)), __ATOMIC_RELAXED, __HIP_MEMORY_SCOPE_WORKGROUP)
#define LPUT(off, v) __hip_atomic_store((LAS u32*)(lds + (off)), (u32)(v), __ATOMIC_RELAXED, __HIP_MEMORY_SCOPE_WORKGROUP)
#define LWAIT() asm volatile("s_waitcnt lgkmcnt(0)" ::: "memory")
#define DRAIN() asm volatile("s_waitcnt vmcnt(0)" ::: "memory")

__device__ __forceinline__ u32 pk(float lo, float hi) { bf16x2 t = {(__bf16)lo, (__bf16)hi}; return __builtin_bit_cast(u32, t); }
__device__ __forceinline__ float rb(float x) { return __uint_as_float(pk(x, x) << 16); }
__device__ __forceinline__ float lo16(u32 v) { return __uint_as_float(v << 16); }
__device__ __forceinline__ float hi16(u32 v) { return __uint_as_float(v & 0xffff0000u); }
__device__ __forceinline__ float dot2(u32 a, u32 b, float c) {
  return __builtin_amdgcn_fdot2_f32_bf16(__builtin_bit_cast(bf16x2, a), __builtin_bit_cast(bf16x2, b), c, false);
}
__device__ __forceinline__ f32x4 mfma(u32x4 a, u32x4 b, f32x4 c) {
  return __builtin_amdgcn_mfma_f32_16x16x32_bf16(__builtin_bit_cast(bf16x8, a), __builtin_bit_cast(bf16x8, b), c, 0, 0, 0);
}
template <int CTRL> __device__ __forceinline__ float dpp(float x) {
  return __builtin_bit_cast(float, __builtin_amdgcn_mov_dpp(__builtin_bit_cast(int, x), CTRL, 0xf, 0xf, true));
}
constexpr int X1 = 0xB1, X2 = 0x4E, HMIR = 0x141, MIR = 0x140, ROR8 = 0x128;
__device__ __forceinline__ float sum16(float x) { x += dpp<X1>(x); x += dpp<X2>(x); x += dpp<HMIR>(x); x += dpp<MIR>(x); return x; }
__device__ __forceinline__ float max16(float x) { x = fmaxf(x, dpp<X1>(x)); x = fmaxf(x, dpp<X2>(x)); x = fmaxf(x, dpp<HMIR>(x)); x = fmaxf(x, dpp<MIR>(x)); return x; }
__device__ __forceinline__ float sumrows(float x) {
  auto s = __builtin_amdgcn_permlane16_swap(__float_as_uint(x), __float_as_uint(x), false, false);
  x = __uint_as_float(s[0]) + __uint_as_float(s[1]);
  auto t = __builtin_amdgcn_permlane32_swap(__float_as_uint(x), __float_as_uint(x), false, false);
  return __uint_as_float(t[0]) + __uint_as_float(t[1]);
}
__device__ __forceinline__ float maxrows(float x) {
  auto s = __builtin_amdgcn_permlane16_swap(__float_as_uint(x), __float_as_uint(x), false, false);
  x = fmaxf(__uint_as_float(s[0]), __uint_as_float(s[1]));
  auto t = __builtin_amdgcn_permlane32_swap(__float_as_uint(x), __float_as_uint(x), false, false);
  return fmaxf(__uint_as_float(t[0]), __uint_as_float(t[1]));
}
__device__ __forceinline__ float fdiv(float a, float d, float rd) { const float q = a * rd; return fmaf(fmaf(-q, d, a), rd, q); }
__device__ __forceinline__ float silu(float x) { const float d = 1.f + __expf(-x); return d < INFINITY ? fdiv(x, d, __builtin_amdgcn_rcpf(d)) : -0.f; }
__device__ __forceinline__ u64 now() { return __builtin_amdgcn_s_memrealtime(); }
constexpr u64 SPIN_CAP = 5000000;

#define TS(i) do {} while (0)
#define TSW(ww, i) do {} while (0)

struct Diag { u32 s[2][4]; };
__device__ __forceinline__ Diag make_diag(int c) {
  Diag d;
#pragma unroll
  for (int par = 0; par < 2; ++par)
#pragma unroll
    for (int w = 0; w < 4; ++w) {
      const u32 b = par ? 0x0302u : 0x0100u;
      d.s[par][w] = (c >> 1) == w ? ((c & 1) ? (b << 16) | 0x0c0cu : 0x0c0c0000u | b) : 0x0c0c0c0cu;
    }
  return d;
}
template <int PAR> __device__ __forceinline__ f32x4 axpy4(u32 val, u32x4 rows, const Diag& d, f32x4 acc) {
  if (ABL & 128) { USE(rows); return acc; }
  u32x4 af;
#pragma unroll
  for (int w = 0; w < 4; ++w) af[w] = __builtin_amdgcn_perm(val, val, d.s[PAR][w]);
  return mfma(af, rows, acc);
}
__device__ __forceinline__ float score4(u32x4 k, u32x4 q) {
  if (ABL & 128) { USE(k); return 1.f; }
  float d = dot2(k[0], q[0], 0.f); d = dot2(k[1], q[1], d); d = dot2(k[2], q[2], d); d = dot2(k[3], q[3], d);
  return sum16(d);
}
__device__ __forceinline__ void tr8(const u32x2 (&raw)[8], u32x4 (&f)[4]) {
#pragma unroll
  for (int e = 0; e < 4; ++e)
#pragma unroll
    for (int d = 0; d < 4; ++d) f[e][d] = __builtin_amdgcn_perm(raw[2 * d + 1][e >> 1], raw[2 * d][e >> 1], (e & 1) ? 0x07060302u : 0x05040100u);
}

__global__ void __launch_bounds__(1024) fused_decode(Args a) {
  __shared__ __attribute__((aligned(16))) char lds_[L_END];
  LAS char* lds = (LAS char*)lds_;
  if (CODE_KB && __builtin_amdgcn_readfirstlane(threadIdx.x >> 6) == 11 && (blockIdx.x >> 3) < CODE_KB) {
    u64 p;
    u32 c0, c1, c2, c3, c4, c5, c6, c7;
    asm volatile("s_getpc_b64 %0" : "=s"(p));
    p = (p & ~255ull) + (blockIdx.x >> 3) * 1024;
    asm volatile("s_load_dword %0, %8, 0x0\n\ts_load_dword %1, %8, 0x80\n\ts_load_dword %2, %8, 0x100\n\ts_load_dword %3, %8, 0x180\n\t"
                 "s_load_dword %4, %8, 0x200\n\ts_load_dword %5, %8, 0x280\n\ts_load_dword %6, %8, 0x300\n\ts_load_dword %7, %8, 0x380\n\ts_waitcnt lgkmcnt(0)"
                 : "=&s"(c0), "=&s"(c1), "=&s"(c2), "=&s"(c3), "=&s"(c4), "=&s"(c5), "=&s"(c6), "=&s"(c7) : "s"(p) : "memory");
  }
  asm volatile("" ::"s"(a.x), "s"(a.ws));
  const int tid = threadIdx.x, lane = tid & 63, w = __builtin_amdgcn_readfirstlane(tid >> 6), r = lane >> 4, c = lane & 15;
  const int blk = blockIdx.x, b = blk >> 5, h = blk & 31;
  const int hrot = (h - b) & 31, task = (hrot < 9 && !(ABL & 2)) ? b * 9 + hrot : -1;
  const char* kbase = a.k + ((size_t)blk << 20);
  const char* vbase = a.v + ((size_t)blk << 20);
  const rsrc_t rk = mk(kbase, 1u << 20);
  const int sw = w & 3;
  const int vA = lane * 16 + sw * 4096, vB = vA + 16384;
#define KVLD_(rs, beta, i, AUX) LD128(rs, ((i) < 4 ? vA : vB) + ((i) & 3) * 1024, (beta) * 32768, AUX)
#define KVLD(rs, beta, i) KVLD_(rs, beta, i, KAUX)
#define VLD(rs, beta, i) KVLD_(rs, beta, i, VAUX)
  float mx = -INFINITY;
  auto put_scores = [&](float sc, int batch) {
    const float s = rb(rb(sc) * 0.08838834764831845f);
    mx = fmaxf(mx, s);
    if (c < 8) *(LAS u16*)(lds + L_PS + batch * 64 + r * 16 + c * 2) = (u16)(__float_as_uint(s) >> 16);
  };
  auto softmax = [&]() {
    const float wm = maxrows(max16(mx));
    if (lane == 0) *(LAS float*)(lds + L_WM + w * 4) = wm;
    BAR();
    const float M = max16(*(LAS float*)(lds + L_WM + c * 4));
    const u32x2 sv = *(LAS u32x2*)(lds + L_PS + tid * 8);
    const float e0 = __expf(lo16(sv[0]) - M), e1 = __expf(hi16(sv[0]) - M), e2 = __expf(lo16(sv[1]) - M), e3 = __expf(hi16(sv[1]) - M);
    const float ws = sumrows(sum16((e0 + e1) + (e2 + e3)));
    if (lane == 0) *(LAS float*)(lds + L_WS + w * 4) = ws;
    BAR();
    const float inv = 1.f / sum16(*(LAS float*)(lds + L_WS + c * 4));
    *(LAS u32x2*)(lds + L_PS + tid * 8) = u32x2{pk(e0 * inv, e1 * inv), pk(e2 * inv, e3 * inv)};
    BAR();
  };

  if (w >= 12) {
    TSW(12, 0);
    u32x4 ring[8];
    RAWBAR();
#pragma unroll
    for (int i = 0; i < 7; ++i) { ring[i] = KVLD(rk, 0, i); PIN(); }
    __builtin_amdgcn_s_setprio(2);
    RAWBAR(); RAWBAR();
    TSW(12, 1);
    const u32x4 q = *(LAS u32x4*)(lds + L_QB + c * 16);
    float sc = 0.f;
    const int win0 = WIN0 + (task >= 0 ? WIN_MLP : 0);
    int win = win0;
#define PACE_IN() const u32 tj = (u32)now(); const u64 sch = __hip_atomic_load((LAS u64*)(lds + L_WN), __ATOMIC_RELAXED, __HIP_MEMORY_SCOPE_WORKGROUP)
#define PACE_OUT(j) do { if (PACE) { const u32 due_ = __builtin_amdgcn_readfirstlane((u32)sch), tau_ = __builtin_amdgcn_readfirstlane((u32)(sch >> 32)); \
      const int late_ = min(max((int)(tj * 256u - due_ - tau_ * (u32)(j)) >> 8, -4096), 4096); \
      win = tau_ ? min(max(WMID + ((late_ * GAIN) >> 8), WLO), WHI) : win0; } } while (0)
#pragma unroll 1
    for (int beta = 0; beta < 32; ++beta) {
      const rsrc_t rn = mk(beta < 31 ? kbase : vbase, 1u << 20);
      const int bn = beta < 31 ? beta + 1 : 0;
      if (beta == 31) win = 64;
      PACE_IN();
      if (tid == 768) LPUT(L_PG, beta);
#define KSTEP(i) { GATE(i); if (i == 0) ring[7] = KVLD(rk, beta, 7); else ring[(i + 7) & 7] = KVLD(rn, bn, (i + 7) & 7); PIN(); \
        const float d = score4(ring[i], q); sc = (c & 7) == i ? d : sc; PIN(); }
      KSTEP(0) KSTEP(1) KSTEP(2) KSTEP(3) KSTEP(4) KSTEP(5) KSTEP(6) KSTEP(7)
      put_scores(sc, beta * 4 + sw);
      PACE_OUT(beta);
    }
    if (!PACE) win = win0;
    const rsrc_t rv = mk(vbase, 1u << 20);
    u32x4 ext[8];
    ring[7] = VLD(rv, 0, 7);
#pragma unroll
    for (int i = 0; i < 8; ++i) { ext[i] = VLD(rv, 1, i); PIN(); }
    TSW(12, 2);
    softmax();
    TSW(12, 3);
    const Diag dg = make_diag(c);
    f32x4 acc = {0.f, 0.f, 0.f, 0.f};
    const int po = L_PS + sw * 64 + r * 16;
    u32x4 pn = *(LAS u32x4*)(lds + po + 512);
    {
      const u32x4 p0 = *(LAS u32x4*)(lds + po), p1 = *(LAS u32x4*)(lds + po + 256);
#pragma unroll
      for (int i = 0; i < 8; ++i) { acc = (i & 1) ? axpy4<1>(p0[i >> 1], ring[i], dg, acc) : axpy4<0>(p0[i >> 1], ring[i], dg, acc); PIN(); }
#define ESTEP(i) { if (i > 0) { GATE(i); ring[(i + 7) & 7] = VLD(rv, 2, (i + 7) & 7); } PIN(); acc = axpy4<i & 1>(p1[i >> 1], ext[i], dg, acc); PIN(); }
      ESTEP(0) ESTEP(1) ESTEP(2) ESTEP(3) ESTEP(4) ESTEP(5) ESTEP(6) ESTEP(7)
    }
#pragma unroll 1
    for (int beta = 2; beta < 32; ++beta) {
      const rsrc_t rn = mk(vbase, beta < 31 ? 1u << 20 : 0u);
      const int bn = beta < 31 ? beta + 1 : 0;
      if (beta == 31) win = 64;
      const u32x4 pv = pn;
      pn = *(LAS u32x4*)(lds + po + bn * 256);
      PACE_IN();
      if (tid == 768) LPUT(L_PG, 32 + beta);
#define VSTEP(i) { GATE(i); if (i == 0) ring[7] = VLD(rv, beta, 7); else ring[(i + 7) & 7] = VLD(rn, bn, (i + 7) & 7); PIN(); \
        acc = axpy4<i & 1>(pv[i >> 1], ring[i], dg, acc); PIN(); }
      VSTEP(0) VSTEP(1) VSTEP(2) VSTEP(3) VSTEP(4) VSTEP(5) VSTEP(6) VSTEP(7)
      PACE_OUT(32 + beta);
    }
    if (r < 2) *(LAS f32x4*)(lds + L_OP + sw * 512 + c * 32 + r * 16) = acc;
    TSW(12, 4);
    BAR();
    return;
  }

  const rsrc_t rws = mk(a.ws, WS_END);
  auto wait_batch = [&](u32 n) { while (LGET(L_PG) < n) __builtin_amdgcn_s_sleep(2); };
  if (w == 11) {
    u32 t0, t1, t2, t3, t4, t5, t6, t7;
    asm volatile("" ::"s"(a.ws), "s"(a.out), "s"(kbase), "s"(vbase), "s"(a.w2));
    TOUCH(t0, kbase); TOUCH(t1, a.ws);
    RAWBAR();
    TOUCH(t2, a.wo + (size_t)h * (DH * DM * 2)); TOUCH(t3, a.w1); TOUCH(t4, a.w3); TOUCH(t5, vbase); TOUCH(t6, a.w2); TOUCH(t7, a.out);
    const u32x2 ep = LD64(rws, WS_EPOCH, 0, AUX_HO);
    if (lane < 48) {
#pragma unroll
      for (int i = 0; i < 3; ++i) ST128((u32x4{EMPTY, EMPTY, EMPTY, EMPTY}), rws, WS_ATT + blk * ATT_LD + lane * 16 + i * 768, 0, AUX_CL);
    }
    RAWBAR(); RAWBAR();
    asm volatile("s_waitcnt lgkmcnt(0)" : "+s"(t0), "+s"(t1), "+s"(t2), "+s"(t3), "+s"(t4), "+s"(t5), "+s"(t6), "+s"(t7)::"memory");
    const u64 tag = (((u64)(u32)__builtin_amdgcn_readfirstlane(ep[1]) << 32) | (u32)__builtin_amdgcn_readfirstlane(ep[0])) + 1;
    DRAIN();
    if (lane == 0) {
      *(LAS u64*)(lds + L_TG) = tag;
      ST64((u32x2{(u32)tag, (u32)(tag >> 32)}), rws, WS_ATT + blk * ATT_LD + DM * 4, 0, AUX_FL);
    }
    TS(17);
    const u32 tb = (u32)now();
    const bool sample = ((blk >> 3) & 3) == (blk & 3);
    float mprev = 0.f;
    int atprev = 0;
    auto pace = [&](int k, int at) {
      wait_batch(at);
      const u32 tk = (u32)now();
      if (sample && lane < 8) ST128((u32x4{tk, (u32)tag, tk, (u32)tag}), rws, WS_PACE + (k * 64 + b * 8 + (blk & 7)) * PACE_LD + lane * 16, 0, AUX_PS);
      wait_batch(at + DLY);
      const u32x2 v = LD64(rws, WS_PACE + (k * 64 + ((b + lane + blk) & 7) * 8 + (lane & 7)) * PACE_LD, 0, AUX_HO);
      const bool in = v[1] == (u32)tag && lane < 8;
      const float n = (float)__popcll(__ballot(in));
      const float m = (float)(int)(tk - tb) + sum16(in ? (float)(int)(v[0] - tk) : 0.f) * __builtin_amdgcn_rcpf(fmaxf(n, 1.f));
      if (k > 0 && lane == 0) {
        const float tau = (m - mprev) * __builtin_amdgcn_rcpf((float)(at - atprev)) * (1.f - EPS);
        const u32 tau256 = (u32)fminf(fmaxf(tau * 256.f, 1.f), 1e6f);
        __hip_atomic_store((LAS u64*)(lds + L_WN), ((u64)tau256 << 32) | (tb * 256u + (u32)(int)(m * 256.f) - tau256 * (u32)at), __ATOMIC_RELAXED, __HIP_MEMORY_SCOPE_WORKGROUP);
      }
      mprev = m; atprev = at;
    };
    if (PACE) {
#pragma unroll 1
      for (int k = 0; k < 4; ++k) pace(k, (CPK >> (8 * k)) & 255);
    }
    softmax();
    if (PACE) {
#pragma unroll 1
      for (int k = 0; k < 4; ++k) pace(4 + k, (CPV >> (8 * k)) & 255);
    }
    BAR();
    return;
  }
  if (w < 9) {
    const int wr = ROT ? (w + b) % 9 : w;
    u32x4 wb[16];
    {
      const rsrc_t rq = mk(a.wq + h * 256, DM * 8192 - h * 256);
      const int vo = (64 * wr + 16 * r) * 8192 + c * 16;
#pragma unroll
      for (int i = 0; i < 16; ++i) wb[i] = (ABL & 32) ? u32x4{1u,1u,1u,1u} : LD128(rq, vo, i * 8192, QAUX);
    }
    PIN();
    TSW(0, 16);
    const Diag dgq = make_diag(c);
    RAWBAR();
    TSW(0, 21);
    {
      const int xo = L_XB + (64 * wr + 16 * r) * 2;
      const u32x4 x0 = *(LAS u32x4*)(lds + xo), x1 = *(LAS u32x4*)(lds + xo + 16);
      f32x4 acc0 = {0.f, 0.f, 0.f, 0.f}, acc1 = acc0;
#pragma unroll
      for (int i = 0; i < 8; ++i) {
        const u32 xv = i < 4 ? x0[i & 3] : x1[i & 3];
        acc0 = axpy4<0>(xv, wb[2 * i], dgq, acc0); acc1 = axpy4<1>(xv, wb[2 * i + 1], dgq, acc1);
      }
      if (r < 2) *(LAS f32x4*)(lds + L_QP + wr * 512 + c * 32 + r * 16) = acc0 + acc1;
    }
    TSW(0, 22); TSW(8, 23);
    BAR();
    TSW(0, 24);
    if (w < 2) {
      float s = 0.f;
#pragma unroll
      for (int j = 0; j < 9; ++j) s += *(LAS float*)(lds + L_QP + j * 512 + tid * 4);
      *(LAS u16*)(lds + L_QB + tid * 2) = (u16)pk(s, s);
    }
    BAR();
  } else {
    __builtin_amdgcn_s_setprio(3);
    if (w == 10) {
      const rsrc_t rx = mk(a.x + b * (DM * 2), DM * 2);
      u32 xr[5];
#pragma unroll
      for (int i = 0; i < 5; ++i) xr[i] = LD32(rx, lane * 4 + i * 256, 0, NT);
      if (lane == 0) { LPUT(L_PG, 0); LPUT(L_WN, 0); LPUT(L_WN + 4, 0); }
      float s = 0.f;
#pragma unroll
      for (int i = 0; i < 5; ++i) s += lo16(xr[i]) + hi16(xr[i]);
      const float mean = sumrows(sum16(s)) * (1.f / DM);
      float ss = 0.f;
#pragma unroll
      for (int i = 0; i < 5; ++i) {
        const float d0 = lo16(xr[i]) - mean, d1 = hi16(xr[i]) - mean;
        ss += (i < 4 || lane < 32) ? d0 * d0 + d1 * d1 : 0.f;
      }
      const float var = sumrows(sum16(ss)) * (1.f / DM);
      const float m = rb(mean), den = rb(__builtin_amdgcn_sqrtf(rb(rb(var) + 1e-5f))), rd = __builtin_amdgcn_rcpf(den);
#pragma unroll
      for (int i = 0; i < 5; ++i)
        if (i < 4 || lane < 32) *(LAS u32*)(lds + L_XB + lane * 4 + i * 256) = pk(fdiv(rb(lo16(xr[i]) - m), den, rd), fdiv(rb(hi16(xr[i]) - m), den, rd));
    }
    TSW(10, 19);
    BAR();
    __builtin_amdgcn_s_setprio(0);
    RAWBAR(); RAWBAR();
  }
  PIN();

  if (w < 9) {
    u32x4 wf[4][4];
    const rsrc_t ro = mk(a.wo + (size_t)h * (DH * DM * 2), DH * DM * 2);
    const int vo = r * 8 * (DM * 2) + w * 128 + c * 8;
    u32x2 raw[4][8];
#pragma unroll
    for (int n = 0; n < 32; ++n) {
      raw[n >> 3][n & 7] = (ABL & 8) ? u32x2{1u,1u} : LD64(ro, vo, ((n >> 3) * 32 + (n & 7)) * (DM * 2), OAUX);
      if (n >= WOD) USE(raw[(n - WOD) >> 3][(n - WOD) & 7]);
      if (n >= WOD + 7 && ((n - WOD) & 7) == 7) tr8(raw[(n - WOD) >> 3], wf[(n - WOD) >> 3]);
      PIN();
    }
    tr8(raw[3], wf[3]);
    TSW(0, 8);
    PIN();
    softmax();
    BAR();
    TSW(0, 10);
    float o0 = 0.f, o1 = 0.f;
#pragma unroll
    for (int j = 0; j < 4; ++j) { const u32x2 t = *(LAS u32x2*)(lds + L_OP + j * 512 + lane * 8); o0 += __uint_as_float(t[0]); o1 += __uint_as_float(t[1]); }
    *(LAS u32*)(lds + L_OB + w * 256 + lane * 4) = pk(o0, o1);
    LWAIT();
    TSW(0, 25);
    f32x4 d[4];
#pragma unroll
    for (int e = 0; e < 4; ++e) d[e] = f32x4{0.f, 0.f, 0.f, 0.f};
#pragma unroll
    for (int s = 0; s < 4; ++s) {
      const u32x4 ob = *(LAS u32x4*)(lds + L_OB + w * 256 + s * 64 + r * 16);
#pragma unroll
      for (int e = 0; e < 4; ++e) d[e] = mfma(wf[s][e], ob, d[e]);
    }
    u32x4 o;
#pragma unroll
    for (int e = 0; e < 4; ++e) {
      const float t = (c & 2) ? ((c & 1) ? d[e][3] : d[e][2]) : ((c & 1) ? d[e][1] : d[e][0]);
      o[e] = min(__float_as_uint(t), EMPTY - 1);
    }
    if (c < 4) ST128(o, rws, WS_ATT + blk * ATT_LD + w * 256 + r * 64 + c * 16, 0, AUX_AS);
    TSW(0, 11); TSW(8, 26);
    return;
  }
  if (w == 9) {
    u32x4 hf[2] = {};
    if (task >= 0) {
      {
        const rsrc_t rx = mk(a.x, NB * DM * 2);
        u32x4 xa[9];
#pragma unroll
        for (int i = 0; i < 9; ++i) xa[i] = LD128(rx, (lane >> 3) * (DM * 2) + (lane & 7) * 16, i * 128, NT);
        float s = 0.f;
#pragma unroll
        for (int i = 0; i < 9; ++i)
#pragma unroll
          for (int j = 0; j < 4; ++j) s += lo16(xa[i][j]) + hi16(xa[i][j]);
        s += dpp<X1>(s); s += dpp<X2>(s); s += dpp<HMIR>(s);
        const float mean = s * (1.f / DM);
        float ss = 0.f;
#pragma unroll
        for (int i = 0; i < 9; ++i)
#pragma unroll
          for (int j = 0; j < 4; ++j) { const float d0 = lo16(xa[i][j]) - mean, d1 = hi16(xa[i][j]) - mean; ss += d0 * d0 + d1 * d1; }
        ss += dpp<X1>(ss); ss += dpp<X2>(ss); ss += dpp<HMIR>(ss);
        const float m = rb(mean), den = rb(__builtin_amdgcn_sqrtf(rb(rb(ss * (1.f / DM)) + 1e-5f))), rd = __builtin_amdgcn_rcpf(den);
#pragma unroll
        for (int i = 0; i < 9; ++i) {
          u32x4 o;
#pragma unroll
          for (int j = 0; j < 4; ++j) o[j] = pk(fdiv(rb(lo16(xa[i][j]) - m), den, rd), fdiv(rb(hi16(xa[i][j]) - m), den, rd));
          *(LAS u32x4*)(lds + L_XN + (lane >> 3) * XN_LD + (lane & 7) * 16 + i * 128) = o;
        }
        LWAIT();
        PIN();
      }
      const rsrc_t r1 = mk(a.w1 + task * 128, DM * FF * 2 - task * 128), r3 = mk(a.w3 + task * 128, DM * FF * 2 - task * 128);
      const int vo = r * 8 * (FF * 2) + c * 8;
      const int xo = L_XN + (c & 7) * XN_LD + r * 16;
      u32x2 ra[8], rc[8];
      f32x4 g[4], u[4];
#pragma unroll
      for (int e = 0; e < 4; ++e) { g[e] = f32x4{0.f, 0.f, 0.f, 0.f}; u[e] = g[e]; }
#pragma unroll
      for (int i = 0; i < 8; ++i) ra[i] = LD64(r1, vo, i * (FF * 2), WAUX);
#pragma unroll 1
      for (int s = 0; s < 18; ++s) {
        const int sc_ = s * 32 * (FF * 2), sn = (s < 17 ? s + 1 : 17) * 32 * (FF * 2);
        const rsrc_t n1 = mk(a.w1 + task * 128, s < 17 ? DM * FF * 2 - task * 128 : 0);
        const u32x4 xb = *(LAS u32x4*)(lds + xo + s * 64);
        u32x4 f[4];
#pragma unroll
        for (int i = 0; i < 8; ++i) { USE(ra[i]); rc[i] = LD64(r3, vo, sc_ + i * (FF * 2), WAUX); PIN(); }
        tr8(ra, f);
#pragma unroll
        for (int e = 0; e < 4; ++e) g[e] = mfma(f[e], xb, g[e]);
        PIN();
#pragma unroll
        for (int i = 0; i < 8; ++i) { USE(rc[i]); ra[i] = LD64(n1, vo, sn + i * (FF * 2), WAUX); PIN(); }
        tr8(rc, f);
#pragma unroll
        for (int e = 0; e < 4; ++e) u[e] = mfma(f[e], xb, u[e]);
        PIN();
      }
      float hv[4][4];
#pragma unroll
      for (int p = 0; p < 4; ++p)
#pragma unroll
        for (int e = 0; e < 4; ++e) hv[p][e] = rb(silu(rb(g[e][p]))) * rb(u[e][p]);
#pragma unroll
      for (int s = 0; s < 2; ++s) hf[s] = u32x4{pk(hv[2 * s][0], hv[2 * s][1]), pk(hv[2 * s][2], hv[2 * s][3]), pk(hv[2 * s + 1][0], hv[2 * s + 1][1]), pk(hv[2 * s + 1][2], hv[2 * s + 1][3])};
      TS(9);
    }
    PIN();
    softmax();
    if (task >= 0) {
      const rsrc_t r2 = mk(a.w2 + (size_t)task * (64 * DM * 2), 64 * DM * 2);
      const int vo = r * 16 * (DM * 2) + c * 8;
      u32x2 ra[8], rc[8];
#pragma unroll
      for (int i = 0; i < 8; ++i) ra[i] = LD64(r2, vo, i * (DM * 2), WAUX);
      const int so = L_MO + (c & 7) * (DM * 4) + r * 64 + (c >> 3) * 32;
#pragma unroll 1
      for (int W = 0; W < 9; ++W) {
        const rsrc_t n2 = mk(a.w2 + (size_t)task * (64 * DM * 2), W < 8 ? 64 * DM * 2 : 0);
        const int sn = (W < 8 ? W + 1 : 8) * 128;
        u32x4 f[4];
        f32x4 d[4];
#pragma unroll
        for (int i = 0; i < 8; ++i) { USE(ra[i]); rc[i] = LD64(r2, vo, W * 128 + (8 + i) * (DM * 2), WAUX); PIN(); }
        tr8(ra, f);
#pragma unroll
        for (int e = 0; e < 4; ++e) d[e] = mfma(f[e], hf[0], f32x4{0.f, 0.f, 0.f, 0.f});
        PIN();
#pragma unroll
        for (int i = 0; i < 8; ++i) { USE(rc[i]); ra[i] = LD64(n2, vo, sn + i * (DM * 2), WAUX); PIN(); }
        tr8(rc, f);
#pragma unroll
        for (int e = 0; e < 4; ++e) d[e] = mfma(f[e], hf[1], d[e]);
#pragma unroll
        for (int p = 0; p < 2; ++p) {
          u32x4 o;
#pragma unroll
          for (int e = 0; e < 4; ++e) o[e] = __float_as_uint((c & 8) ? d[e][2 + p] : d[e][p]);
          *(LAS u32x4*)(lds + so + p * 16 + W * 256) = o;
        }
        PIN();
      }
      LWAIT();
      TS(15);
      if (ALIGN) { const u64 t = now() | (ALIGN - 1); while (now() <= t) __builtin_amdgcn_s_sleep(2); }
      if (!(ABL & 4)) {
#pragma unroll
        for (int i = 0; i < 18; ++i) ST128(*(LAS u32x4*)(lds + L_MO + lane * 16 + i * 1024), rws, WS_MLP + task * MLP_LD + lane * 16, i * 1024, AUX_MS);
      }
      DRAIN();
      const u32x2 tg = *(LAS u32x2*)(lds + L_TG);
      if (lane == 0) ST64(tg, rws, WS_MLP + task * MLP_LD + NB * DM * 4, 0, AUX_FL);
      TS(12);
    }
    BAR();
    return;
  }
  PIN();
  softmax();
  if (h < NCONS) {
    bool ok = true;
    const u32x2 tg = *(LAS u32x2*)(lds + L_TG);
    const u32 t0 = tg[0], t1 = tg[1];
    const int col = h * 32 + (lane & 7) * 4;
    wait_batch(PRE_AT);
    const u32x2 xv = LD64(mk(a.x, NB * DM * 2), (b * DM + col) * 2, 0, NT);
    const u64 start = now();
    for (;;) {
      const u32x2 f1 = LD64(rws, WS_ATT + DM * 4 + (b * NH + (lane & 31)) * ATT_LD, 0, AUX_HO);
      const u32x2 f2 = LD64(rws, WS_MLP + NB * DM * 4 + lane * MLP_LD, 0, AUX_HO), f3 = LD64(rws, WS_MLP + NB * DM * 4 + (64 + (lane & 7)) * MLP_LD, 0, AUX_HO);
      bool all = f1[0] == t0 && f1[1] == t1 && f2[0] == t0 && f2[1] == t1 && f3[0] == t0 && f3[1] == t1;
      if (blk == 0) {
#pragma unroll
        for (int i = 0; i < 4; ++i) { const u32x2 f = LD64(rws, WS_ATT + DM * 4 + lane * ATT_LD, i * 64 * ATT_LD, AUX_HO); all &= f[0] == t0 && f[1] == t1; }
      }
      if (__all(all) || (ABL & 2)) break;
      if (now() - start > SPIN_CAP) { ok = false; break; }
      __builtin_amdgcn_s_sleep(16);
    }
    asm volatile("" ::: "memory");
    if (blk == 0 && lane == 0) ST64((u32x2{t0, t1}), rws, WS_EPOCH, 0, SC1);
    float f[4] = {0.f, 0.f, 0.f, 0.f};
    {
      u32x4 t[9];
#pragma unroll
      for (int i = 0; i < 9; ++i) t[i] = LD128(rws, WS_MLP + (lane >> 3) * MLP_LD + b * (DM * 4) + col * 4, i * 8 * MLP_LD, AUX_HO);
#pragma unroll
      for (int i = 0; i < 9; ++i)
#pragma unroll
        for (int e = 0; e < 4; ++e) f[e] += __uint_as_float(t[i][e]);
    }
    float g[4];
#pragma unroll
    for (int e = 0; e < 4; ++e) { float t = f[e]; t += dpp<ROR8>(t); g[e] = rb(silu(rb(sumrows(t)))); }
    TS(13);
    BAR();
    u32x4 t[4], u[4];
    const int ao = WS_ATT + (b * NH + (lane >> 3)) * ATT_LD + col * 4;
#define SWEEP(t) _Pragma("unroll") for (int i = 0; i < 4; ++i) t[i] = LD128(rws, ao, i * 8 * ATT_LD, AUX_HO)
#define FULL(t) __all(min(min(min(~t[0][0], ~t[0][1]), min(~t[0][2], ~t[0][3])), min(min(min(~t[1][0], ~t[1][1]), min(~t[1][2], ~t[1][3])), \
                min(min(min(~t[2][0], ~t[2][1]), min(~t[2][2], ~t[2][3])), min(min(~t[3][0], ~t[3][1]), min(~t[3][2], ~t[3][3]))))) != 0u)
    const u64 start2 = now();
    TS(7);
    SWEEP(t);
    for (;;) {
      asm volatile("" ::: "memory");
      SWEEP(u);
      PIN();
      if (FULL(t)) break;
      asm volatile("" ::: "memory");
      SWEEP(t);
      PIN();
      if (FULL(u)) {
#pragma unroll
        for (int i = 0; i < 4; ++i) t[i] = u[i];
        break;
      }
      if (!ok || now() - start2 > SPIN_CAP) { ok = false; break; }
    }
    float y[4];
#pragma unroll
    for (int e = 0; e < 4; ++e) {
      float s = (__uint_as_float(t[0][e]) + __uint_as_float(t[1][e])) + (__uint_as_float(t[2][e]) + __uint_as_float(t[3][e]));
      s += dpp<ROR8>(s);
      s = rb(sumrows(s));
      const float xe = (e & 1) ? hi16(xv[e >> 1]) : lo16(xv[e >> 1]);
      y[e] = ok ? xe + rb(s * g[e]) : __uint_as_float(0x7fc00000u);
    }
    if (lane < 8) ST64((u32x2{pk(y[0], y[1]), pk(y[2], y[3])}), mk(a.out, NB * DM * 2), (b * DM + col) * 2, 0, 0);
    TS(14);
  } else {
    BAR();
  }
}

}

extern "C" void kernel_launch(void* const* d_in, const int* in_sizes, int n_in, void* d_out, int out_size, void* d_ws, size_t ws_size,
                              hipStream_t stream) {
  Args a;
  a.x = (const char*)d_in[0]; a.k = (const char*)d_in[1]; a.v = (const char*)d_in[2]; a.wq = (const char*)d_in[3]; a.wo = (const char*)d_in[4];
  a.w1 = (const char*)d_in[6]; a.w3 = (const char*)d_in[7]; a.w2 = (const char*)d_in[8];
  a.out = (char*)d_out; a.ws = (char*)d_ws;
  hipLaunchKernelGGL(fused_decode, dim3(NB * NH), dim3(1024), 0, stream, a);
}
```

```cpp
#include <hip/hip_runtime.h>
#include <hip/hip_bf16.h>
#include <cstdint>

#define WIN0 41
#define WIN_MLP 2
#define PACE 0
#define GAIN 20
#define LAG0 0.f
#define WLO 34
#define WHI 56
#define ALIGN 512
#define WOD 2
#define PRE_AT 58
#define AUX_MS 18
#define AUX_CL 16
#define AUX_AS 16
#define HINT 0
#define DLY 3
#define CODE_KB 0
#define WMID 45
#define EPS 0.01f
#define PGRP 0
#define PRD 0
#define PSTR 8
#define AUX_PS 18
#define CPK 0x19110903
#define CPV 0x39332b23
#define AUX_FL 16
#define REP 1
#define ABL 0
#define KAUX 2
#define VAUX 2
#define QAUX 2
#define OAUX 2
#define WAUX 2
#define AUX_HO 16
#define WARM 1
#define ROT 1

namespace {

typedef unsigned u32;
typedef unsigned long long u64;
typedef unsigned short u16;
typedef u32 u32x4 __attribute__((ext_vector_type(4)));
typedef u32 u32x2 __attribute__((ext_vector_type(2)));
typedef float f32x4 __attribute__((ext_vector_type(4)));
typedef __bf16 bf16x8 __attribute__((ext_vector_type(8)));
typedef __bf16 bf16x2 __attribute__((ext_vector_type(2)));
#define GAS __attribute__((address_space(1)))
#define LAS __attribute__((address_space(3)))

constexpr int NB = 8, DM = 576, NH = 32, SK = 4096, DH = 128, FF = 4608;
constexpr int NTASK = FF / 64;
constexpr int NCONS = DM / 32;
constexpr int NT = 2, SC1 = 16;

constexpr u32 ATT_LD = DM * 4 + 128, MLP_LD = NB * DM * 4 + 128;
constexpr u32 PACE_LD = 4352;
constexpr u32 WS_EPOCH = 0, WS_ATT = 4096, WS_MLP = WS_ATT + NB * NH * ATT_LD, WS_PACE = WS_MLP + NTASK * MLP_LD, WS_END = WS_PACE + 8 * 64 * PACE_LD;
constexpr u32 WS_DBG = 8u << 20;
constexpr u32 EMPTY = 0xffffffffu;

constexpr int XN_LD = 1168;
constexpr int L_XN = 0, L_QP = L_XN + NB * XN_LD, L_QB = L_QP + 12 * DH * 4, L_PS = L_QB + DH * 2, L_WM = L_PS + SK * 2, L_WS = L_WM + 64,
              L_OP = L_WS + 64, L_OB = L_OP + 4 * DH * 4, L_TG = L_OB + 9 * DH * 2, L_PG = L_TG + 8, L_WN = L_TG + 16, L_XB = L_TG + 32, L_MO = L_XB + DM * 2, L_END = L_MO + NB * DM * 4;

struct Args { const char* x; const char* k; const char* v; const char* wq; const char* wo; const char* w1; const char* w3; const char* w2; char* out; char* ws; };

typedef __amdgpu_buffer_rsrc_t rsrc_t;
__device__ __forceinline__ rsrc_t mk(const char* p, u32 n) { return __builtin_amdgcn_make_buffer_rsrc((void*)p, 0, n, 0x00020000); }
#define LD128(r, vo, so, aux) __builtin_amdgcn_raw_buffer_load_b128(r, vo, so, aux)
#define LD64(r, vo, so, aux) __builtin_amdgcn_raw_buffer_load_b64(r, vo, so, aux)
#define LD32(r, vo, so, aux) __builtin_amdgcn_raw_buffer_load_b32(r, vo, so, aux)
#define ST128(v, r, vo, so, aux) __builtin_amdgcn_raw_buffer_store_b128(v, r, vo, so, aux)
#define ST64(v, r, vo, so, aux) __builtin_amdgcn_raw_buffer_store_b64(v, r, vo, so, aux)
#define PIN() __builtin_amdgcn_sched_barrier(0)
#define USE(x) asm volatile("" ::"v"(x) : "memory")
#define BAR() asm volatile("s_waitcnt lgkmcnt(0)\n\ts_barrier" ::: "memory")
#define RAWBAR() asm volatile("s_barrier" ::: "memory")
#define TOUCH(d, p) asm volatile("s_load_dword %0, %1, 0x0" : "=&s"(d) : "s"(p) : "memory")
#define GATE(i) asm volatile("s_cmp_gt_i32 %0, %1\n\ts_cbranch_scc1 Lg7_%=\n\ts_cmp_gt_i32 %0, %2\n\ts_cbranch_scc1 Lg5_%=\n\t" \
                             "s_cmp_gt_i32 %0, %3\n\ts_cbranch_scc1 Lg4_%=\n\ts_waitcnt vmcnt(3)\nLg4_%=:\n\ts_waitcnt vmcnt(4)\n" \
                             "Lg5_%=:\n\ts_waitcnt vmcnt(5)\nLg7_%=:" \
                             ::"s"(win), "i"(48 + DITH(i)), "i"(40 + DITH(i)), "i"(32 + DITH(i)) : "scc", "memory")
#define DITH(i) (((i) & 1) * 4 + ((i) & 2) + ((i) >> 2))
#define LGET(off) __hip_atomic_load((LAS u32*)(lds + (off)), __ATOMIC_RELAXED, __HIP_MEMORY_SCOPE_WORKGROUP)
#define LPUT(off, v) __hip_atomic_store((LAS u32*)(lds + (off)), (u32)(v), __ATOMIC_RELAXED, __HIP_MEMORY_SCOPE_WORKGROUP)
#define LWAIT() asm volatile("s_waitcnt lgkmcnt(0)" ::: "memory")
#define DRAIN() asm volatile("s_waitcnt vmcnt(0)" ::: "memory")

__device__ __forceinline__ u32 pk(float lo, float hi) { bf16x2 t = {(__bf16)lo, (__bf16)hi}; return __builtin_bit_cast(u32, t); }
__device__ __forceinline__ float rb(float x) { return __uint_as_float(pk(x, x) << 16); }
__device__ __forceinline__ float lo16(u32 v) { return __uint_as_float(v << 16); }
__device__ __forceinline__ float hi16(u32 v) { return __uint_as_float(v & 0xffff0000u); }
__device__ __forceinline__ float dot2(u32 a, u32 b, float c) {
  return __builtin_amdgcn_fdot2_f32_bf16(__builtin_bit_cast(bf16x2, a), __builtin_bit_cast(bf16x2, b), c, false);
}
__device__ __forceinline__ f32x4 mfma(u32x4 a, u32x4 b, f32x4 c) {
  return __builtin_amdgcn_mfma_f32_16x16x32_bf16(__builtin_bit_cast(bf16x8, a), __builtin_bit_cast(bf16x8, b), c, 0, 0, 0);
}
template <int CTRL> __device__ __forceinline__ float dpp(float x) {
  return __builtin_bit_cast(float, __builtin_amdgcn_mov_dpp(__builtin_bit_cast(int, x), CTRL, 0xf, 0xf, true));
}
constexpr int X1 = 0xB1, X2 = 0x4E, HMIR = 0x141, MIR = 0x140, ROR8 = 0x128;
__device__ __forceinline__ float sum16(float x) { x += dpp<X1>(x); x += dpp<X2>(x); x += dpp<HMIR>(x); x += dpp<MIR>(x); return x; }
__device__ __forceinline__ float max16(float x) { x = fmaxf(x, dpp<X1>(x)); x = fmaxf(x, dpp<X2>(x)); x = fmaxf(x, dpp<HMIR>(x)); x = fmaxf(x, dpp<MIR>(x)); return x; }
__device__ __forceinline__ float sumrows(float x) {
  auto s = __builtin_amdgcn_permlane16_swap(__float_as_uint(x), __float_as_uint(x), false, false);
  x = __uint_as_float(s[0]) + __uint_as_float(s[1]);
  auto t = __builtin_amdgcn_permlane32_swap(__float_as_uint(x), __float_as_uint(x), false, false);
  return __uint_as_float(t[0]) + __uint_as_float(t[1]);
}
__device__ __forceinline__ float maxrows(float x) {
  auto s = __builtin_amdgcn_permlane16_swap(__float_as_uint(x), __float_as_uint(x), false, false);
  x = fmaxf(__uint_as_float(s[0]), __uint_as_float(s[1]));
  auto t = __builtin_amdgcn_permlane32_swap(__float_as_uint(x), __float_as_uint(x), false, false);
  return fmaxf(__uint_as_float(t[0]), __uint_as_float(t[1]));
}
__device__ __forceinline__ float fdiv(float a, float d, float rd) { const float q = a * rd; return fmaf(fmaf(-q, d, a), rd, q); }
__device__ __forceinline__ float silu(float x) { const float d = 1.f + __expf(-x); return d < INFINITY ? fdiv(x, d, __builtin_amdgcn_rcpf(d)) : -0.f; }
__device__ __forceinline__ u64 now() { return __builtin_amdgcn_s_memrealtime(); }
constexpr u64 SPIN_CAP = 5000000;

#define TS(i) do {} while (0)
#define TSW(ww, i) do {} while (0)

struct Diag { u32 s[2][4]; };
__device__ __forceinline__ Diag make_diag(int c) {
  Diag d;
#pragma unroll
  for (int par = 0; par < 2; ++par)
#pragma unroll
    for (int w = 0; w < 4; ++w) {
      const u32 b = par ? 0x0302u : 0x0100u;
      d.s[par][w] = (c >> 1) == w ? ((c & 1) ? (b << 16) | 0x0c0cu : 0x0c0c0000u | b) : 0x0c0c0c0cu;
    }
  return d;
}
template <int PAR> __device__ __forceinline__ f32x4 axpy4(u32 val, u32x4 rows, const Diag& d, f32x4 acc) {
  if (ABL & 128) { USE(rows); return acc; }
  u32x4 af;
#pragma unroll
  for (int w = 0; w < 4; ++w) af[w] = __builtin_amdgcn_perm(val, val, d.s[PAR][w]);
  return mfma(af, rows, acc);
}
__device__ __forceinline__ float score4(u32x4 k, u32x4 q) {
  if (ABL & 128) { USE(k); return 1.f; }
  float d = dot2(k[0], q[0], 0.f); d = dot2(k[1], q[1], d); d = dot2(k[2], q[2], d); d = dot2(k[3], q[3], d);
  return sum16(d);
}
__device__ __forceinline__ void tr8(const u32x2 (&raw)[8], u32x4 (&f)[4]) {
#pragma unroll
  for (int e = 0; e < 4; ++e)
#pragma unroll
    for (int d = 0; d < 4; ++d) f[e][d] = __builtin_amdgcn_perm(raw[2 * d + 1][e >> 1], raw[2 * d][e >> 1], (e & 1) ? 0x07060302u : 0x05040100u);
}

__global__ void __launch_bounds__(1024) fused_decode(Args a) {
  __shared__ __attribute__((aligned(16))) char lds_[L_END];
  LAS char* lds = (LAS char*)lds_;
  if (CODE_KB && __builtin_amdgcn_readfirstlane(threadIdx.x >> 6) == 11 && (blockIdx.x >> 3) < CODE_KB) {
    u64 p;
    u32 c0, c1, c2, c3, c4, c5, c6, c7;
    asm volatile("s_getpc_b64 %0" : "=s"(p));
    p = (p & ~255ull) + (blockIdx.x >> 3) * 1024;
    asm volatile("s_load_dword %0, %8, 0x0\n\ts_load_dword %1, %8, 0x80\n\ts_load_dword %2, %8, 0x100\n\ts_load_dword %3, %8, 0x180\n\t"
                 "s_load_dword %4, %8, 0x200\n\ts_load_dword %5, %8, 0x280\n\ts_load_dword %6, %8, 0x300\n\ts_load_dword %7, %8, 0x380\n\ts_waitcnt lgkmcnt(0)"
                 : "=&s"(c0), "=&s"(c1), "=&s"(c2), "=&s"(c3), "=&s"(c4), "=&s"(c5), "=&s"(c6), "=&s"(c7) : "s"(p) : "memory");
  }
  asm volatile("" ::"s"(a.x), "s"(a.ws));
  const int tid = threadIdx.x, lane = tid & 63, w = __builtin_amdgcn_readfirstlane(tid >> 6), r = lane >> 4, c = lane & 15;
  const int blk = blockIdx.x, b = blk >> 5, h = blk & 31;
  const int hrot = (h - b) & 31, task = (hrot < 9 && !(ABL & 2)) ? b * 9 + hrot : -1;
  const char* kbase = a.k + ((size_t)blk << 20);
  const char* vbase = a.v + ((size_t)blk << 20);
  const rsrc_t rk = mk(kbase, 1u << 20);
  const int sw = w & 3;
  const int vA = lane * 16 + sw * 4096, vB = vA + 16384;
#define KVLD_(rs, beta, i, AUX) LD128(rs, ((i) < 4 ? vA : vB) + ((i) & 3) * 1024, (beta) * 32768, AUX)
#define KVLD(rs, beta, i) KVLD_(rs, beta, i, KAUX)
#define VLD(rs, beta, i) KVLD_(rs, beta, i, VAUX)
  float mx = -INFINITY;
  auto put_scores = [&](float sc, int batch) {
    const float s = rb(rb(sc) * 0.08838834764831845f);
    mx = fmaxf(mx, s);
    if (c < 8) *(LAS u16*)(lds + L_PS + batch * 64 + r * 16 + c * 2) = (u16)(__float_as_uint(s) >> 16);
  };
  auto softmax = [&]() {
    const float wm = maxrows(max16(mx));
    if (lane == 0) *(LAS float*)(lds + L_WM + w * 4) = wm;
    BAR();
    const float M = max16(*(LAS float*)(lds + L_WM + c * 4));
    const u32x2 sv = *(LAS u32x2*)(lds + L_PS + tid * 8);
    const float e0 = __expf(lo16(sv[0]) - M), e1 = __expf(hi16(sv[0]) - M), e2 = __expf(lo16(sv[1]) - M), e3 = __expf(hi16(sv[1]) - M);
    const float ws = sumrows(sum16((e0 + e1) + (e2 + e3)));
    if (lane == 0) *(LAS float*)(lds + L_WS + w * 4) = ws;
    BAR();
    const float inv = 1.f / sum16(*(LAS float*)(lds + L_WS + c * 4));
    *(LAS u32x2*)(lds + L_PS + tid * 8) = u32x2{pk(e0 * inv, e1 * inv), pk(e2 * inv, e3 * inv)};
    BAR();
  };

  if (w >= 12) {
    TSW(12, 0);
    u32x4 ring[8];
    RAWBAR();
#pragma unroll
    for (int i = 0; i < 7; ++i) { ring[i] = KVLD(rk, 0, i); PIN(); }
    __builtin_amdgcn_s_setprio(2);
    RAWBAR(); RAWBAR();
    TSW(12, 1);
    const u32x4 q = *(LAS u32x4*)(lds + L_QB + c * 16);
    float sc = 0.f;
    const int win0 = WIN0 + (task >= 0 ? WIN_MLP : 0);
    int win = win0;
#define PACE_IN() const u32 tj = (u32)now(); const u64 sch = __hip_atomic_load((LAS u64*)(lds + L_WN), __ATOMIC_RELAXED, __HIP_MEMORY_SCOPE_WORKGROUP)
#define PACE_OUT(j) do { if (PACE) { const u32 due_ = __builtin_amdgcn_readfirstlane((u32)sch), tau_ = __builtin_amdgcn_readfirstlane((u32)(sch >> 32)); \
      const int late_ = min(max((int)(tj * 256u - due_ - tau_ * (u32)(j)) >> 8, -4096), 4096); \
      win = tau_ ? min(max(WMID + ((late_ * GAIN) >> 8), WLO), WHI) : win0; } } while (0)
#pragma unroll 1
    for (int bb = 0; bb < 32 * REP; ++bb) {
      const int beta = bb & 31;
      const rsrc_t rn = mk(bb < 32 * REP - 1 ? kbase : vbase, 1u << 20);
      const int bn = beta < 31 ? beta + 1 : 0;
      if (bb == 32 * REP - 1) win = 64;
      PACE_IN();
      if (tid == 768) LPUT(L_PG, beta);
#define KSTEP(i) { GATE(i); if (i == 0) ring[7] = KVLD(rk, beta, 7); else ring[(i + 7) & 7] = KVLD(rn, bn, (i + 7) & 7); PIN(); \
        const float d = score4(ring[i], q); sc = (c & 7) == i ? d : sc; PIN(); }
      KSTEP(0) KSTEP(1) KSTEP(2) KSTEP(3) KSTEP(4) KSTEP(5) KSTEP(6) KSTEP(7)
      put_scores(sc, beta * 4 + sw);
      PACE_OUT(beta);
    }
    if (!PACE) win = win0;
    const rsrc_t rv = mk(vbase, 1u << 20);
    u32x4 ext[8];
    ring[7] = VLD(rv, 0, 7);
#pragma unroll
    for (int i = 0; i < 8; ++i) { ext[i] = VLD(rv, 1, i); PIN(); }
    TSW(12, 2);
    softmax();
    TSW(12, 3);
    const Diag dg = make_diag(c);
    f32x4 acc = {0.f, 0.f, 0.f, 0.f};
    const int po = L_PS + sw * 64 + r * 16;
    u32x4 pn = *(LAS u32x4*)(lds + po + 512);
    {
      const u32x4 p0 = *(LAS u32x4*)(lds + po), p1 = *(LAS u32x4*)(lds + po + 256);
#pragma unroll
      for (int i = 0; i < 8; ++i) { acc = (i & 1) ? axpy4<1>(p0[i >> 1], ring[i], dg, acc) : axpy4<0>(p0[i >> 1], ring[i], dg, acc); PIN(); }
#define ESTEP(i) { if (i > 0) { GATE(i); ring[(i + 7) & 7] = VLD(rv, 2, (i + 7) & 7); } PIN(); acc = axpy4<i & 1>(p1[i >> 1], ext[i], dg, acc); PIN(); }
      ESTEP(0) ESTEP(1) ESTEP(2) ESTEP(3) ESTEP(4) ESTEP(5) ESTEP(6) ESTEP(7)
    }
#pragma unroll 1
    for (int beta = 2; beta < 32; ++beta) {
      const rsrc_t rn = mk(vbase, beta < 31 ? 1u << 20 : 0u);
      const int bn = beta < 31 ? beta + 1 : 0;
      if (beta == 31) win = 64;
      const u32x4 pv = pn;
      pn = *(LAS u32x4*)(lds + po + bn * 256);
      PACE_IN();
      if (tid == 768) LPUT(L_PG, 32 + beta);
#define VSTEP(i) { GATE(i); if (i == 0) ring[7] = VLD(rv, beta, 7); else ring[(i + 7) & 7] = VLD(rn, bn, (i + 7) & 7); PIN(); \
        acc = axpy4<i & 1>(pv[i >> 1], ring[i], dg, acc); PIN(); }
      VSTEP(0) VSTEP(1) VSTEP(2) VSTEP(3) VSTEP(4) VSTEP(5) VSTEP(6) VSTEP(7)
      PACE_OUT(32 + beta);
    }
    if (r < 2) *(LAS f32x4*)(lds + L_OP + sw * 512 + c * 32 + r * 16) = acc;
    TSW(12, 4);
    BAR();
    return;
  }

  const rsrc_t rws = mk(a.ws, WS_END);
  auto wait_batch = [&](u32 n) { while (LGET(L_PG) < n) __builtin_amdgcn_s_sleep(2); };
  if (w == 11) {
    u32 t0, t1, t2, t3, t4, t5, t6, t7;
    asm volatile("" ::"s"(a.ws), "s"(a.out), "s"(kbase), "s"(vbase), "s"(a.w2));
    TOUCH(t0, kbase); TOUCH(t1, a.ws);
    RAWBAR();
    TOUCH(t2, a.wo + (size_t)h * (DH * DM * 2)); TOUCH(t3, a.w1); TOUCH(t4, a.w3); TOUCH(t5, vbase); TOUCH(t6, a.w2); TOUCH(t7, a.out);
    const u32x2 ep = LD64(rws, WS_EPOCH, 0, AUX_HO);
    if (lane < 48) {
#pragma unroll
      for (int i = 0; i < 3; ++i) ST128((u32x4{EMPTY, EMPTY, EMPTY, EMPTY}), rws, WS_ATT + blk * ATT_LD + lane * 16 + i * 768, 0, AUX_CL);
    }
    RAWBAR(); RAWBAR();
    asm volatile("s_waitcnt lgkmcnt(0)" : "+s"(t0), "+s"(t1), "+s"(t2), "+s"(t3), "+s"(t4), "+s"(t5), "+s"(t6), "+s"(t7)::"memory");
    const u64 tag = (((u64)(u32)__builtin_amdgcn_readfirstlane(ep[1]) << 32) | (u32)__builtin_amdgcn_readfirstlane(ep[0])) + 1;
    DRAIN();
    if (lane == 0) {
      *(LAS u64*)(lds + L_TG) = tag;
      ST64((u32x2{(u32)tag, (u32)(tag >> 32)}), rws, WS_ATT + blk * ATT_LD + DM * 4, 0, AUX_FL);
    }
    TS(17);
    const u32 tb = (u32)now();
    const bool sample = ((blk >> 3) & 3) == (blk & 3);
    float mprev = 0.f;
    int atprev = 0;
    auto pace = [&](int k, int at) {
      wait_batch(at);
      const u32 tk = (u32)now();
      if (sample && lane < 8) ST128((u32x4{tk, (u32)tag, tk, (u32)tag}), rws, WS_PACE + (k * 64 + b * 8 + (blk & 7)) * PACE_LD + lane * 16, 0, AUX_PS);
      wait_batch(at + DLY);
      const u32x2 v = LD64(rws, WS_PACE + (k * 64 + ((b + lane + blk) & 7) * 8 + (lane & 7)) * PACE_LD, 0, AUX_HO);
      const bool in = v[1] == (u32)tag && lane < 8;
      const float n = (float)__popcll(__ballot(in));
      const float m = (float)(int)(tk - tb) + sum16(in ? (float)(int)(v[0] - tk) : 0.f) * __builtin_amdgcn_rcpf(fmaxf(n, 1.f));
      if (k > 0 && lane == 0) {
        const float tau = (m - mprev) * __builtin_amdgcn_rcpf((float)(at - atprev)) * (1.f - EPS);
        const u32 tau256 = (u32)fminf(fmaxf(tau * 256.f, 1.f), 1e6f);
        __hip_atomic_store((LAS u64*)(lds + L_WN), ((u64)tau256 << 32) | (tb * 256u + (u32)(int)(m * 256.f) - tau256 * (u32)at), __ATOMIC_RELAXED, __HIP_MEMORY_SCOPE_WORKGROUP);
      }
      mprev = m; atprev = at;
    };
    if (PACE) {
#pragma unroll 1
      for (int k = 0; k < 4; ++k) pace(k, (CPK >> (8 * k)) & 255);
    }
    softmax();
    if (PACE) {
#pragma unroll 1
      for (int k = 0; k < 4; ++k) pace(4 + k, (CPV >> (8 * k)) & 255);
    }
    BAR();
    return;
  }
  if (w < 9) {
    const int wr = ROT ? (w + b) % 9 : w;
    u32x4 wb[16];
    {
      const rsrc_t rq = mk(a.wq + h * 256, DM * 8192 - h * 256);
      const int vo = (64 * wr + 16 * r) * 8192 + c * 16;
#pragma unroll
      for (int i = 0; i < 16; ++i) wb[i] = (ABL & 32) ? u32x4{1u,1u,1u,1u} : LD128(rq, vo, i * 8192, QAUX);
    }
    PIN();
    TSW(0, 16);
    const Diag dgq = make_diag(c);
    RAWBAR();
    TSW(0, 21);
    {
      const int xo = L_XB + (64 * wr + 16 * r) * 2;
      const u32x4 x0 = *(LAS u32x4*)(lds + xo), x1 = *(LAS u32x4*)(lds + xo + 16);
      f32x4 acc0 = {0.f, 0.f, 0.f, 0.f}, acc1 = acc0;
#pragma unroll
      for (int i = 0; i < 8; ++i) {
        const u32 xv = i < 4 ? x0[i & 3] : x1[i & 3];
        acc0 = axpy4<0>(xv, wb[2 * i], dgq, acc0); acc1 = axpy4<1>(xv, wb[2 * i + 1], dgq, acc1);
      }
      if (r < 2) *(LAS f32x4*)(lds + L_QP + wr * 512 + c * 32 + r * 16) = acc0 + acc1;
    }
    TSW(0, 22); TSW(8, 23);
    BAR();
    TSW(0, 24);
    if (w < 2) {
      float s = 0.f;
#pragma unroll
      for (int j = 0; j < 9; ++j) s += *(LAS float*)(lds + L_QP + j * 512 + tid * 4);
      *(LAS u16*)(lds + L_QB + tid * 2) = (u16)pk(s, s);
    }
    BAR();
  } else {
    __builtin_amdgcn_s_setprio(3);
    if (w == 10) {
      const rsrc_t rx = mk(a.x + b * (DM * 2), DM * 2);
      u32 xr[5];
#pragma unroll
      for (int i = 0; i < 5; ++i) xr[i] = LD32(rx, lane * 4 + i * 256, 0, NT);
      if (lane == 0) { LPUT(L_PG, 0); LPUT(L_WN, 0); LPUT(L_WN + 4, 0); }
      float s = 0.f;
#pragma unroll
      for (int i = 0; i < 5; ++i) s += lo16(xr[i]) + hi16(xr[i]);
      const float mean = sumrows(sum16(s)) * (1.f / DM);
      float ss = 0.f;
#pragma unroll
      for (int i = 0; i < 5; ++i) {
        const float d0 = lo16(xr[i]) - mean, d1 = hi16(xr[i]) - mean;
        ss += (i < 4 || lane < 32) ? d0 * d0 + d1 * d1 : 0.f;
      }
      const float var = sumrows(sum16(ss)) * (1.f / DM);
      const float m = rb(mean), den = rb(__builtin_amdgcn_sqrtf(rb(rb(var) + 1e-5f))), rd = __builtin_amdgcn_rcpf(den);
#pragma unroll
      for (int i = 0; i < 5; ++i)
        if (i < 4 || lane < 32) *(LAS u32*)(lds + L_XB + lane * 4 + i * 256) = pk(fdiv(rb(lo16(xr[i]) - m), den, rd), fdiv(rb(hi16(xr[i]) - m), den, rd));
    }
    TSW(10, 19);
    BAR();
    __builtin_amdgcn_s_setprio(0);
    RAWBAR(); RAWBAR();
  }
  PIN();

  if (w < 9) {
    u32x4 wf[4][4];
    const rsrc_t ro = mk(a.wo + (size_t)h * (DH * DM * 2), DH * DM * 2);
    const int vo = r * 8 * (DM * 2) + w * 128 + c * 8;
    u32x2 raw[4][8];
#pragma unroll
    for (int n = 0; n < 32; ++n) {
      raw[n >> 3][n & 7] = (ABL & 8) ? u32x2{1u,1u} : LD64(ro, vo, ((n >> 3) * 32 + (n & 7)) * (DM * 2), OAUX);
      if (n >= WOD) USE(raw[(n - WOD) >> 3][(n - WOD) & 7]);
      if (n >= WOD + 7 && ((n - WOD) & 7) == 7) tr8(raw[(n - WOD) >> 3], wf[(n - WOD) >> 3]);
      PIN();
    }
    tr8(raw[3], wf[3]);
    TSW(0, 8);
    PIN();
    softmax();
    BAR();
    TSW(0, 10);
    float o0 = 0.f, o1 = 0.f;
#pragma unroll
    for (int j = 0; j < 4; ++j) { const u32x2 t = *(LAS u32x2*)(lds + L_OP + j * 512 + lane * 8); o0 += __uint_as_float(t[0]); o1 += __uint_as_float(t[1]); }
    *(LAS u32*)(lds + L_OB + w * 256 + lane * 4) = pk(o0, o1);
    LWAIT();
    TSW(0, 25);
    f32x4 d[4];
#pragma unroll
    for (int e = 0; e < 4; ++e) d[e] = f32x4{0.f, 0.f, 0.f, 0.f};
#pragma unroll
    for (int s = 0; s < 4; ++s) {
      const u32x4 ob = *(LAS u32x4*)(lds + L_OB + w * 256 + s * 64 + r * 16);
#pragma unroll
      for (int e = 0; e < 4; ++e) d[e] = mfma(wf[s][e], ob, d[e]);
    }
    u32x4 o;
#pragma unroll
    for (int e = 0; e < 4; ++e) {
      const float t = (c & 2) ? ((c & 1) ? d[e][3] : d[e][2]) : ((c & 1) ? d[e][1] : d[e][0]);
      o[e] = min(__float_as_uint(t), EMPTY - 1);
    }
    if (c < 4) ST128(o, rws, WS_ATT + blk * ATT_LD + w * 256 + r * 64 + c * 16, 0, AUX_AS);
    TSW(0, 11); TSW(8, 26);
    return;
  }
  if (w == 9) {
    u32x4 hf[2] = {};
    if (task >= 0) {
      {
        const rsrc_t rx = mk(a.x, NB * DM * 2);
        u32x4 xa[9];
#pragma unroll
        for (int i = 0; i < 9; ++i) xa[i] = LD128(rx, (lane >> 3) * (DM * 2) + (lane & 7) * 16, i * 128, NT);
        float s = 0.f;
#pragma unroll
        for (int i = 0; i < 9; ++i)
#pragma unroll
          for (int j = 0; j < 4; ++j) s += lo16(xa[i][j]) + hi16(xa[i][j]);
        s += dpp<X1>(s); s += dpp<X2>(s); s += dpp<HMIR>(s);
        const float mean = s * (1.f / DM);
        float ss = 0.f;
#pragma unroll
        for (int i = 0; i < 9; ++i)
#pragma unroll
          for (int j = 0; j < 4; ++j) { const float d0 = lo16(xa[i][j]) - mean, d1 = hi16(xa[i][j]) - mean; ss += d0 * d0 + d1 * d1; }
        ss += dpp<X1>(ss); ss += dpp<X2>(ss); ss += dpp<HMIR>(ss);
        const float m = rb(mean), den = rb(__builtin_amdgcn_sqrtf(rb(rb(ss * (1.f / DM)) + 1e-5f))), rd = __builtin_amdgcn_rcpf(den);
#pragma unroll
        for (int i = 0; i < 9; ++i) {
          u32x4 o;
#pragma unroll
          for (int j = 0; j < 4; ++j) o[j] = pk(fdiv(rb(lo16(xa[i][j]) - m), den, rd), fdiv(rb(hi16(xa[i][j]) - m), den, rd));
          *(LAS u32x4*)(lds + L_XN + (lane >> 3) * XN_LD + (lane & 7) * 16 + i * 128) = o;
        }
        LWAIT();
        PIN();
      }
      const rsrc_t r1 = mk(a.w1 + task * 128, DM * FF * 2 - task * 128), r3 = mk(a.w3 + task * 128, DM * FF * 2 - task * 128);
      const int vo = r * 8 * (FF * 2) + c * 8;
      const int xo = L_XN + (c & 7) * XN_LD + r * 16;
      u32x2 ra[8], rc[8];
      f32x4 g[4], u[4];
#pragma unroll
      for (int e = 0; e < 4; ++e) { g[e] = f32x4{0.f, 0.f, 0.f, 0.f}; u[e] = g[e]; }
#pragma unroll
      for (int i = 0; i < 8; ++i) ra[i] = LD64(r1, vo, i * (FF * 2), WAUX);
#pragma unroll 1
      for (int s = 0; s < 18; ++s) {
        const int sc_ = s * 32 * (FF * 2), sn = (s < 17 ? s + 1 : 17) * 32 * (FF * 2);
        const rsrc_t n1 = mk(a.w1 + task * 128, s < 17 ? DM * FF * 2 - task * 128 : 0);
        const u32x4 xb = *(LAS u32x4*)(lds + xo + s * 64);
        u32x4 f[4];
#pragma unroll
        for (int i = 0; i < 8; ++i) { USE(ra[i]); rc[i] = LD64(r3, vo, sc_ + i * (FF * 2), WAUX); PIN(); }
        tr8(ra, f);
#pragma unroll
        for (int e = 0; e < 4; ++e) g[e] = mfma(f[e], xb, g[e]);
        PIN();
#pragma unroll
        for (int i = 0; i < 8; ++i) { USE(rc[i]); ra[i] = LD64(n1, vo, sn + i * (FF * 2), WAUX); PIN(); }
        tr8(rc, f);
#pragma unroll
        for (int e = 0; e < 4; ++e) u[e] = mfma(f[e], xb, u[e]);
        PIN();
      }
      float hv[4][4];
#pragma unroll
      for (int p = 0; p < 4; ++p)
#pragma unroll
        for (int e = 0; e < 4; ++e) hv[p][e] = rb(silu(rb(g[e][p]))) * rb(u[e][p]);
#pragma unroll
      for (int s = 0; s < 2; ++s) hf[s] = u32x4{pk(hv[2 * s][0], hv[2 * s][1]), pk(hv[2 * s][2], hv[2 * s][3]), pk(hv[2 * s + 1][0], hv[2 * s + 1][1]), pk(hv[2 * s + 1][2], hv[2 * s + 1][3])};
      TS(9);
    }
    PIN();
    softmax();
    if (task >= 0) {
      const rsrc_t r2 = mk(a.w2 + (size_t)task * (64 * DM * 2), 64 * DM * 2);
      const int vo = r * 16 * (DM * 2) + c * 8;
      u32x2 ra[8], rc[8];
#pragma unroll
      for (int i = 0; i < 8; ++i) ra[i] = LD64(r2, vo, i * (DM * 2), WAUX);
      const int so = L_MO + (c & 7) * (DM * 4) + r * 64 + (c >> 3) * 32;
#pragma unroll 1
      for (int W = 0; W < 9; ++W) {
        const rsrc_t n2 = mk(a.w2 + (size_t)task * (64 * DM * 2), W < 8 ? 64 * DM * 2 : 0);
        const int sn = (W < 8 ? W + 1 : 8) * 128;
        u32x4 f[4];
        f32x4 d[4];
#pragma unroll
        for (int i = 0; i < 8; ++i) { USE(ra[i]); rc[i] = LD64(r2, vo, W * 128 + (8 + i) * (DM * 2), WAUX); PIN(); }
        tr8(ra, f);
#pragma unroll
        for (int e = 0; e < 4; ++e) d[e] = mfma(f[e], hf[0], f32x4{0.f, 0.f, 0.f, 0.f});
        PIN();
#pragma unroll
        for (int i = 0; i < 8; ++i) { USE(rc[i]); ra[i] = LD64(n2, vo, sn + i * (DM * 2), WAUX); PIN(); }
        tr8(rc, f);
#pragma unroll
        for (int e = 0; e < 4; ++e) d[e] = mfma(f[e], hf[1], d[e]);
#pragma unroll
        for (int p = 0; p < 2; ++p) {
          u32x4 o;
#pragma unroll
          for (int e = 0; e < 4; ++e) o[e] = __float_as_uint((c & 8) ? d[e][2 + p] : d[e][p]);
          *(LAS u32x4*)(lds + so + p * 16 + W * 256) = o;
        }
        PIN();
      }
      LWAIT();
      TS(15);
      if (ALIGN) { const u64 t = now() | (ALIGN - 1); while (now() <= t) __builtin_amdgcn_s_sleep(2); }
      if (!(ABL & 4)) {
#pragma unroll
        for (int i = 0; i < 18; ++i) ST128(*(LAS u32x4*)(lds + L_MO + lane * 16 + i * 1024), rws, WS_MLP + task * MLP_LD + lane * 16, i * 1024, AUX_MS);
      }
      DRAIN();
      const u32x2 tg = *(LAS u32x2*)(lds + L_TG);
      if (lane == 0) ST64(tg, rws, WS_MLP + task * MLP_LD + NB * DM * 4, 0, AUX_FL);
      TS(12);
    }
    BAR();
    return;
  }
  PIN();
  softmax();
  if (h < NCONS) {
    bool ok = true;
    const u32x2 tg = *(LAS u32x2*)(lds + L_TG);
    const u32 t0 = tg[0], t1 = tg[1];
    const int col = h * 32 + (lane & 7) * 4;
    wait_batch(PRE_AT);
    const u32x2 xv = LD64(mk(a.x, NB * DM * 2), (b * DM + col) * 2, 0, NT);
    const u64 start = now();
    for (;;) {
      const u32x2 f1 = LD64(rws, WS_ATT + DM * 4 + (b * NH + (lane & 31)) * ATT_LD, 0, AUX_HO);
      const u32x2 f2 = LD64(rws, WS_MLP + NB * DM * 4 + lane * MLP_LD, 0, AUX_HO), f3 = LD64(rws, WS_MLP + NB * DM * 4 + (64 + (lane & 7)) * MLP_LD, 0, AUX_HO);
      bool all = f1[0] == t0 && f1[1] == t1 && f2[0] == t0 && f2[1] == t1 && f3[0] == t0 && f3[1] == t1;
      if (blk == 0) {
#pragma unroll
        for (int i = 0; i < 4; ++i) { const u32x2 f = LD64(rws, WS_ATT + DM * 4 + lane * ATT_LD, i * 64 * ATT_LD, AUX_HO); all &= f[0] == t0 && f[1] == t1; }
      }
      if (__all(all) || (ABL & 2)) break;
      if (now() - start > SPIN_CAP) { ok = false; break; }
      __builtin_amdgcn_s_sleep(16);
    }
    asm volatile("" ::: "memory");
    if (blk == 0 && lane == 0) ST64((u32x2{t0, t1}), rws, WS_EPOCH, 0, SC1);
    float f[4] = {0.f, 0.f, 0.f, 0.f};
    {
      u32x4 t[9];
#pragma unroll
      for (int i = 0; i < 9; ++i) t[i] = LD128(rws, WS_MLP + (lane >> 3) * MLP_LD + b * (DM * 4) + col * 4, i * 8 * MLP_LD, AUX_HO);
#pragma unroll
      for (int i = 0; i < 9; ++i)
#pragma unroll
        for (int e = 0; e < 4; ++e) f[e] += __uint_as_float(t[i][e]);
    }
    float g[4];
#pragma unroll
    for (int e = 0; e < 4; ++e) { float t = f[e]; t += dpp<ROR8>(t); g[e] = rb(silu(rb(sumrows(t)))); }
    TS(13);
    BAR();
    u32x4 t[4], u[4];
    const int ao = WS_ATT + (b * NH + (lane >> 3)) * ATT_LD + col * 4;
#define SWEEP(t) _Pragma("unroll") for (int i = 0; i < 4; ++i) t[i] = LD128(rws, ao, i * 8 * ATT_LD, AUX_HO)
#define FULL(t) __all(min(min(min(~t[0][0], ~t[0][1]), min(~t[0][2], ~t[0][3])), min(min(min(~t[1][0], ~t[1][1]), min(~t[1][2], ~t[1][3])), \
                min(min(min(~t[2][0], ~t[2][1]), min(~t[2][2], ~t[2][3])), min(min(~t[3][0], ~t[3][1]), min(~t[3][2], ~t[3][3]))))) != 0u)
    const u64 start2 = now();
    TS(7);
    SWEEP(t);
    for (;;) {
      asm volatile("" ::: "memory");
      SWEEP(u);
      PIN();
      if (FULL(t)) break;
      asm volatile("" ::: "memory");
      SWEEP(t);
      PIN();
      if (FULL(u)) {
#pragma unroll
        for (int i = 0; i < 4; ++i) t[i] = u[i];
        break;
      }
      if (!ok || now() - start2 > SPIN_CAP) { ok = false; break; }
    }
    float y[4];
#pragma unroll
    for (int e = 0; e < 4; ++e) {
      float s = (__uint_as_float(t[0][e]) + __uint_as_float(t[1][e])) + (__uint_as_float(t[2][e]) + __uint_as_float(t[3][e]));
      s += dpp<ROR8>(s);
      s = rb(sumrows(s));
      const float xe = (e & 1) ? hi16(xv[e >> 1]) : lo16(xv[e >> 1]);
      y[e] = ok ? xe + rb(s * g[e]) : __uint_as_float(0x7fc00000u);
    }
    if (lane < 8) ST64((u32x2{pk(y[0], y[1]), pk(y[2], y[3])}), mk(a.out, NB * DM * 2), (b * DM + col) * 2, 0, 0);
    TS(14);
  } else {
    BAR();
  }
}

}

extern "C" void kernel_launch(void* const* d_in, const int* in_sizes, int n_in, void* d_out, int out_size, void* d_ws, size_t ws_size,
                              hipStream_t stream) {
  Args a;
  a.x = (const char*)d_in[0]; a.k = (const char*)d_in[1]; a.v = (const char*)d_in[2]; a.wq = (const char*)d_in[3]; a.wo = (const char*)d_in[4];
  a.w1 = (const char*)d_in[6]; a.w3 = (const char*)d_in[7]; a.w2 = (const char*)d_in[8];
  a.out = (char*)d_out; a.ws = (char*)d_ws;
  hipLaunchKernelGGL(fused_decode, dim3(NB * NH), dim3(1024), 0, stream, a);
}
```

```cpp
#include <hip/hip_runtime.h>
#include <hip/hip_bf16.h>
#include <cstdint>

#define WIN0 41
#define WIN_MLP 2
#define PACE 1
#define GAIN 20
#define LAG0 0.f
#define WLO 36
#define WHI 56
#define ALIGN 512
#define WOD 1
#define PRE_AT 58
#define AUX_MS 18
#define AUX_CL 16
#define AUX_AS 16
#define HINT 0
#define DLY 3
#define CODE_KB 0
#define WMID 45
#define EPS 0.01f
#define PGRP 0
#define PRD 0
#define PSTR 8
#define AUX_PS 18
#define CPK 0x19110903
#define CPV 0x39332b23
#define AUX_FL 16
#define KFIRST 0
#define STAG 0
#define STR_(x) #x
#define STR(x) STR_(x)
#define TMAP 1
#define W3OFF 0
#define SROT 0
#define WOPF 0
#define FLAG 6
#define EARLYLN 0
#define NCK 4
#define NCV 4
#define HMAP 0
#define ABL 0
#define KAUX 2
#define VAUX 2
#define QAUX 0
#define OAUX 0
#define WAUX 2
#define AUX_HO 16
#define WARM 1
#define ROT 1

namespace {

typedef unsigned u32;
typedef unsigned long long u64;
typedef unsigned short u16;
typedef u32 u32x4 __attribute__((ext_vector_type(4)));
typedef u32 u32x2 __attribute__((ext_vector_type(2)));
typedef float f32x4 __attribute__((ext_vector_type(4)));
typedef __bf16 bf16x8 __attribute__((ext_vector_type(8)));
typedef __bf16 bf16x2 __attribute__((ext_vector_type(2)));
#define GAS __attribute__((address_space(1)))
#define LAS __attribute__((address_space(3)))

constexpr int NB = 8, DM = 576, NH = 32, SK = 4096, DH = 128, FF = 4608;
constexpr int NTASK = FF / 64;
constexpr int NCONS = DM / 32;
constexpr int NT = 2, SC1 = 16;

constexpr u32 ATT_LD = DM * 4 + 128, HID_LD = NB * 128 + 128, MLP_LD = NB * 256 + 128;
constexpr u32 PACE_LD = 4352;
constexpr u32 WS_EPOCH = 0, WS_ATT = 4096, WS_HID = WS_ATT + NB * NH * ATT_LD, WS_MLP = WS_HID + NTASK * HID_LD, WS_PACE = WS_MLP + NTASK * MLP_LD,
              WS_END = WS_PACE + 8 * 64 * PACE_LD;
constexpr u32 WS_DBG = 8u << 20;
constexpr u32 EMPTY = 0xffffffffu;

constexpr int XN_LD = 1168;
constexpr int L_XN = 0, L_QP = L_XN + NB * XN_LD, L_QB = L_QP + 12 * DH * 4, L_PS = L_QB + DH * 2, L_WM = L_PS + SK * 2, L_WS = L_WM + 64,
              L_OP = L_WS + 64, L_OB = L_OP + 4 * DH * 4, L_TG = L_OB + 9 * DH * 2, L_PG = L_TG + 8, L_TK = L_TG + 12, L_WN = L_TG + 16, L_XB = L_TG + 32, L_MO = L_XB + DM * 2, L_HB = L_MO + NB * 256, L_END = L_HB + 9 * 1024;

struct Args { const char* x; const char* k; const char* v; const char* wq; const char* wo; const char* w1; const char* w3; const char* w2; char* out; char* ws; };

typedef __amdgpu_buffer_rsrc_t rsrc_t;
__device__ __forceinline__ rsrc_t mk(const char* p, u32 n) { return __builtin_amdgcn_make_buffer_rsrc((void*)p, 0, n, 0x00020000); }
#define LD128(r, vo, so, aux) __builtin_amdgcn_raw_buffer_load_b128(r, vo, so, aux)
#define LD64(r, vo, so, aux) __builtin_amdgcn_raw_buffer_load_b64(r, vo, so, aux)
#define LD32(r, vo, so, aux) __builtin_amdgcn_raw_buffer_load_b32(r, vo, so, aux)
#define ST128(v, r, vo, so, aux) __builtin_amdgcn_raw_buffer_store_b128(v, r, vo, so, aux)
#define ST64(v, r, vo, so, aux) __builtin_amdgcn_raw_buffer_store_b64(v, r, vo, so, aux)
#define PIN() __builtin_amdgcn_sched_barrier(0)
#define USE(x) asm volatile("" ::"v"(x) : "memory")
#define BAR() asm volatile("s_waitcnt lgkmcnt(0)\n\ts_barrier" ::: "memory")
#define RAWBAR() asm volatile("s_barrier" ::: "memory")
#define TOUCH(d, p) asm volatile("s_load_dword %0, %1, 0x0" : "=&s"(d) : "s"(p) : "memory")
#define GATE(i) asm volatile("s_cmp_gt_i32 %0, %1\n\ts_cbranch_scc1 Lg7_%=\n\ts_cmp_gt_i32 %0, %2\n\ts_cbranch_scc1 Lg5_%=\n\t" \
                             "s_cmp_gt_i32 %0, %3\n\ts_cbranch_scc1 Lg4_%=\n\ts_waitcnt vmcnt(3)\nLg4_%=:\n\ts_waitcnt vmcnt(4)\n" \
                             "Lg5_%=:\n\ts_waitcnt vmcnt(5)\nLg7_%=:" \
                             ::"s"(win), "i"(48 + DITH(i)), "i"(40 + DITH(i)), "i"(32 + DITH(i)) : "scc", "memory")
#define DITH(i) (((i) & 1) * 4 + ((i) & 2) + ((i) >> 2))
#define LGET(off) __hip_atomic_load((LAS u32*)(lds + (off)), __ATOMIC_RELAXED, __HIP_MEMORY_SCOPE_WORKGROUP)
#define LPUT(off, v) __hip_atomic_store((LAS u32*)(lds + (off)), (u32)(v), __ATOMIC_RELAXED, __HIP_MEMORY_SCOPE_WORKGROUP)
#define LWAIT() asm volatile("s_waitcnt lgkmcnt(0)" ::: "memory")
#define DRAIN() asm volatile("s_waitcnt vmcnt(0)" ::: "memory")

__device__ __forceinline__ u32 pk(float lo, float hi) { bf16x2 t = {(__bf16)lo, (__bf16)hi}; return __builtin_bit_cast(u32, t); }
__device__ __forceinline__ float rb(float x) { return __uint_as_float(pk(x, x) << 16); }
__device__ __forceinline__ float lo16(u32 v) { return __uint_as_float(v << 16); }
__device__ __forceinline__ float hi16(u32 v) { return __uint_as_float(v & 0xffff0000u); }
__device__ __forceinline__ float dot2(u32 a, u32 b, float c) {
  return __builtin_amdgcn_fdot2_f32_bf16(__builtin_bit_cast(bf16x2, a), __builtin_bit_cast(bf16x2, b), c, false);
}
__device__ __forceinline__ f32x4 mfma(u32x4 a, u32x4 b, f32x4 c) {
  return __builtin_amdgcn_mfma_f32_16x16x32_bf16(__builtin_bit_cast(bf16x8, a), __builtin_bit_cast(bf16x8, b), c, 0, 0, 0);
}
template <int CTRL> __device__ __forceinline__ float dpp(float x) {
  return __builtin_bit_cast(float, __builtin_amdgcn_mov_dpp(__builtin_bit_cast(int, x), CTRL, 0xf, 0xf, true));
}
constexpr int X1 = 0xB1, X2 = 0x4E, HMIR = 0x141, MIR = 0x140, ROR8 = 0x128;
__device__ __forceinline__ float sum16(float x) { x += dpp<X1>(x); x += dpp<X2>(x); x += dpp<HMIR>(x); x += dpp<MIR>(x); return x; }
__device__ __forceinline__ float max16(float x) { x = fmaxf(x, dpp<X1>(x)); x = fmaxf(x, dpp<X2>(x)); x = fmaxf(x, dpp<HMIR>(x)); x = fmaxf(x, dpp<MIR>(x)); return x; }
__device__ __forceinline__ float sumrows(float x) {
  auto s = __builtin_amdgcn_permlane16_swap(__float_as_uint(x), __float_as_uint(x), false, false);
  x = __uint_as_float(s[0]) + __uint_as_float(s[1]);
  auto t = __builtin_amdgcn_permlane32_swap(__float_as_uint(x), __float_as_uint(x), false, false);
  return __uint_as_float(t[0]) + __uint_as_float(t[1]);
}
__device__ __forceinline__ float maxrows(float x) {
  auto s = __builtin_amdgcn_permlane16_swap(__float_as_uint(x), __float_as_uint(x), false, false);
  x = fmaxf(__uint_as_float(s[0]), __uint_as_float(s[1]));
  auto t = __builtin_amdgcn_permlane32_swap(__float_as_uint(x), __float_as_uint(x), false, false);
  return fmaxf(__uint_as_float(t[0]), __uint_as_float(t[1]));
}
__device__ __forceinline__ float sum8x4(float v0, float v1, float v2, float v3) {
  auto p = __builtin_amdgcn_permlane32_swap(__float_as_uint(v0), __float_as_uint(v2), false, false);
  auto q = __builtin_amdgcn_permlane32_swap(__float_as_uint(v1), __float_as_uint(v3), false, false);
  auto t = __builtin_amdgcn_permlane16_swap(__float_as_uint(__uint_as_float(p[0]) + __uint_as_float(p[1])), __float_as_uint(__uint_as_float(q[0]) + __uint_as_float(q[1])), false, false);
  const float x = __uint_as_float(t[0]) + __uint_as_float(t[1]);
  return x + dpp<ROR8>(x);
}
__device__ __forceinline__ float fdiv(float a, float d, float rd) { const float q = a * rd; return fmaf(fmaf(-q, d, a), rd, q); }
__device__ __forceinline__ float silu(float x) { const float d = 1.f + __expf(-x); return d < INFINITY ? fdiv(x, d, __builtin_amdgcn_rcpf(d)) : -0.f; }
__device__ __forceinline__ u64 now() { return __builtin_amdgcn_s_memrealtime(); }
constexpr u64 SPIN_CAP = 5000000;

#define TS(i) do {} while (0)
#define TSW(ww, i) do {} while (0)

struct Diag { u32 s[2][4]; };
__device__ __forceinline__ Diag make_diag(int c) {
  Diag d;
#pragma unroll
  for (int par = 0; par < 2; ++par)
#pragma unroll
    for (int w = 0; w < 4; ++w) {
      const u32 b = par ? 0x0302u : 0x0100u;
      d.s[par][w] = (c >> 1) == w ? ((c & 1) ? (b << 16) | 0x0c0cu : 0x0c0c0000u | b) : 0x0c0c0c0cu;
    }
  return d;
}
template <int PAR> __device__ __forceinline__ f32x4 axpy4(u32 val, u32x4 rows, const Diag& d, f32x4 acc) {
  if (ABL & 128) { USE(rows); return acc; }
  u32x4 af;
#pragma unroll
  for (int w = 0; w < 4; ++w) af[w] = __builtin_amdgcn_perm(val, val, d.s[PAR][w]);
  return mfma(af, rows, acc);
}
__device__ __forceinline__ float score4(u32x4 k, u32x4 q) {
  if (ABL & 128) { USE(k); return 1.f; }
  float d = dot2(k[0], q[0], 0.f); d = dot2(k[1], q[1], d); d = dot2(k[2], q[2], d); d = dot2(k[3], q[3], d);
  return sum16(d);
}
__device__ __forceinline__ void tr8(const u32x2 (&raw)[8], u32x4 (&f)[4]) {
#pragma unroll
  for (int e = 0; e < 4; ++e)
#pragma unroll
    for (int d = 0; d < 4; ++d) f[e][d] = __builtin_amdgcn_perm(raw[2 * d + 1][e >> 1], raw[2 * d][e >> 1], (e & 1) ? 0x07060302u : 0x05040100u);
}

__global__ void __launch_bounds__(1024) fused_decode(Args a) {
  __shared__ __attribute__((aligned(16))) char lds_[L_END];
  LAS char* lds = (LAS char*)lds_;
  if (CODE_KB && __builtin_amdgcn_readfirstlane(threadIdx.x >> 6) == 11 && (blockIdx.x >> 3) < CODE_KB) {
    u64 p;
    u32 c0, c1, c2, c3, c4, c5, c6, c7;
    asm volatile("s_getpc_b64 %0" : "=s"(p));
    p = (p & ~255ull) + (blockIdx.x >> 3) * 1024;
    asm volatile("s_load_dword %0, %8, 0x0\n\ts_load_dword %1, %8, 0x80\n\ts_load_dword %2, %8, 0x100\n\ts_load_dword %3, %8, 0x180\n\t"
                 "s_load_dword %4, %8, 0x200\n\ts_load_dword %5, %8, 0x280\n\ts_load_dword %6, %8, 0x300\n\ts_load_dword %7, %8, 0x380\n\ts_waitcnt lgkmcnt(0)"
                 : "=&s"(c0), "=&s"(c1), "=&s"(c2), "=&s"(c3), "=&s"(c4), "=&s"(c5), "=&s"(c6), "=&s"(c7) : "s"(p) : "memory");
  }
  asm volatile("" ::"s"(a.x), "s"(a.ws));
  const int tid = threadIdx.x, lane = tid & 63, w = __builtin_amdgcn_readfirstlane(tid >> 6), r = lane >> 4, c = lane & 15;
  const int blk = blockIdx.x, b = blk >> 5, hh = blk & 31, h = HMAP ? (hh & 7) * 4 + (hh >> 3) : hh, bh = b * NH + h;
  const int hrot = (hh - b) & 31, job = (ABL & 2) ? 3 : hrot / 9, task = (hh & 7) * 9 + (hrot % 9 < 8 ? b : 8);
  const char* kbase = a.k + ((size_t)bh << 20);
  const char* vbase = a.v + ((size_t)bh << 20);
  const rsrc_t rk = mk(kbase, 1u << 20);
  const int sw = w & 3;
  const int vA = lane * 16 + sw * 4096, vB = vA + 16384;
#define KVLD_(rs, beta, i, AUX) LD128(rs, ((i) < 4 ? vA : vB) + ((i) & 3) * 1024, (beta) * 32768, AUX)
#define KVLD(rs, beta, i) KVLD_(rs, beta, i, KAUX)
#define VLD(rs, beta, i) KVLD_(rs, beta, i, VAUX)
  float mx = -INFINITY;
  auto put_scores = [&](float sc, int batch) {
    const float s = rb(rb(sc) * 0.08838834764831845f);
    mx = fmaxf(mx, s);
    if (c < 8) *(LAS u16*)(lds + L_PS + batch * 64 + r * 16 + c * 2) = (u16)(__float_as_uint(s) >> 16);
  };
  auto softmax = [&]() {
    const float wm = maxrows(max16(mx));
    if (lane == 0) *(LAS float*)(lds + L_WM + w * 4) = wm;
    BAR();
    const float M = max16(*(LAS float*)(lds + L_WM + c * 4));
    const u32x2 sv = *(LAS u32x2*)(lds + L_PS + tid * 8);
    const float e0 = __expf(lo16(sv[0]) - M), e1 = __expf(hi16(sv[0]) - M), e2 = __expf(lo16(sv[1]) - M), e3 = __expf(hi16(sv[1]) - M);
    const float ws = sumrows(sum16((e0 + e1) + (e2 + e3)));
    if (lane == 0) *(LAS float*)(lds + L_WS + w * 4) = ws;
    BAR();
    const float inv = 1.f / sum16(*(LAS float*)(lds + L_WS + c * 4));
    *(LAS u32x2*)(lds + L_PS + tid * 8) = u32x2{pk(e0 * inv, e1 * inv), pk(e2 * inv, e3 * inv)};
    BAR();
  };

  if (w >= 12) {
    TSW(12, 0);
    u32x4 ring[8];
    u32x4 ext[8];
    __builtin_amdgcn_s_setprio(3);
    if (!KFIRST) RAWBAR();
#pragma unroll
    for (int i = 0; i < 7; ++i) { ring[i] = KVLD(rk, 0, i); PIN(); }
    if (KFIRST) {
      RAWBAR();
      ring[7] = KVLD(rk, 0, 7);
#pragma unroll
      for (int i = 0; i < 8; ++i) { ext[i] = KVLD(rk, 1, i); PIN(); }
    }
    RAWBAR(); RAWBAR();
    TSW(12, 1);
    const u32x4 q = *(LAS u32x4*)(lds + L_QB + c * 16);
    float sc = 0.f;
    const int win0 = WIN0 + (job == 0 ? WIN_MLP : 0);
    int win = win0;
#define PACE_IN() const u32 tj = (u32)now(); const u64 sch = __hip_atomic_load((LAS u64*)(lds + L_WN), __ATOMIC_RELAXED, __HIP_MEMORY_SCOPE_WORKGROUP)
#define PACE_OUT(j) do { if (PACE) { const u32 due_ = __builtin_amdgcn_readfirstlane((u32)sch), tau_ = __builtin_amdgcn_readfirstlane((u32)(sch >> 32)); \
      const int late_ = min(max((int)(tj * 256u - due_ - tau_ * (u32)(j)) >> 8, -4096), 4096); \
      win = tau_ ? min(max(WMID + ((late_ * GAIN) >> 8), WLO), WHI) : win0; } } while (0)
    if (KFIRST) {
#pragma unroll
      for (int i = 0; i < 8; ++i) { const float d = score4(ring[i], q); sc = (c & 7) == i ? d : sc; PIN(); }
      put_scores(sc, sw);
#define FSTEP(i) { if (i > 0) { GATE(i); ring[(i + 7) & 7] = KVLD(rk, 2, (i + 7) & 7); } PIN(); const float d = score4(ext[i], q); sc = (c & 7) == i ? d : sc; PIN(); }
      FSTEP(0) FSTEP(1) FSTEP(2) FSTEP(3) FSTEP(4) FSTEP(5) FSTEP(6) FSTEP(7)
      put_scores(sc, 4 + sw);
    }
#pragma unroll 1
    for (int beta = KFIRST ? 2 : 0; beta < 32; ++beta) {
      const rsrc_t rn = mk(beta < 31 ? kbase : vbase, 1u << 20);
      const int bn = beta < 31 ? beta + 1 : 0;
      if (beta == 31) win = 64;
      PACE_IN();
      if (tid == 768) LPUT(L_PG, beta);
#define KSTEP(i) { GATE(i); if (i == 0) ring[7] = KVLD(rk, beta, 7); else ring[(i + 7) & 7] = KVLD(rn, bn, (i + 7) & 7); PIN(); \
        const float d = score4(ring[i], q); sc = (c & 7) == i ? d : sc; PIN(); }
      KSTEP(0) KSTEP(1) KSTEP(2) KSTEP(3) KSTEP(4) KSTEP(5) KSTEP(6) KSTEP(7)
      put_scores(sc, beta * 4 + sw);
      PACE_OUT(beta);
    }
    if (!PACE) win = win0;
    const rsrc_t rv = mk(vbase, 1u << 20);
    ring[7] = VLD(rv, 0, 7);
#pragma unroll
    for (int i = 0; i < 8; ++i) { ext[i] = VLD(rv, 1, i); PIN(); }
    TSW(12, 2);
    softmax();
    TSW(12, 3);
    const Diag dg = make_diag(c);
    f32x4 acc = {0.f, 0.f, 0.f, 0.f};
    const int po = L_PS + sw * 64 + r * 16;
    u32x4 pn = *(LAS u32x4*)(lds + po + 512);
    {
      const u32x4 p0 = *(LAS u32x4*)(lds + po), p1 = *(LAS u32x4*)(lds + po + 256);
#pragma unroll
      for (int i = 0; i < 8; ++i) { acc = (i & 1) ? axpy4<1>(p0[i >> 1], ring[i], dg, acc) : axpy4<0>(p0[i >> 1], ring[i], dg, acc); PIN(); }
#define ESTEP(i) { if (i > 0) { GATE(i); ring[(i + 7) & 7] = VLD(rv, 2, (i + 7) & 7); } PIN(); acc = axpy4<i & 1>(p1[i >> 1], ext[i], dg, acc); PIN(); }
      ESTEP(0) ESTEP(1) ESTEP(2) ESTEP(3) ESTEP(4) ESTEP(5) ESTEP(6) ESTEP(7)
    }
#pragma unroll 1
    for (int beta = 2; beta < 32; ++beta) {
      const rsrc_t rn = mk(vbase, beta < 31 ? 1u << 20 : 0u);
      const int bn = beta < 31 ? beta + 1 : 0;
      if (beta == 31) win = 64;
      const u32x4 pv = pn;
      pn = *(LAS u32x4*)(lds + po + bn * 256);
      PACE_IN();
      if (tid == 768) LPUT(L_PG, 32 + beta);
#define VSTEP(i) { GATE(i); if (i == 0) ring[7] = VLD(rv, beta, 7); else ring[(i + 7) & 7] = VLD(rn, bn, (i + 7) & 7); PIN(); \
        acc = axpy4<i & 1>(pv[i >> 1], ring[i], dg, acc); PIN(); }
      VSTEP(0) VSTEP(1) VSTEP(2) VSTEP(3) VSTEP(4) VSTEP(5) VSTEP(6) VSTEP(7)
      PACE_OUT(32 + beta);
    }
    if (r < 2) *(LAS f32x4*)(lds + L_OP + sw * 512 + c * 32 + r * 16) = acc;
    TSW(12, 4);
    BAR();
    return;
  }

  const rsrc_t rws = mk(a.ws, WS_END);
  auto wait_batch = [&](u32 n) { while (LGET(L_PG) < n) __builtin_amdgcn_s_sleep(2); };
  if (w == 11) {
    u32 t0, t1, t2, t3, t4, t5, t6, t7;
    asm volatile("" ::"s"(a.ws), "s"(a.out), "s"(kbase), "s"(vbase), "s"(a.w2));
    TOUCH(t0, kbase); TOUCH(t1, a.ws);
    RAWBAR();
    TOUCH(t2, a.wo + (size_t)h * (DH * DM * 2)); TOUCH(t3, a.w1); TOUCH(t4, a.w3); TOUCH(t5, vbase); TOUCH(t6, a.w2); TOUCH(t7, a.out);
    const u32x2 ep = LD64(rws, WS_EPOCH, 0, AUX_HO);
    if (lane < 48 && !(ABL & 16)) {
#pragma unroll
      for (int i = 0; i < 3; ++i) ST128((u32x4{EMPTY, EMPTY, EMPTY, EMPTY}), rws, WS_ATT + bh * ATT_LD + lane * 16 + i * 768, 0, AUX_CL);
    }
    RAWBAR(); RAWBAR();
    asm volatile("s_waitcnt lgkmcnt(0)" : "+s"(t0), "+s"(t1), "+s"(t2), "+s"(t3), "+s"(t4), "+s"(t5), "+s"(t6), "+s"(t7)::"memory");
    const u64 tag = (((u64)(u32)__builtin_amdgcn_readfirstlane(ep[1]) << 32) | (u32)__builtin_amdgcn_readfirstlane(ep[0])) + 1;
    DRAIN();
    if (lane == 0) {
      *(LAS u64*)(lds + L_TG) = tag;
      LWAIT();
      LPUT(L_TK, 1);
      ST64((u32x2{(u32)tag, (u32)(tag >> 32)}), rws, WS_ATT + bh * ATT_LD + DM * 4, 0, AUX_FL);
    }
    TS(17);
    const u32 tb = (u32)now();
    const bool sample = ((blk >> 3) & 3) == (blk & 3);
    float mprev = 0.f;
    int atprev = 0;
    auto pace = [&](int k, int at) {
      wait_batch(at);
      const u32 tk = (u32)now();
      if (sample && lane < 8) ST128((u32x4{tk, (u32)tag, tk, (u32)tag}), rws, WS_PACE + (k * 64 + b * 8 + (blk & 7)) * PACE_LD + lane * 16, 0, AUX_PS);
      wait_batch(at + DLY);
      const u32x2 v = LD64(rws, WS_PACE + (k * 64 + ((b + lane + blk) & 7) * 8 + (lane & 7)) * PACE_LD, 0, AUX_HO);
      const bool in = v[1] == (u32)tag && lane < 8;
      const float n = (float)__popcll(__ballot(in));
      const float m = (float)(int)(tk - tb) + sum16(in ? (float)(int)(v[0] - tk) : 0.f) * __builtin_amdgcn_rcpf(fmaxf(n, 1.f));
      if (k > 0 && lane == 0) {
        const float tau = (m - mprev) * __builtin_amdgcn_rcpf((float)(at - atprev)) * (1.f - EPS);
        const u32 tau256 = (u32)fminf(fmaxf(tau * 256.f, 1.f), 1e6f);
        __hip_atomic_store((LAS u64*)(lds + L_WN), ((u64)tau256 << 32) | (tb * 256u + (u32)(int)(m * 256.f) - tau256 * (u32)at), __ATOMIC_RELAXED, __HIP_MEMORY_SCOPE_WORKGROUP);
      }
      mprev = m; atprev = at;
    };
    if (PACE) {
#pragma unroll 1
      for (int k = 0; k < NCK; ++k) pace(k, (CPK >> (8 * k)) & 255);
    }
    softmax();
    if (PACE) {
#pragma unroll 1
      for (int k = 0; k < NCV; ++k) pace(4 + k, (CPV >> (8 * k)) & 255);
    }
    BAR();
    return;
  }
  auto ln_all = [&]()
      {
        const rsrc_t rx = mk(a.x, NB * DM * 2);
        u32x4 xa[9];
#pragma unroll
        for (int i = 0; i < 9; ++i) xa[i] = LD128(rx, (lane >> 3) * (DM * 2) + (lane & 7) * 16, i * 128, NT);
        float s = 0.f;
#pragma unroll
        for (int i = 0; i < 9; ++i)
#pragma unroll
          for (int j = 0; j < 4; ++j) s += lo16(xa[i][j]) + hi16(xa[i][j]);
        s += dpp<X1>(s); s += dpp<X2>(s); s += dpp<HMIR>(s);
        const float mean = s * (1.f / DM);
        float ss = 0.f;
#pragma unroll
        for (int i = 0; i < 9; ++i)
#pragma unroll
          for (int j = 0; j < 4; ++j) { const float d0 = lo16(xa[i][j]) - mean, d1 = hi16(xa[i][j]) - mean; ss += d0 * d0 + d1 * d1; }
        ss += dpp<X1>(ss); ss += dpp<X2>(ss); ss += dpp<HMIR>(ss);
        const float m = rb(mean), den = rb(__builtin_amdgcn_sqrtf(rb(rb(ss * (1.f / DM)) + 1e-5f))), rd = __builtin_amdgcn_rcpf(den);
#pragma unroll
        for (int i = 0; i < 9; ++i) {
          u32x4 o;
#pragma unroll
          for (int j = 0; j < 4; ++j) o[j] = pk(fdiv(rb(lo16(xa[i][j]) - m), den, rd), fdiv(rb(hi16(xa[i][j]) - m), den, rd));
          *(LAS u32x4*)(lds + L_XN + (lane >> 3) * XN_LD + (lane & 7) * 16 + i * 128) = o;
        }
        LWAIT();
        PIN();
      };
  if (w < 9) {
    const int wr = ROT ? (w + b) % 9 : w;
    u32x4 wb[16];
    {
      const rsrc_t rq = mk(a.wq + h * 256, DM * 8192 - h * 256);
      const int vo = (64 * wr + 16 * r) * 8192 + c * 16;
#pragma unroll
      for (int i = 0; i < 16; ++i) wb[i] = (ABL & 32) ? u32x4{1u,1u,1u,1u} : LD128(rq, vo, i * 8192, QAUX);
    }
    PIN();

    const Diag dgq = make_diag(c);
    RAWBAR();
    TSW(8, 21);
    {
      const int xo = L_XB + (64 * wr + 16 * r) * 2;
      const u32x4 x0 = *(LAS u32x4*)(lds + xo), x1 = *(LAS u32x4*)(lds + xo + 16);
      f32x4 acc0 = {0.f, 0.f, 0.f, 0.f}, acc1 = acc0;
#pragma unroll
      for (int i = 0; i < 8; ++i) {
        const u32 xv = i < 4 ? x0[i & 3] : x1[i & 3];
        acc0 = axpy4<0>(xv, wb[2 * i], dgq, acc0); acc1 = axpy4<1>(xv, wb[2 * i + 1], dgq, acc1);
      }
      if (r < 2) *(LAS f32x4*)(lds + L_QP + wr * 512 + c * 32 + r * 16) = acc0 + acc1;
    }
    TSW(0, 22); TSW(8, 23);
    BAR();
    TSW(0, 24);
    if (w < 2) {
      float s = 0.f;
#pragma unroll
      for (int j = 0; j < 9; ++j) s += *(LAS float*)(lds + L_QP + j * 512 + tid * 4);
      *(LAS u16*)(lds + L_QB + tid * 2) = (u16)pk(s, s);
    }
    BAR();
  } else {
    __builtin_amdgcn_s_setprio(3);
    if (w == 10) {
      const rsrc_t rx = mk(a.x + b * (DM * 2), DM * 2);
      u32 xr[5];
#pragma unroll
      for (int i = 0; i < 5; ++i) xr[i] = LD32(rx, lane * 4 + i * 256, 0, NT);
      if (lane == 0) { LPUT(L_PG, 0); LPUT(L_TK, 0); LPUT(L_WN, 0); LPUT(L_WN + 4, 0); }
      float s = 0.f;
#pragma unroll
      for (int i = 0; i < 5; ++i) s += lo16(xr[i]) + hi16(xr[i]);
      const float mean = sumrows(sum16(s)) * (1.f / DM);
      float ss = 0.f;
#pragma unroll
      for (int i = 0; i < 5; ++i) {
        const float d0 = lo16(xr[i]) - mean, d1 = hi16(xr[i]) - mean;
        ss += (i < 4 || lane < 32) ? d0 * d0 + d1 * d1 : 0.f;
      }
      const float var = sumrows(sum16(ss)) * (1.f / DM);
      const float m = rb(mean), den = rb(__builtin_amdgcn_sqrtf(rb(rb(var) + 1e-5f))), rd = __builtin_amdgcn_rcpf(den);
#pragma unroll
      for (int i = 0; i < 5; ++i)
        if (i < 4 || lane < 32) *(LAS u32*)(lds + L_XB + lane * 4 + i * 256) = pk(fdiv(rb(lo16(xr[i]) - m), den, rd), fdiv(rb(hi16(xr[i]) - m), den, rd));
    }
    if (EARLYLN && w == 9 && job == 0) { RAWBAR(); ln_all(); RAWBAR(); RAWBAR(); } else
    if (WOPF && w == 9) {
      const rsrc_t ro = mk(a.wo + (size_t)h * (DH * DM * 2) + b * (DH * DM / 4), DH * DM / 4);
      u32x4 t[18];
#pragma unroll
      for (int i = 0; i < 18; ++i) t[i] = LD128(ro, lane * 16, i * 1024, 0);
      RAWBAR(); RAWBAR(); RAWBAR();
#pragma unroll
      for (int i = 0; i < 18; ++i) USE(t[i]);
    } else {
    TSW(10, 19);
    BAR();
    RAWBAR(); RAWBAR();
    }
    __builtin_amdgcn_s_setprio(0);
  }
  PIN();

  if (w < 9) {
    u32x4 wf[4][4];
    const rsrc_t ro = mk(a.wo + (size_t)h * (DH * DM * 2), DH * DM * 2);
    const int vo = r * 8 * (DM * 2) + w * 128 + c * 8;
    u32x2 raw[4][8];
#pragma unroll
    for (int n = 0; n < 32; ++n) {
      raw[n >> 3][n & 7] = (ABL & 8) ? u32x2{1u,1u} : LD64(ro, vo, ((n >> 3) * 32 + (n & 7)) * (DM * 2), OAUX);
      if (n >= WOD) USE(raw[(n - WOD) >> 3][(n - WOD) & 7]);
      if (n >= WOD + 7 && ((n - WOD) & 7) == 7) tr8(raw[(n - WOD) >> 3], wf[(n - WOD) >> 3]);
      PIN();
    }
    tr8(raw[3], wf[3]);
    int so = WS_ATT + bh * ATT_LD + w * 256 + r * 64 + c * 16, lo = L_OP + lane * 8, lb = L_OB + w * 256 + lane * 4, lr = L_OB + w * 256 + r * 16;
    u32 m1 = 0u - (c & 1), m2 = 0u - ((c >> 1) & 1);
#pragma unroll
    for (int s = 0; s < 4; ++s) asm volatile("" : "+v"(wf[s][0]), "+v"(wf[s][1]), "+v"(wf[s][2]), "+v"(wf[s][3]));
    asm volatile("" : "+v"(so), "+v"(lo), "+v"(lb), "+v"(lr), "+v"(m1), "+v"(m2));
    TSW(0, 8);
    PIN();
    softmax();
    BAR();
    TSW(0, 10);
    float o0, o1;
    {
      const u32x2 t0 = *(LAS u32x2*)(lds + lo), t1 = *(LAS u32x2*)(lds + lo + 512), t2 = *(LAS u32x2*)(lds + lo + 1024), t3 = *(LAS u32x2*)(lds + lo + 1536);
      o0 = (__uint_as_float(t0[0]) + __uint_as_float(t1[0])) + (__uint_as_float(t2[0]) + __uint_as_float(t3[0]));
      o1 = (__uint_as_float(t0[1]) + __uint_as_float(t1[1])) + (__uint_as_float(t2[1]) + __uint_as_float(t3[1]));
    }
    *(LAS u32*)(lds + lb) = pk(o0, o1);
    LWAIT();
    TSW(0, 25);
    u32x4 ob[4];
#pragma unroll
    for (int s = 0; s < 4; ++s) ob[s] = *(LAS u32x4*)(lds + lr + s * 64);
    PIN();
    f32x4 d[4];
#pragma unroll
    for (int e = 0; e < 4; ++e) d[e] = f32x4{0.f, 0.f, 0.f, 0.f};
#pragma unroll
    for (int s = 0; s < 4; ++s)
#pragma unroll
      for (int e = 0; e < 4; ++e) d[e] = mfma(wf[s][e], ob[s], d[e]);
    u32x4 o;
#pragma unroll
    for (int e = 0; e < 4; ++e) {
      const u32 x = (m1 & __float_as_uint(d[e][1])) | (~m1 & __float_as_uint(d[e][0])), y = (m1 & __float_as_uint(d[e][3])) | (~m1 & __float_as_uint(d[e][2]));
      o[e] = min((m2 & y) | (~m2 & x), EMPTY - 1);
    }
    if (c < 4) ST128(o, rws, so, 0, AUX_AS);
    TSW(0, 11); TSW(8, 26);
    return;
  }
  if (w == 9) {
    if (job == 0) {
      if (!EARLYLN) ln_all();
      const rsrc_t r1 = mk(a.w1 + task * 128, DM * FF * 2 - task * 128), r3 = mk(a.w3 + task * 128, DM * FF * 2 - task * 128);
      const int vo = r * 8 * (FF * 2) + c * 8;
      const int xo = L_XN + (c & 7) * XN_LD + r * 16;
      u32x2 ra[8], rc[8];
      f32x4 g[4], u[4];
#pragma unroll
      for (int e = 0; e < 4; ++e) { g[e] = f32x4{0.f, 0.f, 0.f, 0.f}; u[e] = g[e]; }
#pragma unroll
      for (int i = 0; i < 8; ++i) ra[i] = LD64(r1, vo, i * (FF * 2), WAUX);
#pragma unroll
      for (int i = 0; i < 8 - FLAG; ++i) rc[i] = LD64(r3, vo, i * (FF * 2), WAUX);
#define PAIR(x, y, d, rsx, sx, rsy, sy) { USE(x[2 * d + 1]); \
        _Pragma("unroll") for (int e = 0; e < 4; ++e) f[e][d] = __builtin_amdgcn_perm(x[2 * d + 1][e >> 1], x[2 * d][e >> 1], (e & 1) ? 0x07060302u : 0x05040100u); \
        PIN(); \
        _Pragma("unroll") for (int j = 2 * d - FLAG; j < 2 * d + 2 - FLAG; ++j) { if (j < 0) y[8 + j] = LD64(rsy, vo, (sy + 8 + j) * (FF * 2), WAUX); else x[j] = LD64(rsx, vo, (sx + j) * (FF * 2), WAUX); } \
        PIN(); }
#pragma unroll 1
      for (int s = 0; s < ((ABL & 512) ? 1 : 18); ++s) {
        const int sc_ = s * 32, sn = (s < 17 ? s + 1 : 17) * 32;
        const u32 nb = s < 17 ? DM * FF * 2 - task * 128 : 0;
        const rsrc_t n1 = mk(a.w1 + task * 128, nb), n3 = mk(a.w3 + task * 128, nb);
        const u32x4 xb = *(LAS u32x4*)(lds + xo + s * 64);
        u32x4 f[4];
        PAIR(ra, rc, 0, n1, sn, r3, sc_) PAIR(ra, rc, 1, n1, sn, r3, sc_) PAIR(ra, rc, 2, n1, sn, r3, sc_) PAIR(ra, rc, 3, n1, sn, r3, sc_)
#pragma unroll
        for (int e = 0; e < 4; ++e) g[e] = mfma(f[e], xb, g[e]);
        PIN();
        PAIR(rc, ra, 0, n3, sn, n1, sn) PAIR(rc, ra, 1, n3, sn, n1, sn) PAIR(rc, ra, 2, n3, sn, n1, sn) PAIR(rc, ra, 3, n3, sn, n1, sn)
#pragma unroll
        for (int e = 0; e < 4; ++e) u[e] = mfma(f[e], xb, u[e]);
        PIN();
      }
      float hv[4][4];
#pragma unroll
      for (int p = 0; p < 4; ++p)
#pragma unroll
        for (int e = 0; e < 4; ++e) hv[p][e] = rb(silu(rb(g[e][p]))) * rb(u[e][p]);
      const u32x4 h0 = {pk(hv[0][0], hv[0][1]), pk(hv[0][2], hv[0][3]), pk(hv[1][0], hv[1][1]), pk(hv[1][2], hv[1][3])};
      const u32x4 h1 = {pk(hv[2][0], hv[2][1]), pk(hv[2][2], hv[2][3]), pk(hv[3][0], hv[3][1]), pk(hv[3][2], hv[3][3])};
      ST128((c & 8) ? h1 : h0, rws, WS_HID + task * HID_LD + (c & 7) * 128 + r * 32 + (c >> 3) * 16, 0, AUX_MS);
      DRAIN();
      while (!LGET(L_TK)) __builtin_amdgcn_s_sleep(2);
      asm volatile("" ::: "memory");
      if (lane == 0) ST64(*(LAS u32x2*)(lds + L_TG), rws, WS_HID + task * HID_LD + NB * 128, 0, AUX_FL);
      TS(9);
    }
    PIN();
    softmax();
    if (job == 1) {
      const int ks = task / 9, cg = task % 9;
      const rsrc_t r2 = mk(a.w2 + (size_t)ks * (576 * DM * 2) + cg * 128, 576 * DM * 2 - cg * 128);
      const int vo = r * 8 * (DM * 2) + c * 8;
      u32x2 ra[8], rc[8];
#pragma unroll
      for (int i = 0; i < 8; ++i) ra[i] = LD64(r2, vo, i * (DM * 2), WAUX);
      const u32x2 tg = *(LAS u32x2*)(lds + L_TG);
      bool ok = true;
      const u64 start = now();
      for (;;) {
        const u32x2 f = LD64(rws, WS_HID + NB * 128 + (ks * 9 + min(lane, 8)) * HID_LD, 0, AUX_HO);
        if (__all(f[0] == tg[0] && f[1] == tg[1])) break;
        if (now() - start > SPIN_CAP) { ok = false; break; }
        __builtin_amdgcn_s_sleep(16);
      }
      asm volatile("" ::: "memory");
      {
        u32x4 t[9];
#pragma unroll
        for (int i = 0; i < 9; ++i) t[i] = LD128(rws, WS_HID + ks * 9 * HID_LD + lane * 16, i * HID_LD, AUX_HO);
#pragma unroll
        for (int i = 0; i < 9; ++i) *(LAS u32x4*)(lds + L_HB + i * 1024 + lane * 16) = t[i];
      }
      LWAIT();
      const int ho = L_HB + (c & 7) * 128 + r * 16;
      f32x4 d[4];
#pragma unroll
      for (int e = 0; e < 4; ++e) d[e] = f32x4{0.f, 0.f, 0.f, 0.f};
#pragma unroll 1
      for (int s = 0; s < ((ABL & 1024) ? 1 : 9); ++s) {
        const rsrc_t n2 = mk(a.w2 + (size_t)ks * (576 * DM * 2) + cg * 128, s < 8 ? 576 * DM * 2 - cg * 128 : 0);
        const int sn = (s < 8 ? s + 1 : 8) * 64;
        const u32x4 hb = *(LAS u32x4*)(lds + ho + s * 1024), hc = *(LAS u32x4*)(lds + ho + s * 1024 + 64);
        u32x4 f[4];
#pragma unroll
        for (int i = 0; i < 8; ++i) { USE(ra[i]); rc[i] = LD64(r2, vo, (s * 64 + 32 + i) * (DM * 2), WAUX); PIN(); }
        tr8(ra, f);
#pragma unroll
        for (int e = 0; e < 4; ++e) d[e] = mfma(f[e], hb, d[e]);
        PIN();
#pragma unroll
        for (int i = 0; i < 8; ++i) { USE(rc[i]); ra[i] = LD64(n2, vo, (sn + i) * (DM * 2), WAUX); PIN(); }
        tr8(rc, f);
#pragma unroll
        for (int e = 0; e < 4; ++e) d[e] = mfma(f[e], hc, d[e]);
        PIN();
      }
#pragma unroll
      for (int p = 0; p < 2; ++p) {
        u32x4 o;
#pragma unroll
        for (int e = 0; e < 4; ++e) o[e] = __float_as_uint((c & 8) ? d[e][2 + p] : d[e][p]);
        *(LAS u32x4*)(lds + L_MO + (c & 7) * 256 + r * 64 + (c >> 3) * 32 + p * 16) = o;
      }
      LWAIT();
      TS(15);
#pragma unroll
      for (int i = 0; i < 2; ++i) ST128(*(LAS u32x4*)(lds + L_MO + lane * 16 + i * 1024), rws, WS_MLP + task * MLP_LD + lane * 16, i * 1024, AUX_MS);
      DRAIN();
      if (lane == 0 && ok) ST64(tg, rws, WS_MLP + task * MLP_LD + NB * 256, 0, AUX_FL);
      TS(12);
    }
    BAR();
    return;
  }
  PIN();
  softmax();
  if (hh < NCONS) {
    bool ok = true;
    const u32x2 tg = *(LAS u32x2*)(lds + L_TG);
    const u32 t0 = tg[0], t1 = tg[1];
    const int col = hh * 32 + (lane & 7) * 4;
    wait_batch(PRE_AT);
    int oo = (b * DM + col + r) * 2;
    const u32 xv = __builtin_amdgcn_raw_buffer_load_b16(mk(a.x, NB * DM * 2), oo, 0, NT);
    const u64 start = now();
    for (;;) {
      const u32x2 f1 = LD64(rws, WS_ATT + DM * 4 + (b * NH + (lane & 31)) * ATT_LD, 0, AUX_HO);
      const u32x2 f2 = LD64(rws, WS_MLP + NB * 256 + ((lane & 7) * 9 + (hh >> 1)) * MLP_LD, 0, AUX_HO);
      bool all = f1[0] == t0 && f1[1] == t1 && f2[0] == t0 && f2[1] == t1;
      if (blk == 0) {
#pragma unroll
        for (int i = 0; i < 4; ++i) { const u32x2 f = LD64(rws, WS_ATT + DM * 4 + lane * ATT_LD, i * 64 * ATT_LD, AUX_HO); all &= f[0] == t0 && f[1] == t1; }
      }
      if (__all(all) || (ABL & 2)) break;
      if (now() - start > SPIN_CAP) { ok = false; break; }
      __builtin_amdgcn_s_sleep(16);
    }
    asm volatile("" ::: "memory");
    if (blk == 0 && lane == 0) ST64((u32x2{t0, t1}), rws, WS_EPOCH, 0, SC1);
    const u32x4 f = LD128(rws, WS_MLP + ((lane >> 3) * 9 + (hh >> 1)) * MLP_LD + b * 256 + (hh & 1) * 128 + (lane & 7) * 16, 0, AUX_HO);
    float g = rb(silu(rb(sum8x4(__uint_as_float(f[0]), __uint_as_float(f[1]), __uint_as_float(f[2]), __uint_as_float(f[3]))))), xe = lo16(xv);
    int ao = WS_ATT + (b * NH + (lane >> 3)) * ATT_LD + col * 4;
    oo = c < 8 ? oo : 0x40000000;
    asm volatile("" : "+v"(g), "+v"(xe), "+v"(ao), "+v"(oo));
    const u32x4 dws = {(u32)(size_t)a.ws, (u32)((size_t)a.ws >> 32) & 0xffffu, WS_END, 0x00020000u};
    const u32x4 dout = {(u32)(size_t)a.out, (u32)((size_t)a.out >> 32) & 0xffffu, NB * DM * 2, 0x00020000u};
    TS(13);
    BAR();
    if (ok) {
      const u32 start2 = (u32)now();
      asm volatile(
        "s_waitcnt vmcnt(0)\n\t"
        "buffer_load_dwordx4 v[64:67], %[ao], %[ws], 0 offen sc1\n\t"
        "buffer_load_dwordx4 v[68:71], %[ao], %[ws], %[o1] offen sc1\n\t"
        "buffer_load_dwordx4 v[72:75], %[ao], %[ws], %[o2] offen sc1\n\t"
        "buffer_load_dwordx4 v[76:79], %[ao], %[ws], %[o3] offen sc1\n\t"
        "s_sleep " STR(STAG) "\nLsw%=:\n\t"
        "buffer_load_dwordx4 v[80:83], %[ao], %[ws], 0 offen sc1\n\t"
        "buffer_load_dwordx4 v[84:87], %[ao], %[ws], %[o1] offen sc1\n\t"
        "buffer_load_dwordx4 v[88:91], %[ao], %[ws], %[o2] offen sc1\n\t"
        "buffer_load_dwordx4 v[92:95], %[ao], %[ws], %[o3] offen sc1\n\t"
        "s_waitcnt vmcnt(4)\n\t"
        "v_max3_u32 v96, v64, v65, v66\n\tv_max3_u32 v97, v67, v68, v69\n\tv_max3_u32 v98, v70, v71, v72\n\tv_max3_u32 v99, v73, v74, v75\n\t"
        "v_max3_u32 v96, v96, v76, v77\n\tv_max3_u32 v97, v97, v78, v79\n\tv_max3_u32 v96, v96, v97, v98\n\tv_max_u32 v96, v96, v99\n\t"
        "v_cmp_eq_u32 vcc, -1, v96\n\ts_nop 1\n\ts_cbranch_vccz LswA%=\n\t"
        "buffer_load_dwordx4 v[64:67], %[ao], %[ws], 0 offen sc1\n\t"
        "buffer_load_dwordx4 v[68:71], %[ao], %[ws], %[o1] offen sc1\n\t"
        "buffer_load_dwordx4 v[72:75], %[ao], %[ws], %[o2] offen sc1\n\t"
        "buffer_load_dwordx4 v[76:79], %[ao], %[ws], %[o3] offen sc1\n\t"
        "s_waitcnt vmcnt(4)\n\t"
        "v_max3_u32 v96, v80, v81, v82\n\tv_max3_u32 v97, v83, v84, v85\n\tv_max3_u32 v98, v86, v87, v88\n\tv_max3_u32 v99, v89, v90, v91\n\t"
        "v_max3_u32 v96, v96, v92, v93\n\tv_max3_u32 v97, v97, v94, v95\n\tv_max3_u32 v96, v96, v97, v98\n\tv_max_u32 v96, v96, v99\n\t"
        "v_cmp_eq_u32 vcc, -1, v96\n\ts_nop 1\n\ts_cbranch_vccz LswB%=\n\t"
        "s_memrealtime vcc\n\ts_waitcnt lgkmcnt(0)\n\ts_sub_u32 vcc_lo, vcc_lo, %[st]\n\ts_cmp_lt_u32 vcc_lo, %[cap]\n\ts_cbranch_scc1 Lsw%=\n\t"
        "v_mov_b32 v96, 0x7fc0\n\ts_branch Lsws%=\n"
        "LswB%=:\n\t"
        "v_add_f32 v96, v80, v84\n\tv_add_f32 v88, v88, v92\n\t"
        "v_add_f32 v97, v81, v85\n\tv_add_f32 v89, v89, v93\n\t"
        "v_add_f32 v98, v82, v86\n\tv_add_f32 v90, v90, v94\n\t"
        "v_add_f32 v99, v83, v87\n\tv_add_f32 v91, v91, v95\n\t"
        "v_add_f32 v96, v96, v88\n\t" "v_add_f32 v97, v97, v89\n\t"
        "v_add_f32 v98, v98, v90\n\t" "v_add_f32 v99, v99, v91\n\t"
        "s_branch Lswf%=\n"
        "LswA%=:\n\t"
        "v_add_f32 v96, v64, v68\n\tv_add_f32 v72, v72, v76\n\t"
        "v_add_f32 v97, v65, v69\n\tv_add_f32 v73, v73, v77\n\t"
        "v_add_f32 v98, v66, v70\n\tv_add_f32 v74, v74, v78\n\t"
        "v_add_f32 v99, v67, v71\n\tv_add_f32 v75, v75, v79\n\t"
        "v_add_f32 v96, v96, v72\n\t" "v_add_f32 v97, v97, v73\n\t"
        "v_add_f32 v98, v98, v74\n\t" "v_add_f32 v99, v99, v75\n\t"
        "\nLswf%=:\n\ts_nop 1\n\t"
        "v_permlane32_swap_b32 v96, v98\n\tv_permlane32_swap_b32 v97, v99\n\ts_nop 1\n\tv_add_f32 v96, v96, v98\n\tv_add_f32 v97, v97, v99\n\ts_nop 1\n\t"
        "v_permlane16_swap_b32 v96, v97\n\ts_nop 1\n\tv_add_f32 v96, v96, v97\n\ts_nop 1\n\t"
        "v_add_f32_dpp v96, v96, v96 row_ror:8 row_mask:0xf bank_mask:0xf\n\t"
        "v_cvt_pk_bf16_f32 v96, v96, v96\n\tv_lshlrev_b32 v96, 16, v96\n\tv_mul_f32 v96, v96, %[g]\n\t"
        "v_cvt_pk_bf16_f32 v96, v96, v96\n\tv_lshlrev_b32 v96, 16, v96\n\tv_add_f32 v96, %[x], v96\n\tv_cvt_pk_bf16_f32 v96, v96, v96\n"
        "Lsws%=:\n\tbuffer_store_short v96, %[oo], %[out], 0 offen\n\ts_waitcnt vmcnt(0)"
        :
        : [ao] "v"(ao), [ws] "s"(dws), [o1] "s"(8 * ATT_LD), [o2] "s"(16 * ATT_LD), [o3] "s"(24 * ATT_LD), [st] "s"(start2), [cap] "s"((u32)SPIN_CAP),
          [g] "v"(g), [x] "v"(xe), [oo] "v"(oo), [out] "s"(dout)
        : "memory", "vcc", "scc", "v64", "v65", "v66", "v67", "v68", "v69", "v70", "v71", "v72", "v73", "v74", "v75", "v76", "v77", "v78", "v79", "v80", "v81", "v82", "v83", "v84", "v85", "v86", "v87", "v88", "v89", "v90", "v91", "v92", "v93", "v94", "v95", "v96", "v97", "v98", "v99");
    } else {
      __builtin_amdgcn_raw_buffer_store_b16((u16)0x7fc0, mk(a.out, NB * DM * 2), oo, 0, 0);
    }
    TS(14);
  } else {
    BAR();
  }
}

}

extern "C" void kernel_launch(void* const* d_in, const int* in_sizes, int n_in, void* d_out, int out_size, void* d_ws, size_t ws_size,
                              hipStream_t stream) {
  Args a;
  a.x = (const char*)d_in[0]; a.k = (const char*)d_in[1]; a.v = (const char*)d_in[2]; a.wq = (const char*)d_in[3]; a.wo = (const char*)d_in[4];
  a.w1 = (const char*)d_in[6]; a.w3 = (const char*)d_in[7]; a.w2 = (const char*)d_in[8];
  a.out = (char*)d_out; a.ws = (char*)d_ws;
  hipLaunchKernelGGL(fused_decode, dim3(NB * NH), dim3(1024), 0, stream, a);
}
```

```cpp
#include <hip/hip_runtime.h>
#include <hip/hip_bf16.h>
#include <cstdint>

namespace {

typedef unsigned u32;
typedef unsigned long long u64;
typedef unsigned short u16;
typedef u32 u32x4 __attribute__((ext_vector_type(4)));
typedef u32 u32x2 __attribute__((ext_vector_type(2)));
typedef float f32x4 __attribute__((ext_vector_type(4)));
typedef __bf16 bf16x8 __attribute__((ext_vector_type(8)));
typedef __bf16 bf16x2 __attribute__((ext_vector_type(2)));
#define LAS __attribute__((address_space(3)))

constexpr int NB = 8, DM = 576, NH = 32, SK = 4096, DH = 128, FF = 4608;
constexpr int NTASK = FF / 64;
constexpr int NCONS = DM / 32;
constexpr int KEEP = 0, NT = 2, SC1 = 16, SC1NT = 18;

constexpr int WIN0 = 41, WIN_MLP = 2;
constexpr int WMID = 45, WLO = 36, WHI = 56, GAIN = 20;
constexpr int OPENW = 10;
constexpr u32 PACE_K = 0x19110903u, PACE_V = 0x39332b23u;
constexpr int PACE_DLY = 3;
constexpr int PRE_AT = 58;
constexpr int MLP_GAP = 6;
constexpr u64 SPIN_CAP = 5000000;

constexpr u32 ATT_LD = DM * 4 + 128, HID_LD = NB * 128 + 128, MLP_LD = NB * 256 + 128, PACE_LD = 4352;
constexpr u32 WS_EPOCH = 0, WS_ATT = 4096, WS_HID = WS_ATT + NB * NH * ATT_LD, WS_MLP = WS_HID + NTASK * HID_LD, WS_PACE = WS_MLP + NTASK * MLP_LD,
              WS_END = WS_PACE + 8 * 64 * PACE_LD;
constexpr u32 EMPTY = 0xffffffffu;
constexpr int NOWHERE = 0x40000000;

constexpr int XN_LD = 1168;
constexpr int L_XN = 0, L_QP = L_XN + NB * XN_LD, L_QB = L_QP + 12 * DH * 4, L_PS = L_QB + DH * 2, L_WM = L_PS + SK * 2, L_WS = L_WM + 64,
              L_OP = L_WS + 64, L_OB = L_OP + 4 * DH * 4, L_TG = L_OB + 9 * DH * 2, L_PG = L_TG + 8, L_TK = L_TG + 12, L_WN = L_TG + 16,
              L_XK = L_TG + 24, L_QC = L_TG + 28, L_QK = L_TG + 32, L_XB = L_TG + 48, L_MO = L_XB + DM * 2, L_HB = L_MO + NB * 256,
              L_END = L_HB + 9 * 1024;

struct Args { const char* x; const char* k; const char* v; const char* wq; const char* wo; const char* w1; const char* w3; const char* w2; char* out; char* ws; };

typedef __amdgpu_buffer_rsrc_t rsrc_t;
__device__ __forceinline__ rsrc_t mk(const char* p, u32 n) { return __builtin_amdgcn_make_buffer_rsrc((void*)p, 0, n, 0x00020000); }
#define LD128(r, vo, so, aux) __builtin_amdgcn_raw_buffer_load_b128(r, vo, so, aux)
#define LD64(r, vo, so, aux) __builtin_amdgcn_raw_buffer_load_b64(r, vo, so, aux)
#define LD32(r, vo, so, aux) __builtin_amdgcn_raw_buffer_load_b32(r, vo, so, aux)
#define ST128(v, r, vo, so, aux) __builtin_amdgcn_raw_buffer_store_b128(v, r, vo, so, aux)
#define ST64(v, r, vo, so, aux) __builtin_amdgcn_raw_buffer_store_b64(v, r, vo, so, aux)
#define PIN() __builtin_amdgcn_sched_barrier(0)
#define USE(x) asm volatile("" ::"v"(x) : "memory")
#define BAR() asm volatile("s_waitcnt lgkmcnt(0)\n\ts_barrier" ::: "memory")
#define RAWBAR() asm volatile("s_barrier" ::: "memory")
#define LWAIT() asm volatile("s_waitcnt lgkmcnt(0)" ::: "memory")
#define DRAIN() asm volatile("s_waitcnt vmcnt(0)" ::: "memory")
#define TOUCH(d, p) asm volatile("s_load_dword %0, %1, 0x0" : "=&s"(d) : "s"(p) : "memory")
#define LGET(off) __hip_atomic_load((LAS u32*)(lds + (off)), __ATOMIC_RELAXED, __HIP_MEMORY_SCOPE_WORKGROUP)
#define LPUT(off, v) __hip_atomic_store((LAS u32*)(lds + (off)), (u32)(v), __ATOMIC_RELAXED, __HIP_MEMORY_SCOPE_WORKGROUP)
#define LADD(off) __hip_atomic_fetch_add((LAS u32*)(lds + (off)), 1u, __ATOMIC_RELAXED, __HIP_MEMORY_SCOPE_WORKGROUP)
#define LSPIN(off, n) do { while (LGET(off) < (n)) __builtin_amdgcn_s_sleep(1); asm volatile("" ::: "memory"); } while (0)
#define DITH(i) (((i) & 1) * 4 + ((i) & 2) + ((i) >> 2))
#define GATE(i) asm volatile("s_cmp_gt_i32 %0, %1\n\ts_cbranch_scc1 Lg7_%=\n\ts_cmp_gt_i32 %0, %2\n\ts_cbranch_scc1 Lg5_%=\n\t" \
                             "s_cmp_gt_i32 %0, %3\n\ts_cbranch_scc1 Lg4_%=\n\ts_waitcnt vmcnt(3)\nLg4_%=:\n\ts_waitcnt vmcnt(4)\n" \
                             "Lg5_%=:\n\ts_waitcnt vmcnt(5)\nLg7_%=:" \
                             ::"s"(win), "i"(48 + DITH(i)), "i"(40 + DITH(i)), "i"(32 + DITH(i)) : "scc", "memory")
#define EIGHT(STEP) STEP(0) STEP(1) STEP(2) STEP(3) STEP(4) STEP(5) STEP(6) STEP(7)

__device__ __forceinline__ u32 pk(float lo, float hi) { bf16x2 t = {(__bf16)lo, (__bf16)hi}; return __builtin_bit_cast(u32, t); }
__device__ __forceinline__ float rb(float x) { return __uint_as_float(pk(x, x) << 16); }
__device__ __forceinline__ float lo16(u32 v) { return __uint_as_float(v << 16); }
__device__ __forceinline__ float hi16(u32 v) { return __uint_as_float(v & 0xffff0000u); }
__device__ __forceinline__ float dot2(u32 a, u32 b, float c) {
  return __builtin_amdgcn_fdot2_f32_bf16(__builtin_bit_cast(bf16x2, a), __builtin_bit_cast(bf16x2, b), c, false);
}
__device__ __forceinline__ f32x4 mfma(u32x4 a, u32x4 b, f32x4 c) {
  return __builtin_amdgcn_mfma_f32_16x16x32_bf16(__builtin_bit_cast(bf16x8, a), __builtin_bit_cast(bf16x8, b), c, 0, 0, 0);
}
template <int CTRL> __device__ __forceinline__ float dpp(float x) {
  return __builtin_bit_cast(float, __builtin_amdgcn_mov_dpp(__builtin_bit_cast(int, x), CTRL, 0xf, 0xf, true));
}
constexpr int X1 = 0xB1, X2 = 0x4E, HMIR = 0x141, MIR = 0x140, ROR8 = 0x128;
__device__ __forceinline__ float sum16(float x) { x += dpp<X1>(x); x += dpp<X2>(x); x += dpp<HMIR>(x); x += dpp<MIR>(x); return x; }
__device__ __forceinline__ float max16(float x) { x = fmaxf(x, dpp<X1>(x)); x = fmaxf(x, dpp<X2>(x)); x = fmaxf(x, dpp<HMIR>(x)); x = fmaxf(x, dpp<MIR>(x)); return x; }
__device__ __forceinline__ float sumrows(float x) {
  auto s = __builtin_amdgcn_permlane16_swap(__float_as_uint(x), __float_as_uint(x), false, false);
  x = __uint_as_float(s[0]) + __uint_as_float(s[1]);
  auto t = __builtin_amdgcn_permlane32_swap(__float_as_uint(x), __float_as_uint(x), false, false);
  return __uint_as_float(t[0]) + __uint_as_float(t[1]);
}
__device__ __forceinline__ float maxrows(float x) {
  auto s = __builtin_amdgcn_permlane16_swap(__float_as_uint(x), __float_as_uint(x), false, false);
  x = fmaxf(__uint_as_float(s[0]), __uint_as_float(s[1]));
  auto t = __builtin_amdgcn_permlane32_swap(__float_as_uint(x), __float_as_uint(x), false, false);
  return fmaxf(__uint_as_float(t[0]), __uint_as_float(t[1]));
}
__device__ __forceinline__ float sum8x4(float v0, float v1, float v2, float v3) {
  auto p = __builtin_amdgcn_permlane32_swap(__float_as_uint(v0), __float_as_uint(v2), false, false);
  auto q = __builtin_amdgcn_permlane32_swap(__float_as_uint(v1), __float_as_uint(v3), false, false);
  auto t = __builtin_amdgcn_permlane16_swap(__float_as_uint(__uint_as_float(p[0]) + __uint_as_float(p[1])), __float_as_uint(__uint_as_float(q[0]) + __uint_as_float(q[1])), false, false);
  const float x = __uint_as_float(t[0]) + __uint_as_float(t[1]);
  return x + dpp<ROR8>(x);
}
__device__ __forceinline__ float fdiv(float a, float d, float rd) { const float q = a * rd; return fmaf(fmaf(-q, d, a), rd, q); }
__device__ __forceinline__ float silu(float x) { const float d = 1.f + __expf(-x); return d < INFINITY ? fdiv(x, d, __builtin_amdgcn_rcpf(d)) : x * 0.f; }
__device__ __forceinline__ u64 now() { return __builtin_amdgcn_s_memrealtime(); }

struct Diag { u32 s[2][4]; };
__device__ __forceinline__ Diag make_diag(int c) {
  Diag d;
#pragma unroll
  for (int par = 0; par < 2; ++par)
#pragma unroll
    for (int w = 0; w < 4; ++w) {
      const u32 b = par ? 0x0302u : 0x0100u;
      d.s[par][w] = (c >> 1) == w ? ((c & 1) ? (b << 16) | 0x0c0cu : 0x0c0c0000u | b) : 0x0c0c0c0cu;
    }
  return d;
}
template <int PAR> __device__ __forceinline__ f32x4 axpy4(u32 val, u32x4 rows, const Diag& d, f32x4 acc) {
  u32x4 af;
#pragma unroll
  for (int w = 0; w < 4; ++w) af[w] = __builtin_amdgcn_perm(val, val, d.s[PAR][w]);
  return mfma(af, rows, acc);
}
__device__ __forceinline__ float score4(u32x4 k, u32x4 q) {
  float d = dot2(k[0], q[0], 0.f); d = dot2(k[1], q[1], d); d = dot2(k[2], q[2], d); d = dot2(k[3], q[3], d);
  return sum16(d);
}
__device__ __forceinline__ void tr8(const u32x2 (&raw)[8], u32x4 (&f)[4]) {
#pragma unroll
  for (int e = 0; e < 4; ++e)
#pragma unroll
    for (int d = 0; d < 4; ++d) f[e][d] = __builtin_amdgcn_perm(raw[2 * d + 1][e >> 1], raw[2 * d][e >> 1], (e & 1) ? 0x07060302u : 0x05040100u);
}

__global__ void __launch_bounds__(1024, 4) fused_decode(Args a) {
  __shared__ __attribute__((aligned(16))) char lds_[L_END];
  LAS char* lds = (LAS char*)lds_;
  if (threadIdx.x == 640) { LPUT(L_PG, 0); LPUT(L_TK, 0); LPUT(L_WN, 0); LPUT(L_WN + 4, 0); LPUT(L_XK, 0); LPUT(L_QC, 0); LPUT(L_QK, 0); LWAIT(); }
  PIN(); RAWBAR(); PIN();
  asm volatile("" ::"s"(a.x), "s"(a.ws));
  const int tid = threadIdx.x, lane = tid & 63, w = __builtin_amdgcn_readfirstlane(tid >> 6), r = lane >> 4, c = lane & 15;
  const int blk = blockIdx.x, b = blk >> 5, h = blk & 31;
  const int hrot = (h - b) & 31, job = hrot / 9, task = (h & 7) * 9 + (hrot % 9 < 8 ? b : 8);
  const char* kbase = a.k + ((size_t)blk << 20);
  const char* vbase = a.v + ((size_t)blk << 20);
  const rsrc_t rk = mk(kbase, 1u << 20);
  const int sw = w & 3;
  const int vA = lane * 16 + sw * 4096, vB = vA + 16384;
#define KVLD(rs, beta, i) LD128(rs, ((i) < 4 ? vA : vB) + ((i) & 3) * 1024, (beta) * 32768, NT)
  float mx = -INFINITY;
  auto put_scores = [&](float sc, int batch) {
    const float s = rb(rb(sc) * 0.08838834764831845f);
    mx = fmaxf(mx, s);
    if (c < 8) *(LAS u16*)(lds + L_PS + batch * 64 + r * 16 + c * 2) = (u16)(__float_as_uint(s) >> 16);
  };
  auto softmax = [&]() {
    const float wm = maxrows(max16(mx));
    if (lane == 0) *(LAS float*)(lds + L_WM + w * 4) = wm;
    BAR();
    const float M = max16(*(LAS float*)(lds + L_WM + c * 4));
    const u32x2 sv = *(LAS u32x2*)(lds + L_PS + tid * 8);
    const float e0 = __expf(lo16(sv[0]) - M), e1 = __expf(hi16(sv[0]) - M), e2 = __expf(lo16(sv[1]) - M), e3 = __expf(hi16(sv[1]) - M);
    const float ws = sumrows(sum16((e0 + e1) + (e2 + e3)));
    if (lane == 0) *(LAS float*)(lds + L_WS + w * 4) = ws;
    BAR();
    const float inv = 1.f / sum16(*(LAS float*)(lds + L_WS + c * 4));
    *(LAS u32x2*)(lds + L_PS + tid * 8) = u32x2{pk(e0 * inv, e1 * inv), pk(e2 * inv, e3 * inv)};
    BAR();
  };

  if (w >= 12) {
    u32x4 ring[8];
    u32x4 ext[8];
    __builtin_amdgcn_s_sleep(32);
#pragma unroll
    for (int i = 0; i < 16; ++i) {
      if (i >= OPENW) USE((i - OPENW < 8 ? ring : ext)[(i - OPENW) & 7]);
      if (i < 8) ring[i] = KVLD(rk, 0, i); else ext[i - 8] = KVLD(rk, 1, i - 8);
      PIN();
    }
    __builtin_amdgcn_s_setprio(3);
    LSPIN(L_QK, 2);
    const u32x4 q = *(LAS u32x4*)(lds + L_QB + c * 16);
    float sc = 0.f;
    const int win0 = WIN0 + (job == 0 ? WIN_MLP : 0);
    int win = win0;
#define PACE_IN() const u32 tj = (u32)now(); const u64 sch = __hip_atomic_load((LAS u64*)(lds + L_WN), __ATOMIC_RELAXED, __HIP_MEMORY_SCOPE_WORKGROUP)
#define PACE_OUT(j) do { const u32 due_ = __builtin_amdgcn_readfirstlane((u32)sch), tau_ = __builtin_amdgcn_readfirstlane((u32)(sch >> 32)); \
      const int late_ = min(max((int)(tj * 256u - due_ - tau_ * (u32)(j)) >> 8, -4096), 4096); \
      win = tau_ ? min(max(WMID + ((late_ * GAIN) >> 8), WLO), WHI) : win0; } while (0)
#pragma unroll
    for (int i = 0; i < 8; ++i) { const float d = score4(ring[i], q); sc = (c & 7) == i ? d : sc; PIN(); }
    put_scores(sc, sw);
#define FSTEP(i) { if (i > 0) { GATE(i); ring[(i + 7) & 7] = KVLD(rk, 2, (i + 7) & 7); } PIN(); const float d = score4(ext[i], q); sc = (c & 7) == i ? d : sc; PIN(); }
    EIGHT(FSTEP)
    put_scores(sc, 4 + sw);
#pragma unroll 1
    for (int beta = 2; beta < 32; ++beta) {
      const rsrc_t rn = mk(beta < 31 ? kbase : vbase, 1u << 20);
      const int bn = beta < 31 ? beta + 1 : 0;
      if (beta == 31) win = 64;
      PACE_IN();
      if (tid == 768) LPUT(L_PG, beta);
#define KSTEP(i) { GATE(i); if (i == 0) ring[7] = KVLD(rk, beta, 7); else ring[(i + 7) & 7] = KVLD(rn, bn, (i + 7) & 7); PIN(); \
        const float d = score4(ring[i], q); sc = (c & 7) == i ? d : sc; PIN(); }
      EIGHT(KSTEP)
      put_scores(sc, beta * 4 + sw);
      PACE_OUT(beta);
    }
    const rsrc_t rv = mk(vbase, 1u << 20);
    ring[7] = KVLD(rv, 0, 7);
#pragma unroll
    for (int i = 0; i < 8; ++i) { ext[i] = KVLD(rv, 1, i); PIN(); }
    softmax();
    const Diag dg = make_diag(c);
    f32x4 acc = {0.f, 0.f, 0.f, 0.f};
    const int po = L_PS + sw * 64 + r * 16;
    u32x4 pn = *(LAS u32x4*)(lds + po + 512);
    {
      const u32x4 p0 = *(LAS u32x4*)(lds + po), p1 = *(LAS u32x4*)(lds + po + 256);
#pragma unroll
      for (int i = 0; i < 8; ++i) { acc = (i & 1) ? axpy4<1>(p0[i >> 1], ring[i], dg, acc) : axpy4<0>(p0[i >> 1], ring[i], dg, acc); PIN(); }
#define ESTEP(i) { if (i > 0) { GATE(i); ring[(i + 7) & 7] = KVLD(rv, 2, (i + 7) & 7); } PIN(); acc = axpy4<i & 1>(p1[i >> 1], ext[i], dg, acc); PIN(); }
      EIGHT(ESTEP)
    }
#pragma unroll 1
    for (int beta = 2; beta < 32; ++beta) {
      const rsrc_t rn = mk(vbase, beta < 31 ? 1u << 20 : 0u);
      const int bn = beta < 31 ? beta + 1 : 0;
      if (beta == 31) win = 64;
      const u32x4 pv = pn;
      pn = *(LAS u32x4*)(lds + po + bn * 256);
      PACE_IN();
      if (tid == 768) LPUT(L_PG, 32 + beta);
#define VSTEP(i) { GATE(i); if (i == 0) ring[7] = KVLD(rv, beta, 7); else ring[(i + 7) & 7] = KVLD(rn, bn, (i + 7) & 7); PIN(); \
        acc = axpy4<i & 1>(pv[i >> 1], ring[i], dg, acc); PIN(); }
      EIGHT(VSTEP)
      PACE_OUT(32 + beta);
    }
    if (r < 2) *(LAS f32x4*)(lds + L_OP + sw * 512 + c * 32 + r * 16) = acc;
    BAR();
    return;
  }

  const rsrc_t rws = mk(a.ws, WS_END);
  auto wait_batch = [&](u32 n) { while (LGET(L_PG) < n) __builtin_amdgcn_s_sleep(2); };
  if (w == 11) {
    u32 t0, t1, t2, t3, t4, t5, t6, t7;
    asm volatile("" ::"s"(a.ws), "s"(a.out), "s"(kbase), "s"(vbase), "s"(a.w2));
    TOUCH(t0, kbase); TOUCH(t1, a.ws);
    __builtin_amdgcn_s_sleep(32);
    TOUCH(t2, a.wo + (size_t)h * (DH * DM * 2)); TOUCH(t3, a.w1); TOUCH(t4, a.w3); TOUCH(t5, vbase); TOUCH(t6, a.w2); TOUCH(t7, a.out);
    const u32x2 ep = LD64(rws, WS_EPOCH, 0, SC1);
    if (lane < 48) {
#pragma unroll
      for (int i = 0; i < 3; ++i) ST128((u32x4{EMPTY, EMPTY, EMPTY, EMPTY}), rws, WS_ATT + blk * ATT_LD + lane * 16 + i * 768, 0, SC1);
    }
    asm volatile("s_waitcnt lgkmcnt(0)" : "+s"(t0), "+s"(t1), "+s"(t2), "+s"(t3), "+s"(t4), "+s"(t5), "+s"(t6), "+s"(t7)::"memory");
    const u64 tag = (((u64)(u32)__builtin_amdgcn_readfirstlane(ep[1]) << 32) | (u32)__builtin_amdgcn_readfirstlane(ep[0])) + 1;
    DRAIN();
    if (lane == 0) {
      *(LAS u64*)(lds + L_TG) = tag;
      LWAIT();
      LPUT(L_TK, 1);
      ST64((u32x2{(u32)tag, (u32)(tag >> 32)}), rws, WS_ATT + blk * ATT_LD + DM * 4, 0, SC1);
    }
    const u32 tb = (u32)now();
    const bool sample = ((blk >> 3) & 3) == (blk & 3);
    float mprev = 0.f;
    int atprev = 0;
    auto pace = [&](int k, int at) {
      wait_batch(at);
      const u32 tk = (u32)now();
      if (sample && lane < 8) ST128((u32x4{tk, (u32)tag, tk, (u32)tag}), rws, WS_PACE + (k * 64 + b * 8 + (blk & 7)) * PACE_LD + lane * 16, 0, SC1NT);
      wait_batch(at + PACE_DLY);
      const u32x2 v = LD64(rws, WS_PACE + (k * 64 + ((b + lane + blk) & 7) * 8 + (lane & 7)) * PACE_LD, 0, SC1);
      const bool in = v[1] == (u32)tag && lane < 8;
      const float n = (float)__popcll(__ballot(in));
      const float m = (float)(int)(tk - tb) + sum16(in ? (float)(int)(v[0] - tk) : 0.f) * __builtin_amdgcn_rcpf(fmaxf(n, 1.f));
      if (k > 0 && lane == 0) {
        const float tau = (m - mprev) * __builtin_amdgcn_rcpf((float)(at - atprev)) * 0.99f;
        const u32 tau256 = (u32)fminf(fmaxf(tau * 256.f, 1.f), 1e6f);
        __hip_atomic_store((LAS u64*)(lds + L_WN), ((u64)tau256 << 32) | (tb * 256u + (u32)(int)(m * 256.f) - tau256 * (u32)at), __ATOMIC_RELAXED, __HIP_MEMORY_SCOPE_WORKGROUP);
      }
      mprev = m; atprev = at;
    };
#pragma unroll 1
    for (int k = 0; k < 4; ++k) pace(k, (PACE_K >> (8 * k)) & 255);
    softmax();
#pragma unroll 1
    for (int k = 0; k < 4; ++k) pace(4 + k, (PACE_V >> (8 * k)) & 255);
    BAR();
    return;
  }
  if (w < 9) {
    __builtin_amdgcn_s_setprio(1);
    const int wr = (w + b) % 9;
    u32x4 wb[16];
    {
      const rsrc_t rq = mk(a.wq + h * 256, DM * 8192 - h * 256);
      const int vo = (64 * wr + 16 * r) * 8192 + c * 16;
#pragma unroll
      for (int i = 0; i < 16; ++i) wb[i] = LD128(rq, vo, i * 8192, KEEP);
    }
    PIN();
    const Diag dgq = make_diag(c);
    LSPIN(L_XK, 1);
    const u32x4 x0 = *(LAS u32x4*)(lds + L_XB + (64 * wr + 16 * r) * 2), x1 = *(LAS u32x4*)(lds + L_XB + (64 * wr + 16 * r) * 2 + 16);
    PIN();
    {
      f32x4 acc0 = {0.f, 0.f, 0.f, 0.f}, acc1 = acc0;
#pragma unroll
      for (int i = 0; i < 8; ++i) {
        const u32 xv = i < 4 ? x0[i & 3] : x1[i & 3];
        acc0 = axpy4<0>(xv, wb[2 * i], dgq, acc0); acc1 = axpy4<1>(xv, wb[2 * i + 1], dgq, acc1);
      }
      if (r < 2) *(LAS f32x4*)(lds + L_QP + wr * 512 + c * 32 + r * 16) = acc0 + acc1;
    }
    LWAIT();
    if (lane == 0) LADD(L_QC);
    if (w < 2) {
      LSPIN(L_QC, 9);
      float s = 0.f;
#pragma unroll
      for (int j = 0; j < 9; ++j) s += *(LAS float*)(lds + L_QP + j * 512 + tid * 4);
      *(LAS u16*)(lds + L_QB + tid * 2) = (u16)pk(s, s);
      LWAIT();
      if (lane == 0) LADD(L_QK);
    }
    __builtin_amdgcn_s_setprio(0);
    LSPIN(L_QK, 2);
  } else {
    if (w == 10) {
      __builtin_amdgcn_s_setprio(3);
      const rsrc_t rx = mk(a.x + b * (DM * 2), DM * 2);
      u32 xr[5];
#pragma unroll
      for (int i = 0; i < 5; ++i) xr[i] = LD32(rx, lane * 4 + i * 256, 0, KEEP);
      float s = 0.f;
#pragma unroll
      for (int i = 0; i < 5; ++i) s += lo16(xr[i]) + hi16(xr[i]);
      const float mean = sumrows(sum16(s)) * (1.f / DM);
      float ss = 0.f;
#pragma unroll
      for (int i = 0; i < 5; ++i) {
        const float d0 = lo16(xr[i]) - mean, d1 = hi16(xr[i]) - mean;
        ss += (i < 4 || lane < 32) ? d0 * d0 + d1 * d1 : 0.f;
      }
      const float var = sumrows(sum16(ss)) * (1.f / DM);
      const float m = rb(mean), den = rb(__builtin_amdgcn_sqrtf(rb(rb(var) + 1e-5f))), rd = __builtin_amdgcn_rcpf(den);
#pragma unroll
      for (int i = 0; i < 5; ++i)
        if (i < 4 || lane < 32) *(LAS u32*)(lds + L_XB + lane * 4 + i * 256) = pk(fdiv(rb(lo16(xr[i]) - m), den, rd), fdiv(rb(hi16(xr[i]) - m), den, rd));
      LWAIT();
      if (lane == 0) LPUT(L_XK, 1);
      __builtin_amdgcn_s_setprio(0);
    }
    LSPIN(L_QK, 2);
  }
  PIN();

  if (w < 9) {
    u32x4 wf[4][4];
    const rsrc_t ro = mk(a.wo + (size_t)h * (DH * DM * 2), DH * DM * 2);
    const int vo = r * 8 * (DM * 2) + w * 128 + c * 8;
    u32x2 raw[4][8];
#pragma unroll
    for (int n = 0; n < 32; ++n) {
      raw[n >> 3][n & 7] = LD64(ro, vo, ((n >> 3) * 32 + (n & 7)) * (DM * 2), KEEP);
      if (n >= 1) USE(raw[(n - 1) >> 3][(n - 1) & 7]);
      if (n >= 8 && (n & 7) == 0) tr8(raw[(n - 1) >> 3], wf[(n - 1) >> 3]);
      PIN();
    }
    tr8(raw[3], wf[3]);
    int so = lane < 16 ? WS_ATT + blk * ATT_LD + w * 256 + c * 16 : NOWHERE, lo = L_OP + lane * 8, lb = L_OB + w * 256 + lane * 4, lr = L_OB + w * 256 + r * 16;
#pragma unroll
    for (int s = 0; s < 4; ++s) asm volatile("" : "+v"(wf[s][0]), "+v"(wf[s][1]), "+v"(wf[s][2]), "+v"(wf[s][3]));
    asm volatile("" : "+v"(so), "+v"(lo), "+v"(lb), "+v"(lr));
    PIN();
    softmax();
    BAR();
    float o0, o1;
    {
      const u32x2 t0 = *(LAS u32x2*)(lds + lo), t1 = *(LAS u32x2*)(lds + lo + 512), t2 = *(LAS u32x2*)(lds + lo + 1024), t3 = *(LAS u32x2*)(lds + lo + 1536);
      o0 = (__uint_as_float(t0[0]) + __uint_as_float(t1[0])) + (__uint_as_float(t2[0]) + __uint_as_float(t3[0]));
      o1 = (__uint_as_float(t0[1]) + __uint_as_float(t1[1])) + (__uint_as_float(t2[1]) + __uint_as_float(t3[1]));
    }
    *(LAS u32*)(lds + lb) = pk(o0, o1);
    LWAIT();
    u32x4 ob[4];
#pragma unroll
    for (int s = 0; s < 4; ++s) ob[s] = *(LAS u32x4*)(lds + lr + s * 64);
    PIN();
    f32x4 d[4];
#pragma unroll
    for (int e = 0; e < 4; ++e) d[e] = f32x4{0.f, 0.f, 0.f, 0.f};
#pragma unroll
    for (int s = 0; s < 4; ++s)
#pragma unroll
      for (int e = 0; e < 4; ++e) d[e] = mfma(ob[s], wf[s][e], d[e]);
    PIN();
    ST128((u32x4{min(__float_as_uint(d[0][0]), EMPTY - 1), min(__float_as_uint(d[1][0]), EMPTY - 1), min(__float_as_uint(d[2][0]), EMPTY - 1),
                 min(__float_as_uint(d[3][0]), EMPTY - 1)}), rws, so, 0, SC1);
    return;
  }
  if (w == 9) {
    if (job == 0) {
      {
        const rsrc_t rx = mk(a.x, NB * DM * 2);
        u32x4 xa[9];
#pragma unroll
        for (int i = 0; i < 9; ++i) xa[i] = LD128(rx, (lane >> 3) * (DM * 2) + (lane & 7) * 16, i * 128, NT);
        float s = 0.f;
#pragma unroll
        for (int i = 0; i < 9; ++i)
#pragma unroll
          for (int j = 0; j < 4; ++j) s += lo16(xa[i][j]) + hi16(xa[i][j]);
        s += dpp<X1>(s); s += dpp<X2>(s); s += dpp<HMIR>(s);
        const float mean = s * (1.f / DM);
        float ss = 0.f;
#pragma unroll
        for (int i = 0; i < 9; ++i)
#pragma unroll
          for (int j = 0; j < 4; ++j) { const float d0 = lo16(xa[i][j]) - mean, d1 = hi16(xa[i][j]) - mean; ss += d0 * d0 + d1 * d1; }
        ss += dpp<X1>(ss); ss += dpp<X2>(ss); ss += dpp<HMIR>(ss);
        const float m = rb(mean), den = rb(__builtin_amdgcn_sqrtf(rb(rb(ss * (1.f / DM)) + 1e-5f))), rd = __builtin_amdgcn_rcpf(den);
#pragma unroll
        for (int i = 0; i < 9; ++i) {
          u32x4 o;
#pragma unroll
          for (int j = 0; j < 4; ++j) o[j] = pk(fdiv(rb(lo16(xa[i][j]) - m), den, rd), fdiv(rb(hi16(xa[i][j]) - m), den, rd));
          *(LAS u32x4*)(lds + L_XN + (lane >> 3) * XN_LD + (lane & 7) * 16 + i * 128) = o;
        }
        LWAIT();
        PIN();
      }
      const rsrc_t r1 = mk(a.w1 + task * 128, DM * FF * 2 - task * 128), r3 = mk(a.w3 + task * 128, DM * FF * 2 - task * 128);
      const int vo = r * 8 * (FF * 2) + c * 8;
      const int xo = L_XN + (c & 7) * XN_LD + r * 16;
      u32x2 ra[8], rc[8];
      f32x4 g[4], u[4];
#pragma unroll
      for (int e = 0; e < 4; ++e) { g[e] = f32x4{0.f, 0.f, 0.f, 0.f}; u[e] = g[e]; }
#pragma unroll
      for (int i = 0; i < 8; ++i) ra[i] = LD64(r1, vo, i * (FF * 2), NT);
#pragma unroll
      for (int i = 0; i < 8 - MLP_GAP; ++i) rc[i] = LD64(r3, vo, i * (FF * 2), NT);
#define PAIR(x, y, d, rsx, sx, rsy, sy) { USE(x[2 * d + 1]); \
        _Pragma("unroll") for (int e = 0; e < 4; ++e) f[e][d] = __builtin_amdgcn_perm(x[2 * d + 1][e >> 1], x[2 * d][e >> 1], (e & 1) ? 0x07060302u : 0x05040100u); \
        PIN(); \
        _Pragma("unroll") for (int j = 2 * d - MLP_GAP; j < 2 * d + 2 - MLP_GAP; ++j) { \
          if (j < 0) y[8 + j] = LD64(rsy, vo, (sy + 8 + j) * (FF * 2), NT); else x[j] = LD64(rsx, vo, (sx + j) * (FF * 2), NT); } \
        PIN(); }
#pragma unroll 1
      for (int s = 0; s < 18; ++s) {
        const int sc_ = s * 32, sn = (s < 17 ? s + 1 : 17) * 32;
        const u32 nb = s < 17 ? DM * FF * 2 - task * 128 : 0;
        const rsrc_t n1 = mk(a.w1 + task * 128, nb), n3 = mk(a.w3 + task * 128, nb);
        const u32x4 xb = *(LAS u32x4*)(lds + xo + s * 64);
        u32x4 f[4];
        PAIR(ra, rc, 0, n1, sn, r3, sc_) PAIR(ra, rc, 1, n1, sn, r3, sc_) PAIR(ra, rc, 2, n1, sn, r3, sc_) PAIR(ra, rc, 3, n1, sn, r3, sc_)
#pragma unroll
        for (int e = 0; e < 4; ++e) g[e] = mfma(f[e], xb, g[e]);
        PIN();
        PAIR(rc, ra, 0, n3, sn, n1, sn) PAIR(rc, ra, 1, n3, sn, n1, sn) PAIR(rc, ra, 2, n3, sn, n1, sn) PAIR(rc, ra, 3, n3, sn, n1, sn)
#pragma unroll
        for (int e = 0; e < 4; ++e) u[e] = mfma(f[e], xb, u[e]);
        PIN();
      }
      float hv[4][4];
#pragma unroll
      for (int p = 0; p < 4; ++p)
#pragma unroll
        for (int e = 0; e < 4; ++e) hv[p][e] = rb(silu(rb(g[e][p]))) * rb(u[e][p]);
      const u32x4 h0 = {pk(hv[0][0], hv[0][1]), pk(hv[0][2], hv[0][3]), pk(hv[1][0], hv[1][1]), pk(hv[1][2], hv[1][3])};
      const u32x4 h1 = {pk(hv[2][0], hv[2][1]), pk(hv[2][2], hv[2][3]), pk(hv[3][0], hv[3][1]), pk(hv[3][2], hv[3][3])};
      ST128((c & 8) ? h1 : h0, rws, WS_HID + task * HID_LD + (c & 7) * 128 + r * 32 + (c >> 3) * 16, 0, SC1NT);
      DRAIN();
      while (!LGET(L_TK)) __builtin_amdgcn_s_sleep(2);
      asm volatile("" ::: "memory");
      if (lane == 0) ST64(*(LAS u32x2*)(lds + L_TG), rws, WS_HID + task * HID_LD + NB * 128, 0, SC1);
    }
    PIN();
    softmax();
    if (job == 1) {
      const int ks = task / 9, cg = task % 9;
      const rsrc_t r2 = mk(a.w2 + (size_t)ks * (576 * DM * 2) + cg * 128, 576 * DM * 2 - cg * 128);
      const int vo = r * 8 * (DM * 2) + c * 8;
      u32x2 ra[8], rc[8];
#pragma unroll
      for (int i = 0; i < 8; ++i) ra[i] = LD64(r2, vo, i * (DM * 2), NT);
      const u32x2 tg = *(LAS u32x2*)(lds + L_TG);
      bool ok = true;
      const u64 start = now();
      for (;;) {
        const u32x2 f = LD64(rws, WS_HID + NB * 128 + (ks * 9 + min(lane, 8)) * HID_LD, 0, SC1);
        if (__all(f[0] == tg[0] && f[1] == tg[1])) break;
        if (now() - start > SPIN_CAP) { ok = false; break; }
        __builtin_amdgcn_s_sleep(16);
      }
      asm volatile("" ::: "memory");
      {
        u32x4 t[9];
#pragma unroll
        for (int i = 0; i < 9; ++i) t[i] = LD128(rws, WS_HID + ks * 9 * HID_LD + lane * 16, i * HID_LD, SC1);
#pragma unroll
        for (int i = 0; i < 9; ++i) *(LAS u32x4*)(lds + L_HB + i * 1024 + lane * 16) = t[i];
      }
      LWAIT();
      const int ho = L_HB + (c & 7) * 128 + r * 16;
      f32x4 d[4];
#pragma unroll
      for (int e = 0; e < 4; ++e) d[e] = f32x4{0.f, 0.f, 0.f, 0.f};
#pragma unroll 1
      for (int s = 0; s < 9; ++s) {
        const rsrc_t n2 = mk(a.w2 + (size_t)ks * (576 * DM * 2) + cg * 128, s < 8 ? 576 * DM * 2 - cg * 128 : 0);
        const int sn = (s < 8 ? s + 1 : 8) * 64;
        const u32x4 hb = *(LAS u32x4*)(lds + ho + s * 1024), hc = *(LAS u32x4*)(lds + ho + s * 1024 + 64);
        u32x4 f[4];
#pragma unroll
        for (int i = 0; i < 8; ++i) { USE(ra[i]); rc[i] = LD64(r2, vo, (s * 64 + 32 + i) * (DM * 2), NT); PIN(); }
        tr8(ra, f);
#pragma unroll
        for (int e = 0; e < 4; ++e) d[e] = mfma(f[e], hb, d[e]);
        PIN();
#pragma unroll
        for (int i = 0; i < 8; ++i) { USE(rc[i]); ra[i] = LD64(n2, vo, (sn + i) * (DM * 2), NT); PIN(); }
        tr8(rc, f);
#pragma unroll
        for (int e = 0; e < 4; ++e) d[e] = mfma(f[e], hc, d[e]);
        PIN();
      }
#pragma unroll
      for (int p = 0; p < 2; ++p) {
        u32x4 o;
#pragma unroll
        for (int e = 0; e < 4; ++e) o[e] = __float_as_uint((c & 8) ? d[e][2 + p] : d[e][p]);
        *(LAS u32x4*)(lds + L_MO + (c & 7) * 256 + r * 64 + (c >> 3) * 32 + p * 16) = o;
      }
      LWAIT();
#pragma unroll
      for (int i = 0; i < 2; ++i) ST128(*(LAS u32x4*)(lds + L_MO + lane * 16 + i * 1024), rws, WS_MLP + task * MLP_LD + lane * 16, i * 1024, SC1NT);
      DRAIN();
      if (lane == 0 && ok) ST64(tg, rws, WS_MLP + task * MLP_LD + NB * 256, 0, SC1);
    }
    BAR();
    return;
  }
  PIN();
  softmax();
  if (h < NCONS) {
    bool ok = true;
    const u32x2 tg = *(LAS u32x2*)(lds + L_TG);
    const u32 t0 = tg[0], t1 = tg[1];
    const int col = h * 32 + (lane & 7) * 4;
    wait_batch(PRE_AT);
    int oo = (b * DM + col + r) * 2;
    const u32 xv = __builtin_amdgcn_raw_buffer_load_b16(mk(a.x, NB * DM * 2), oo, 0, NT);
    const u64 start = now();
    for (;;) {
      const u32x2 f1 = LD64(rws, WS_ATT + DM * 4 + (b * NH + (lane & 31)) * ATT_LD, 0, SC1);
      const u32x2 f2 = LD64(rws, WS_MLP + NB * 256 + ((lane & 7) * 9 + (h >> 1)) * MLP_LD, 0, SC1);
      bool all = f1[0] == t0 && f1[1] == t1 && f2[0] == t0 && f2[1] == t1;
      if (blk == 0) {
#pragma unroll
        for (int i = 0; i < 4; ++i) { const u32x2 f = LD64(rws, WS_ATT + DM * 4 + lane * ATT_LD, i * 64 * ATT_LD, SC1); all &= f[0] == t0 && f[1] == t1; }
      }
      if (__all(all)) break;
      if (now() - start > SPIN_CAP) { ok = false; break; }
      __builtin_amdgcn_s_sleep(16);
    }
    asm volatile("" ::: "memory");
    if (blk == 0 && lane == 0) ST64((u32x2{t0, t1}), rws, WS_EPOCH, 0, SC1);
    const u32x4 f = LD128(rws, WS_MLP + ((lane >> 3) * 9 + (h >> 1)) * MLP_LD + b * 256 + (h & 1) * 128 + (lane & 7) * 16, 0, SC1);
    float g = rb(silu(rb(sum8x4(__uint_as_float(f[0]), __uint_as_float(f[1]), __uint_as_float(f[2]), __uint_as_float(f[3]))))), xe = lo16(xv);
    int ao = WS_ATT + (b * NH + (lane >> 3)) * ATT_LD + col * 4;
    oo = c < 8 ? oo : NOWHERE;
    asm volatile("" : "+v"(g), "+v"(xe), "+v"(ao), "+v"(oo));
    const u32x4 dws = {(u32)(size_t)a.ws, (u32)((size_t)a.ws >> 32) & 0xffffu, WS_END, 0x00020000u};
    const u32x4 dout = {(u32)(size_t)a.out, (u32)((size_t)a.out >> 32) & 0xffffu, NB * DM * 2, 0x00020000u};
    BAR();
    if (ok) {
#define SWEEP(a0, a1, a2, a3) "buffer_load_dwordx4 " a0 ", %[ao], %[ws], 0 offen sc1\n\tbuffer_load_dwordx4 " a1 ", %[ao], %[ws], %[o1] offen sc1\n\t" \
                              "buffer_load_dwordx4 " a2 ", %[ao], %[ws], %[o2] offen sc1\n\tbuffer_load_dwordx4 " a3 ", %[ao], %[ws], %[o3] offen sc1\n\t"
      const u32 start2 = (u32)now();
      asm volatile(
        "s_waitcnt vmcnt(0)\n\t"
        SWEEP("v[64:67]", "v[68:71]", "v[72:75]", "v[76:79]")
        "s_sleep 0\nLsw%=:\n\t"
        SWEEP("v[80:83]", "v[84:87]", "v[88:91]", "v[92:95]")
        "s_waitcnt vmcnt(4)\n\t"
        "v_max3_u32 v96, v64, v65, v66\n\tv_max3_u32 v97, v67, v68, v69\n\tv_max3_u32 v98, v70, v71, v72\n\tv_max3_u32 v99, v73, v74, v75\n\t"
        "v_max3_u32 v96, v96, v76, v77\n\tv_max3_u32 v97, v97, v78, v79\n\tv_max3_u32 v96, v96, v97, v98\n\tv_max_u32 v96, v96, v99\n\t"
        "v_cmp_eq_u32 vcc, -1, v96\n\ts_nop 1\n\ts_cbranch_vccz LswA%=\n\t"
        SWEEP("v[64:67]", "v[68:71]", "v[72:75]", "v[76:79]")
        "s_waitcnt vmcnt(4)\n\t"
        "v_max3_u32 v96, v80, v81, v82\n\tv_max3_u32 v97, v83, v84, v85\n\tv_max3_u32 v98, v86, v87, v88\n\tv_max3_u32 v99, v89, v90, v91\n\t"
        "v_max3_u32 v96, v96, v92, v93\n\tv_max3_u32 v97, v97, v94, v95\n\tv_max3_u32 v96, v96, v97, v98\n\tv_max_u32 v96, v96, v99\n\t"
        "v_cmp_eq_u32 vcc, -1, v96\n\ts_nop 1\n\ts_cbranch_vccz LswB%=\n\t"
        "s_memrealtime vcc\n\ts_waitcnt lgkmcnt(0)\n\ts_sub_u32 vcc_lo, vcc_lo, %[st]\n\ts_cmp_lt_u32 vcc_lo, %[cap]\n\ts_cbranch_scc1 Lsw%=\n\t"
        "v_mov_b32 v96, 0x7fc0\n\ts_branch Lsws%=\n"
        "LswB%=:\n\t"
        "v_add_f32 v96, v80, v84\n\tv_add_f32 v88, v88, v92\n\tv_add_f32 v97, v81, v85\n\tv_add_f32 v89, v89, v93\n\t"
        "v_add_f32 v98, v82, v86\n\tv_add_f32 v90, v90, v94\n\tv_add_f32 v99, v83, v87\n\tv_add_f32 v91, v91, v95\n\t"
        "v_add_f32 v96, v96, v88\n\tv_add_f32 v97, v97, v89\n\tv_add_f32 v98, v98, v90\n\tv_add_f32 v99, v99, v91\n\t"
        "s_branch Lswf%=\n"
        "LswA%=:\n\t"
        "v_add_f32 v96, v64, v68\n\tv_add_f32 v72, v72, v76\n\tv_add_f32 v97, v65, v69\n\tv_add_f32 v73, v73, v77\n\t"
        "v_add_f32 v98, v66, v70\n\tv_add_f32 v74, v74, v78\n\tv_add_f32 v99, v67, v71\n\tv_add_f32 v75, v75, v79\n\t"
        "v_add_f32 v96, v96, v72\n\tv_add_f32 v97, v97, v73\n\tv_add_f32 v98, v98, v74\n\tv_add_f32 v99, v99, v75\n"
        "Lswf%=:\n\ts_nop 1\n\t"
        "v_permlane32_swap_b32 v96, v98\n\tv_permlane32_swap_b32 v97, v99\n\ts_nop 1\n\tv_add_f32 v96, v96, v98\n\tv_add_f32 v97, v97, v99\n\ts_nop 1\n\t"
        "v_permlane16_swap_b32 v96, v97\n\ts_nop 1\n\tv_add_f32 v96, v96, v97\n\ts_nop 1\n\t"
        "v_add_f32_dpp v96, v96, v96 row_ror:8 row_mask:0xf bank_mask:0xf\n\t"
        "v_cvt_pk_bf16_f32 v96, v96, v96\n\tv_lshlrev_b32 v96, 16, v96\n\tv_mul_f32 v96, v96, %[g]\n\t"
        "v_cvt_pk_bf16_f32 v96, v96, v96\n\tv_lshlrev_b32 v96, 16, v96\n\tv_add_f32 v96, %[x], v96\n\tv_cvt_pk_bf16_f32 v96, v96, v96\n"
        "Lsws%=:\n\tbuffer_store_short v96, %[oo], %[out], 0 offen\n\ts_waitcnt vmcnt(0)"
        :
        : [ao] "v"(ao), [ws] "s"(dws), [o1] "s"(8 * ATT_LD), [o2] "s"(16 * ATT_LD), [o3] "s"(24 * ATT_LD), [st] "s"(start2), [cap] "s"((u32)SPIN_CAP),
          [g] "v"(g), [x] "v"(xe), [oo] "v"(oo), [out] "s"(dout)
        : "memory", "vcc", "scc", "v64", "v65", "v66", "v67", "v68", "v69", "v70", "v71", "v72", "v73", "v74", "v75", "v76", "v77", "v78", "v79", "v80", "v81",
          "v82", "v83", "v84", "v85", "v86", "v87", "v88", "v89", "v90", "v91", "v92", "v93", "v94", "v95", "v96", "v97", "v98", "v99");
    } else {
      __builtin_amdgcn_raw_buffer_store_b16((u16)0x7fc0, mk(a.out, NB * DM * 2), oo, 0, 0);
    }
  } else {
    BAR();
  }
}

}

extern "C" void kernel_launch(void* const* d_in, const int* in_sizes, int n_in, void* d_out, int out_size, void* d_ws, size_t ws_size,
                              hipStream_t stream) {
  Args a;
  a.x = (const char*)d_in[0]; a.k = (const char*)d_in[1]; a.v = (const char*)d_in[2]; a.wq = (const char*)d_in[3]; a.wo = (const char*)d_in[4];
  a.w1 = (const char*)d_in[6]; a.w3 = (const char*)d_in[7]; a.w2 = (const char*)d_in[8];
  a.out = (char*)d_out; a.ws = (char*)d_ws;
  hipLaunchKernelGGL(fused_decode, dim3(NB * NH), dim3(1024), 0, stream, a);
}
```

```cpp
#include <hip/hip_runtime.h>
#include <hip/hip_bf16.h>
#include <cstdint>

#define RUN 2
#define ORD 1
namespace {

typedef unsigned u32;
typedef unsigned long long u64;
typedef unsigned short u16;
typedef u32 u32x4 __attribute__((ext_vector_type(4)));
typedef u32 u32x2 __attribute__((ext_vector_type(2)));
typedef float f32x4 __attribute__((ext_vector_type(4)));
typedef __bf16 bf16x8 __attribute__((ext_vector_type(8)));
typedef __bf16 bf16x2 __attribute__((ext_vector_type(2)));
typedef __bf16 bf16x16 __attribute__((ext_vector_type(16)));
typedef u32 u32x8 __attribute__((ext_vector_type(8)));
#define LAS __attribute__((address_space(3)))

constexpr int NB = 8, DM = 576, NH = 32, SK = 4096, DH = 128, FF = 4608;
constexpr int NTASK = FF / 64;
constexpr int NCONS = DM / 32;
constexpr int KEEP = 0, NT = 2, SC1 = 16, SC1NT = 18;

constexpr int WIN0 = 41, WIN_MLP = 2;
constexpr int WMID = 45, WLO = 36, WHI = 56, GAIN = 20;
constexpr int OPENW = 10;
constexpr u32 PACE_K = 0x19110903u, PACE_V = 0x39332b23u;
constexpr int PACE_DLY = 3;
constexpr int PRE_AT = 58;
constexpr int MLP_GAP = 6;
constexpr u64 SPIN_CAP = 5000000;

constexpr u32 ATT_LD = DM * 4 + 128, HID_LD = NB * 128 + 128, MLP_LD = NB * 256 + 128, PACE_LD = 4352;
constexpr u32 WS_EPOCH = 0, WS_ATT = 4096, WS_HID = WS_ATT + NB * NH * ATT_LD, WS_MLP = WS_HID + NTASK * HID_LD, WS_PACE = WS_MLP + NTASK * MLP_LD,
              WS_END = WS_PACE + 8 * 64 * PACE_LD;
constexpr u32 EMPTY = 0xffffffffu;
constexpr int NOWHERE = 0x40000000;

constexpr int XN_LD = 1168;
constexpr int L_XN = 0, L_QP = L_XN + NB * XN_LD, L_QB = L_QP + 12 * DH * 4, L_PS = L_QB + DH * 2, L_WM = L_PS + SK * 2, L_WS = L_WM + 64,
              L_OP = L_WS + 64, L_OB = L_OP + 4 * DH * 4, L_TG = L_OB + 9 * DH * 2, L_PG = L_TG + 8, L_TK = L_TG + 12, L_WN = L_TG + 16,
              L_XK = L_TG + 24, L_QC = L_TG + 28, L_QK = L_TG + 32, L_XB = L_TG + 48, L_MO = L_XB + DM * 2, L_HB = L_MO + NB * 256,
              L_END = L_HB + 9 * 1024;

struct Args { const char* x; const char* k; const char* v; const char* wq; const char* wo; const char* w1; const char* w3; const char* w2; char* out; char* ws; };

typedef __amdgpu_buffer_rsrc_t rsrc_t;
__device__ __forceinline__ rsrc_t mk(const char* p, u32 n) { return __builtin_amdgcn_make_buffer_rsrc((void*)p, 0, n, 0x00020000); }
#define LD128(r, vo, so, aux) __builtin_amdgcn_raw_buffer_load_b128(r, vo, so, aux)
#define LD64(r, vo, so, aux) __builtin_amdgcn_raw_buffer_load_b64(r, vo, so, aux)
#define LD32(r, vo, so, aux) __builtin_amdgcn_raw_buffer_load_b32(r, vo, so, aux)
#define ST128(v, r, vo, so, aux) __builtin_amdgcn_raw_buffer_store_b128(v, r, vo, so, aux)
#define ST64(v, r, vo, so, aux) __builtin_amdgcn_raw_buffer_store_b64(v, r, vo, so, aux)
#define PIN() __builtin_amdgcn_sched_barrier(0)
#define USE(x) asm volatile("" ::"v"(x) : "memory")
#define BAR() asm volatile("s_waitcnt lgkmcnt(0)\n\ts_barrier" ::: "memory")
#define RAWBAR() asm volatile("s_barrier" ::: "memory")
#define LWAIT() asm volatile("s_waitcnt lgkmcnt(0)" ::: "memory")
#define DRAIN() asm volatile("s_waitcnt vmcnt(0)" ::: "memory")
#define TOUCH(d, p) asm volatile("s_load_dword %0, %1, 0x0" : "=&s"(d) : "s"(p) : "memory")
#define LGET(off) __hip_atomic_load((LAS u32*)(lds + (off)), __ATOMIC_RELAXED, __HIP_MEMORY_SCOPE_WORKGROUP)
#define LPUT(off, v) __hip_atomic_store((LAS u32*)(lds + (off)), (u32)(v), __ATOMIC_RELAXED, __HIP_MEMORY_SCOPE_WORKGROUP)
#define LADD(off) __hip_atomic_fetch_add((LAS u32*)(lds + (off)), 1u, __ATOMIC_RELAXED, __HIP_MEMORY_SCOPE_WORKGROUP)
#define LSPIN(off, n) do { while (LGET(off) < (n)) __builtin_amdgcn_s_sleep(1); asm volatile("" ::: "memory"); } while (0)
#define DITH(i) (((i) & 1) * 4 + ((i) & 2) + ((i) >> 2))
#define GATE(i) asm volatile("s_cmp_gt_i32 %0, %1\n\ts_cbranch_scc1 Lg7_%=\n\ts_cmp_gt_i32 %0, %2\n\ts_cbranch_scc1 Lg5_%=\n\t" \
                             "s_cmp_gt_i32 %0, %3\n\ts_cbranch_scc1 Lg4_%=\n\ts_waitcnt vmcnt(3)\nLg4_%=:\n\ts_waitcnt vmcnt(4)\n" \
                             "Lg5_%=:\n\ts_waitcnt vmcnt(5)\nLg7_%=:" \
                             ::"s"(win), "i"(48 + DITH(i)), "i"(40 + DITH(i)), "i"(32 + DITH(i)) : "scc", "memory")
#define EIGHT(STEP) STEP(0) STEP(1) STEP(2) STEP(3) STEP(4) STEP(5) STEP(6) STEP(7)

__device__ __forceinline__ u32 pk(float lo, float hi) { bf16x2 t = {(__bf16)lo, (__bf16)hi}; return __builtin_bit_cast(u32, t); }
__device__ __forceinline__ float rb(float x) { return __uint_as_float(pk(x, x) << 16); }
__device__ __forceinline__ float lo16(u32 v) { return __uint_as_float(v << 16); }
__device__ __forceinline__ float hi16(u32 v) { return __uint_as_float(v & 0xffff0000u); }
__device__ __forceinline__ float dot2(u32 a, u32 b, float c) {
  return __builtin_amdgcn_fdot2_f32_bf16(__builtin_bit_cast(bf16x2, a), __builtin_bit_cast(bf16x2, b), c, false);
}
__device__ __forceinline__ f32x4 mfma(u32x4 a, u32x4 b, f32x4 c) {
  return __builtin_amdgcn_mfma_f32_16x16x32_bf16(__builtin_bit_cast(bf16x8, a), __builtin_bit_cast(bf16x8, b), c, 0, 0, 0);
}
template <int CTRL> __device__ __forceinline__ float dpp(float x) {
  return __builtin_bit_cast(float, __builtin_amdgcn_mov_dpp(__builtin_bit_cast(int, x), CTRL, 0xf, 0xf, true));
}
constexpr int X1 = 0xB1, X2 = 0x4E, HMIR = 0x141, MIR = 0x140, ROR8 = 0x128;
__device__ __forceinline__ float sum16(float x) { x += dpp<X1>(x); x += dpp<X2>(x); x += dpp<HMIR>(x); x += dpp<MIR>(x); return x; }
__device__ __forceinline__ float max16(float x) { x = fmaxf(x, dpp<X1>(x)); x = fmaxf(x, dpp<X2>(x)); x = fmaxf(x, dpp<HMIR>(x)); x = fmaxf(x, dpp<MIR>(x)); return x; }
__device__ __forceinline__ float sumrows(float x) {
  auto s = __builtin_amdgcn_permlane16_swap(__float_as_uint(x), __float_as_uint(x), false, false);
  x = __uint_as_float(s[0]) + __uint_as_float(s[1]);
  auto t = __builtin_amdgcn_permlane32_swap(__float_as_uint(x), __float_as_uint(x), false, false);
  return __uint_as_float(t[0]) + __uint_as_float(t[1]);
}
__device__ __forceinline__ float maxrows(float x) {
  auto s = __builtin_amdgcn_permlane16_swap(__float_as_uint(x), __float_as_uint(x), false, false);
  x = fmaxf(__uint_as_float(s[0]), __uint_as_float(s[1]));
  auto t = __builtin_amdgcn_permlane32_swap(__float_as_uint(x), __float_as_uint(x), false, false);
  return fmaxf(__uint_as_float(t[0]), __uint_as_float(t[1]));
}
__device__ __forceinline__ float sum8x4(float v0, float v1, float v2, float v3) {
  auto p = __builtin_amdgcn_permlane32_swap(__float_as_uint(v0), __float_as_uint(v2), false, false);
  auto q = __builtin_amdgcn_permlane32_swap(__float_as_uint(v1), __float_as_uint(v3), false, false);
  auto t = __builtin_amdgcn_permlane16_swap(__float_as_uint(__uint_as_float(p[0]) + __uint_as_float(p[1])), __float_as_uint(__uint_as_float(q[0]) + __uint_as_float(q[1])), false, false);
  const float x = __uint_as_float(t[0]) + __uint_as_float(t[1]);
  return x + dpp<ROR8>(x);
}
__device__ __forceinline__ float fdiv(float a, float d, float rd) { const float q = a * rd; return fmaf(fmaf(-q, d, a), rd, q); }
__device__ __forceinline__ float silu(float x) { const float d = 1.f + __expf(-x); return d < INFINITY ? fdiv(x, d, __builtin_amdgcn_rcpf(d)) : x * 0.f; }
__device__ __forceinline__ u64 now() { return __builtin_amdgcn_s_memrealtime(); }

struct Diag { u32 s[2][4]; };
__device__ __forceinline__ Diag make_diag(int c) {
  Diag d;
#pragma unroll
  for (int par = 0; par < 2; ++par)
#pragma unroll
    for (int w = 0; w < 4; ++w) {
      const u32 b = par ? 0x0302u : 0x0100u;
      d.s[par][w] = (c >> 1) == w ? ((c & 1) ? (b << 16) | 0x0c0cu : 0x0c0c0000u | b) : 0x0c0c0c0cu;
    }
  return d;
}
template <int PAR> __device__ __forceinline__ f32x4 axpy4(u32 val, u32x4 rows, const Diag& d, f32x4 acc) {
  u32x4 af;
#pragma unroll
  for (int w = 0; w < 4; ++w) af[w] = __builtin_amdgcn_perm(val, val, d.s[PAR][w]);
  return mfma(af, rows, acc);
}
__device__ __forceinline__ float score4(u32x4 k, u32x4 q) {
  float d = dot2(k[0], q[0], 0.f); d = dot2(k[1], q[1], d); d = dot2(k[2], q[2], d); d = dot2(k[3], q[3], d);
  return sum16(d);
}
__device__ __forceinline__ void tr8(const u32x2 (&raw)[8], u32x4 (&f)[4]) {
#pragma unroll
  for (int e = 0; e < 4; ++e)
#pragma unroll
    for (int d = 0; d < 4; ++d) f[e][d] = __builtin_amdgcn_perm(raw[2 * d + 1][e >> 1], raw[2 * d][e >> 1], (e & 1) ? 0x07060302u : 0x05040100u);
}

__global__ void __launch_bounds__(1024, 4) fused_decode(Args a) {
  __shared__ __attribute__((aligned(16))) char lds_[L_END];
  LAS char* lds = (LAS char*)lds_;
  if (threadIdx.x == 640) { LPUT(L_PG, 0); LPUT(L_TK, 0); LPUT(L_WN, 0); LPUT(L_WN + 4, 0); LPUT(L_XK, 0); LPUT(L_QC, 0); LPUT(L_QK, 0); LWAIT(); }
  PIN(); RAWBAR(); PIN();
  asm volatile("" ::"s"(a.x), "s"(a.ws));
  const int tid = threadIdx.x, lane = tid & 63, w = __builtin_amdgcn_readfirstlane(tid >> 6), r = lane >> 4, c = lane & 15;
  const int blk = blockIdx.x, b = blk >> 5, h = blk & 31;
  const int hrot = (h - b) & 31, job = hrot / 9, task = (h & 7) * 9 + (hrot % 9 < 8 ? b : 8);
  const char* kbase = a.k + ((size_t)blk << 20);
  const char* vbase = a.v + ((size_t)blk << 20);
  const rsrc_t rk = mk(kbase, 1u << 20);
  const int sw = w & 3;
  const int vA = lane * 16 + sw * 4096;
  constexpr int QN = RUN == 1 ? 4 : RUN == 2 ? 4 : 8 / RUN;
  const int swx = ORD ? sw : 0;
  auto kvbase = [&](int beta) { const int p = beta % RUN * (8 / RUN); return beta / RUN * RUN * 32768 + (p >> 2) * 16384 + ((p ^ swx) & 3 & ~(QN - 1)) * 1024; };
#define KVLD(rs, base, i) LD128(rs, vA + (i) % RUN * 32768 + ((i) / RUN >> 2) * 16384 + ((((i) / RUN) ^ swx) & (QN - 1)) * 1024, base, NT)
  float mx = -INFINITY;
  auto put_scores = [&](float sc, int batch) {
    const float s = rb(rb(sc) * 0.08838834764831845f);
    mx = fmaxf(mx, s);
    if (c < 8) *(LAS u16*)(lds + L_PS + batch * 64 + r * 16 + c * 2) = (u16)(__float_as_uint(s) >> 16);
  };
  auto softmax = [&]() {
    const float wm = maxrows(max16(mx));
    if (lane == 0) *(LAS float*)(lds + L_WM + w * 4) = wm;
    BAR();
    const float M = max16(*(LAS float*)(lds + L_WM + c * 4));
    const u32x2 sv = *(LAS u32x2*)(lds + L_PS + tid * 8);
    const float e0 = __expf(lo16(sv[0]) - M), e1 = __expf(hi16(sv[0]) - M), e2 = __expf(lo16(sv[1]) - M), e3 = __expf(hi16(sv[1]) - M);
    const float ws = sumrows(sum16((e0 + e1) + (e2 + e3)));
    if (lane == 0) *(LAS float*)(lds + L_WS + w * 4) = ws;
    BAR();
    const float inv = 1.f / sum16(*(LAS float*)(lds + L_WS + c * 4));
    *(LAS u32x2*)(lds + L_PS + tid * 8) = u32x2{pk(e0 * inv, e1 * inv), pk(e2 * inv, e3 * inv)};
    BAR();
  };

  if (w >= 12) {
    u32x4 ring[8];
    u32x4 ext[8];
    __builtin_amdgcn_s_sleep(32);
#pragma unroll
    for (int i = 0; i < 16; ++i) {
      if (i >= OPENW) USE((i - OPENW < 8 ? ring : ext)[(i - OPENW) & 7]);
      if (i < 8) ring[i] = KVLD(rk, kvbase(0), i); else ext[i - 8] = KVLD(rk, kvbase(1), i - 8);
      PIN();
    }
    __builtin_amdgcn_s_setprio(3);
    LSPIN(L_QK, 2);
    const u32x4 q = *(LAS u32x4*)(lds + L_QB + c * 16);
    float sc = 0.f;
    const int win0 = WIN0 + (job == 0 ? WIN_MLP : 0);
    int win = win0;
#define PACE_IN() const u32 tj = (u32)now(); const u64 sch = __hip_atomic_load((LAS u64*)(lds + L_WN), __ATOMIC_RELAXED, __HIP_MEMORY_SCOPE_WORKGROUP)
#define PACE_OUT(j) do { const u32 due_ = __builtin_amdgcn_readfirstlane((u32)sch), tau_ = __builtin_amdgcn_readfirstlane((u32)(sch >> 32)); \
      const int late_ = min(max((int)(tj * 256u - due_ - tau_ * (u32)(j)) >> 8, -4096), 4096); \
      win = tau_ ? min(max(WMID + ((late_ * GAIN) >> 8), WLO), WHI) : win0; } while (0)
#pragma unroll
    for (int i = 0; i < 8; ++i) { const float d = score4(ring[i], q); sc = (c & 7) == i ? d : sc; PIN(); }
    put_scores(sc, sw);
#define FSTEP(i) { if (i > 0) { GATE(i); ring[(i + 7) & 7] = KVLD(rk, kvbase(2), (i + 7) & 7); } PIN(); const float d = score4(ext[i], q); sc = (c & 7) == i ? d : sc; PIN(); }
    EIGHT(FSTEP)
    put_scores(sc, 4 + sw);
#pragma unroll 1
    for (int beta = 2; beta < 32; ++beta) {
      const rsrc_t rn = mk(beta < 31 ? kbase : vbase, 1u << 20);
      const int sc0 = __builtin_amdgcn_readfirstlane(kvbase(beta)), sn0 = __builtin_amdgcn_readfirstlane(kvbase(beta < 31 ? beta + 1 : 0));
      if (beta == 31) win = 64;
      PACE_IN();
      if (tid == 768) LPUT(L_PG, beta);
#define KSTEP(i) { GATE(i); if (i == 0) ring[7] = KVLD(rk, sc0, 7); else ring[(i + 7) & 7] = KVLD(rn, sn0, (i + 7) & 7); PIN(); \
        const float d = score4(ring[i], q); sc = (c & 7) == i ? d : sc; PIN(); }
      EIGHT(KSTEP)
      put_scores(sc, beta * 4 + sw);
      PACE_OUT(beta);
    }
    const rsrc_t rv = mk(vbase, 1u << 20);
    ring[7] = KVLD(rv, kvbase(0), 7);
#pragma unroll
    for (int i = 0; i < 8; ++i) { ext[i] = KVLD(rv, kvbase(1), i); PIN(); }
    softmax();
    const Diag dg = make_diag(c);
    f32x4 acc = {0.f, 0.f, 0.f, 0.f};
    const int po = L_PS + sw * 64 + r * 16;
    u32x4 pn = *(LAS u32x4*)(lds + po + 512);
    {
      const u32x4 p0 = *(LAS u32x4*)(lds + po), p1 = *(LAS u32x4*)(lds + po + 256);
#pragma unroll
      for (int i = 0; i < 8; ++i) { acc = (i & 1) ? axpy4<1>(p0[i >> 1], ring[i], dg, acc) : axpy4<0>(p0[i >> 1], ring[i], dg, acc); PIN(); }
#define ESTEP(i) { if (i > 0) { GATE(i); ring[(i + 7) & 7] = KVLD(rv, kvbase(2), (i + 7) & 7); } PIN(); acc = axpy4<i & 1>(p1[i >> 1], ext[i], dg, acc); PIN(); }
      EIGHT(ESTEP)
    }
#pragma unroll 1
    for (int beta = 2; beta < 32; ++beta) {
      const rsrc_t rn = mk(vbase, beta < 31 ? 1u << 20 : 0u);
      const int bn = beta < 31 ? beta + 1 : 0;
      const int sc0 = __builtin_amdgcn_readfirstlane(kvbase(beta)), sn0 = __builtin_amdgcn_readfirstlane(kvbase(bn));
      if (beta == 31) win = 64;
      const u32x4 pv = pn;
      pn = *(LAS u32x4*)(lds + po + bn * 256);
      PACE_IN();
      if (tid == 768) LPUT(L_PG, 32 + beta);
#define VSTEP(i) { GATE(i); if (i == 0) ring[7] = KVLD(rv, sc0, 7); else ring[(i + 7) & 7] = KVLD(rn, sn0, (i + 7) & 7); PIN(); \
        acc = axpy4<i & 1>(pv[i >> 1], ring[i], dg, acc); PIN(); }
      EIGHT(VSTEP)
      PACE_OUT(32 + beta);
    }
    if (r < 2) *(LAS f32x4*)(lds + L_OP + sw * 512 + c * 32 + r * 16) = acc;
    BAR();
    return;
  }

  const rsrc_t rws = mk(a.ws, WS_END);
  auto wait_batch = [&](u32 n) { while (LGET(L_PG) < n) __builtin_amdgcn_s_sleep(2); };
  if (w == 11) {
    u32 t0, t1, t2, t3, t4, t5, t6, t7;
    asm volatile("" ::"s"(a.ws), "s"(a.out), "s"(kbase), "s"(vbase), "s"(a.w2));
    TOUCH(t0, kbase); TOUCH(t1, a.ws);
    __builtin_amdgcn_s_sleep(32);
    TOUCH(t2, a.wo + (size_t)h * (DH * DM * 2)); TOUCH(t3, a.w1); TOUCH(t4, a.w3); TOUCH(t5, vbase); TOUCH(t6, a.w2); TOUCH(t7, a.out);
    const u32x2 ep = LD64(rws, WS_EPOCH, 0, SC1);
    if (lane < 48) {
#pragma unroll
      for (int i = 0; i < 3; ++i) ST128((u32x4{EMPTY, EMPTY, EMPTY, EMPTY}), rws, WS_ATT + blk * ATT_LD + lane * 16 + i * 768, 0, SC1);
    }
    asm volatile("s_waitcnt lgkmcnt(0)" : "+s"(t0), "+s"(t1), "+s"(t2), "+s"(t3), "+s"(t4), "+s"(t5), "+s"(t6), "+s"(t7)::"memory");
    const u64 tag = (((u64)(u32)__builtin_amdgcn_readfirstlane(ep[1]) << 32) | (u32)__builtin_amdgcn_readfirstlane(ep[0])) + 1;
    DRAIN();
    if (lane == 0) {
      *(LAS u64*)(lds + L_TG) = tag;
      LWAIT();
      LPUT(L_TK, 1);
      ST64((u32x2{(u32)tag, (u32)(tag >> 32)}), rws, WS_ATT + blk * ATT_LD + DM * 4, 0, SC1);
    }
    const u32 tb = (u32)now();
    const bool sample = ((blk >> 3) & 3) == (blk & 3);
    float mprev = 0.f;
    int atprev = 0;
    auto pace = [&](int k, int at) {
      wait_batch(at);
      const u32 tk = (u32)now();
      if (sample && lane < 8) ST128((u32x4{tk, (u32)tag, tk, (u32)tag}), rws, WS_PACE + (k * 64 + b * 8 + (blk & 7)) * PACE_LD + lane * 16, 0, SC1NT);
      wait_batch(at + PACE_DLY);
      const u32x2 v = LD64(rws, WS_PACE + (k * 64 + ((b + lane + blk) & 7) * 8 + (lane & 7)) * PACE_LD, 0, SC1);
      const bool in = v[1] == (u32)tag && lane < 8;
      const float n = (float)__popcll(__ballot(in));
      const float m = (float)(int)(tk - tb) + sum16(in ? (float)(int)(v[0] - tk) : 0.f) * __builtin_amdgcn_rcpf(fmaxf(n, 1.f));
      if (k > 0 && lane == 0) {
        const float tau = (m - mprev) * __builtin_amdgcn_rcpf((float)(at - atprev)) * 0.99f;
        const u32 tau256 = (u32)fminf(fmaxf(tau * 256.f, 1.f), 1e6f);
        __hip_atomic_store((LAS u64*)(lds + L_WN), ((u64)tau256 << 32) | (tb * 256u + (u32)(int)(m * 256.f) - tau256 * (u32)at), __ATOMIC_RELAXED, __HIP_MEMORY_SCOPE_WORKGROUP);
      }
      mprev = m; atprev = at;
    };
#pragma unroll 1
    for (int k = 0; k < 4; ++k) pace(k, (PACE_K >> (8 * k)) & 255);
    softmax();
#pragma unroll 1
    for (int k = 0; k < 4; ++k) pace(4 + k, (PACE_V >> (8 * k)) & 255);
    BAR();
    return;
  }
  if (w < 9) {
    __builtin_amdgcn_s_setprio(1);
    const int wr = (w + b) % 9;
    u32x4 wb[16];
    {
      const rsrc_t rq = mk(a.wq + h * 256, DM * 8192 - h * 256);
      const int vo = (64 * wr + 16 * r) * 8192 + c * 16;
#pragma unroll
      for (int i = 0; i < 16; ++i) wb[i] = LD128(rq, vo, i * 8192, KEEP);
    }
    PIN();
    const Diag dgq = make_diag(c);
    LSPIN(L_XK, 1);
    const u32x4 x0 = *(LAS u32x4*)(lds + L_XB + (64 * wr + 16 * r) * 2), x1 = *(LAS u32x4*)(lds + L_XB + (64 * wr + 16 * r) * 2 + 16);
    PIN();
    {
      f32x4 acc0 = {0.f, 0.f, 0.f, 0.f}, acc1 = acc0;
#pragma unroll
      for (int i = 0; i < 8; ++i) {
        const u32 xv = i < 4 ? x0[i & 3] : x1[i & 3];
        acc0 = axpy4<0>(xv, wb[2 * i], dgq, acc0); acc1 = axpy4<1>(xv, wb[2 * i + 1], dgq, acc1);
      }
      if (r < 2) *(LAS f32x4*)(lds + L_QP + wr * 512 + c * 32 + r * 16) = acc0 + acc1;
    }
    LWAIT();
    if (lane == 0) LADD(L_QC);
    if (w < 2) {
      LSPIN(L_QC, 9);
      float s = 0.f;
#pragma unroll
      for (int j = 0; j < 9; ++j) s += *(LAS float*)(lds + L_QP + j * 512 + tid * 4);
      *(LAS u16*)(lds + L_QB + tid * 2) = (u16)pk(s, s);
      LWAIT();
      if (lane == 0) LADD(L_QK);
    }
    __builtin_amdgcn_s_setprio(0);
    LSPIN(L_QK, 2);
  } else {
    if (w == 10) {
      __builtin_amdgcn_s_setprio(3);
      const rsrc_t rx = mk(a.x + b * (DM * 2), DM * 2);
      u32 xr[5];
#pragma unroll
      for (int i = 0; i < 5; ++i) xr[i] = LD32(rx, lane * 4 + i * 256, 0, KEEP);
      float s = 0.f;
#pragma unroll
      for (int i = 0; i < 5; ++i) s += lo16(xr[i]) + hi16(xr[i]);
      const float mean = sumrows(sum16(s)) * (1.f / DM);
      float ss = 0.f;
#pragma unroll
      for (int i = 0; i < 5; ++i) {
        const float d0 = lo16(xr[i]) - mean, d1 = hi16(xr[i]) - mean;
        ss += (i < 4 || lane < 32) ? d0 * d0 + d1 * d1 : 0.f;
      }
      const float var = sumrows(sum16(ss)) * (1.f / DM);
      const float m = rb(mean), den = rb(__builtin_amdgcn_sqrtf(rb(rb(var) + 1e-5f))), rd = __builtin_amdgcn_rcpf(den);
#pragma unroll
      for (int i = 0; i < 5; ++i)
        if (i < 4 || lane < 32) *(LAS u32*)(lds + L_XB + lane * 4 + i * 256) = pk(fdiv(rb(lo16(xr[i]) - m), den, rd), fdiv(rb(hi16(xr[i]) - m), den, rd));
      LWAIT();
      if (lane == 0) LPUT(L_XK, 1);
      __builtin_amdgcn_s_setprio(0);
    }
    LSPIN(L_QK, 2);
  }
  PIN();

  if (w < 9) {
    u32x4 wf[4][4];
    const rsrc_t ro = mk(a.wo + (size_t)h * (DH * DM * 2), DH * DM * 2);
    const int vo = r * 8 * (DM * 2) + w * 128 + c * 8;
    u32x2 raw[4][8];
#pragma unroll
    for (int n = 0; n < 32; ++n) {
      raw[n >> 3][n & 7] = LD64(ro, vo, ((n >> 3) * 32 + (n & 7)) * (DM * 2), KEEP);
      if (n >= 1) USE(raw[(n - 1) >> 3][(n - 1) & 7]);
      if (n >= 8 && (n & 7) == 0) tr8(raw[(n - 1) >> 3], wf[(n - 1) >> 3]);
      PIN();
    }
    tr8(raw[3], wf[3]);
    u32x8 wg[2][4];
    f32x4 d[4];
#pragma unroll
    for (int e = 0; e < 4; ++e) {
      wg[0][e] = __builtin_shufflevector(wf[0][e], wf[1][e], 0, 1, 2, 3, 4, 5, 6, 7);
      wg[1][e] = __builtin_shufflevector(wf[2][e], wf[3][e], 0, 1, 2, 3, 4, 5, 6, 7);
      d[e] = f32x4{0.f, 0.f, 0.f, 0.f};
      asm volatile("" : "+v"(wg[0][e]), "+v"(wg[1][e]), "+v"(d[e]));
    }
    int so = lane < 16 ? WS_ATT + blk * ATT_LD + w * 256 + c * 16 : NOWHERE, lo = L_OP + lane * 8, pick = (c & 1) ? 0xeeee : 0x4444;
    int lb = L_OB + w * 256 + (lane >> 3) * 32 + (lane & 1) * 16 + ((lane >> 1) & 3) * 4, lr = L_OB + w * 256 + r * 32 + (c & 1) * 16;
    asm volatile("" : "+v"(so), "+v"(lo), "+v"(lb), "+v"(lr), "+v"(pick));
    PIN();
    softmax();
    BAR();
    float o0, o1;
    {
      const u32x2 t0 = *(LAS u32x2*)(lds + lo), t1 = *(LAS u32x2*)(lds + lo + 512), t2 = *(LAS u32x2*)(lds + lo + 1024), t3 = *(LAS u32x2*)(lds + lo + 1536);
      o0 = (__uint_as_float(t0[0]) + __uint_as_float(t1[0])) + (__uint_as_float(t2[0]) + __uint_as_float(t3[0]));
      o1 = (__uint_as_float(t0[1]) + __uint_as_float(t1[1])) + (__uint_as_float(t2[1]) + __uint_as_float(t3[1]));
    }
    *(LAS u32*)(lds + lb) = pk(o0, o1);
    LWAIT();
    const u32x4 ob[2] = {*(LAS u32x4*)(lds + lr), *(LAS u32x4*)(lds + lr + 128)};
    PIN();
#pragma unroll
    for (int s = 0; s < 2; ++s)
#pragma unroll
      for (int e = 0; e < 4; ++e)
        d[e] = __builtin_amdgcn_smfmac_f32_16x16x64_bf16(__builtin_bit_cast(bf16x8, ob[s]), __builtin_bit_cast(bf16x16, wg[s][e]), d[e], pick, 0, 0);
    PIN();
    ST128((u32x4{min(__float_as_uint(d[0][0] + d[0][1]), EMPTY - 1), min(__float_as_uint(d[1][0] + d[1][1]), EMPTY - 1),
                 min(__float_as_uint(d[2][0] + d[2][1]), EMPTY - 1), min(__float_as_uint(d[3][0] + d[3][1]), EMPTY - 1)}), rws, so, 0, SC1);
    return;
  }
  if (w == 9) {
    if (job == 0) {
      {
        const rsrc_t rx = mk(a.x, NB * DM * 2);
        u32x4 xa[9];
#pragma unroll
        for (int i = 0; i < 9; ++i) xa[i] = LD128(rx, (lane >> 3) * (DM * 2) + (lane & 7) * 16, i * 128, NT);
        float s = 0.f;
#pragma unroll
        for (int i = 0; i < 9; ++i)
#pragma unroll
          for (int j = 0; j < 4; ++j) s += lo16(xa[i][j]) + hi16(xa[i][j]);
        s += dpp<X1>(s); s += dpp<X2>(s); s += dpp<HMIR>(s);
        const float mean = s * (1.f / DM);
        float ss = 0.f;
#pragma unroll
        for (int i = 0; i < 9; ++i)
#pragma unroll
          for (int j = 0; j < 4; ++j) { const float d0 = lo16(xa[i][j]) - mean, d1 = hi16(xa[i][j]) - mean; ss += d0 * d0 + d1 * d1; }
        ss += dpp<X1>(ss); ss += dpp<X2>(ss); ss += dpp<HMIR>(ss);
        const float m = rb(mean), den = rb(__builtin_amdgcn_sqrtf(rb(rb(ss * (1.f / DM)) + 1e-5f))), rd = __builtin_amdgcn_rcpf(den);
#pragma unroll
        for (int i = 0; i < 9; ++i) {
          u32x4 o;
#pragma unroll
          for (int j = 0; j < 4; ++j) o[j] = pk(fdiv(rb(lo16(xa[i][j]) - m), den, rd), fdiv(rb(hi16(xa[i][j]) - m), den, rd));
          *(LAS u32x4*)(lds + L_XN + (lane >> 3) * XN_LD + (lane & 7) * 16 + i * 128) = o;
        }
        LWAIT();
        PIN();
      }
      const rsrc_t r1 = mk(a.w1 + task * 128, DM * FF * 2 - task * 128), r3 = mk(a.w3 + task * 128, DM * FF * 2 - task * 128);
      const int vo = r * 8 * (FF * 2) + c * 8;
      const int xo = L_XN + (c & 7) * XN_LD + r * 16;
      u32x2 ra[8], rc[8];
      f32x4 g[4], u[4];
#pragma unroll
      for (int e = 0; e < 4; ++e) { g[e] = f32x4{0.f, 0.f, 0.f, 0.f}; u[e] = g[e]; }
#pragma unroll
      for (int i = 0; i < 8; ++i) ra[i] = LD64(r1, vo, i * (FF * 2), NT);
#pragma unroll
      for (int i = 0; i < 8 - MLP_GAP; ++i) rc[i] = LD64(r3, vo, i * (FF * 2), NT);
#define PAIR(x, y, d, rsx, sx, rsy, sy) { USE(x[2 * d + 1]); \
        _Pragma("unroll") for (int e = 0; e < 4; ++e) f[e][d] = __builtin_amdgcn_perm(x[2 * d + 1][e >> 1], x[2 * d][e >> 1], (e & 1) ? 0x07060302u : 0x05040100u); \
        PIN(); \
        _Pragma("unroll") for (int j = 2 * d - MLP_GAP; j < 2 * d + 2 - MLP_GAP; ++j) { \
          if (j < 0) y[8 + j] = LD64(rsy, vo, (sy + 8 + j) * (FF * 2), NT); else x[j] = LD64(rsx, vo, (sx + j) * (FF * 2), NT); } \
        PIN(); }
#pragma unroll 1
      for (int s = 0; s < 18; ++s) {
        const int sc_ = s * 32, sn = (s < 17 ? s + 1 : 17) * 32;
        const u32 nb = s < 17 ? DM * FF * 2 - task * 128 : 0;
        const rsrc_t n1 = mk(a.w1 + task * 128, nb), n3 = mk(a.w3 + task * 128, nb);
        const u32x4 xb = *(LAS u32x4*)(lds + xo + s * 64);
        u32x4 f[4];
        PAIR(ra, rc, 0, n1, sn, r3, sc_) PAIR(ra, rc, 1, n1, sn, r3, sc_) PAIR(ra, rc, 2, n1, sn, r3, sc_) PAIR(ra, rc, 3, n1, sn, r3, sc_)
#pragma unroll
        for (int e = 0; e < 4; ++e) g[e] = mfma(f[e], xb, g[e]);
        PIN();
        PAIR(rc, ra, 0, n3, sn, n1, sn) PAIR(rc, ra, 1, n3, sn, n1, sn) PAIR(rc, ra, 2, n3, sn, n1, sn) PAIR(rc, ra, 3, n3, sn, n1, sn)
#pragma unroll
        for (int e = 0; e < 4; ++e) u[e] = mfma(f[e], xb, u[e]);
        PIN();
      }
      float hv[4][4];
#pragma unroll
      for (int p = 0; p < 4; ++p)
#pragma unroll
        for (int e = 0; e < 4; ++e) hv[p][e] = rb(silu(rb(g[e][p]))) * rb(u[e][p]);
      const u32x4 h0 = {pk(hv[0][0], hv[0][1]), pk(hv[0][2], hv[0][3]), pk(hv[1][0], hv[1][1]), pk(hv[1][2], hv[1][3])};
      const u32x4 h1 = {pk(hv[2][0], hv[2][1]), pk(hv[2][2], hv[2][3]), pk(hv[3][0], hv[3][1]), pk(hv[3][2], hv[3][3])};
      ST128((c & 8) ? h1 : h0, rws, WS_HID + task * HID_LD + (c & 7) * 128 + r * 32 + (c >> 3) * 16, 0, SC1NT);
      DRAIN();
      while (!LGET(L_TK)) __builtin_amdgcn_s_sleep(2);
      asm volatile("" ::: "memory");
      if (lane == 0) ST64(*(LAS u32x2*)(lds + L_TG), rws, WS_HID + task * HID_LD + NB * 128, 0, SC1);
    }
    PIN();
    softmax();
    if (job == 1) {
      const int ks = task / 9, cg = task % 9;
      const rsrc_t r2 = mk(a.w2 + (size_t)ks * (576 * DM * 2) + cg * 128, 576 * DM * 2 - cg * 128);
      const int vo = r * 8 * (DM * 2) + c * 8;
      u32x2 ra[8], rc[8];
#pragma unroll
      for (int i = 0; i < 8; ++i) ra[i] = LD64(r2, vo, i * (DM * 2), NT);
      const u32x2 tg = *(LAS u32x2*)(lds + L_TG);
      bool ok = true;
      const u64 start = now();
      for (;;) {
        const u32x2 f = LD64(rws, WS_HID + NB * 128 + (ks * 9 + min(lane, 8)) * HID_LD, 0, SC1);
        if (__all(f[0] == tg[0] && f[1] == tg[1])) break;
        if (now() - start > SPIN_CAP) { ok = false; break; }
        __builtin_amdgcn_s_sleep(16);
      }
      asm volatile("" ::: "memory");
      {
        u32x4 t[9];
#pragma unroll
        for (int i = 0; i < 9; ++i) t[i] = LD128(rws, WS_HID + ks * 9 * HID_LD + lane * 16, i * HID_LD, SC1);
#pragma unroll
        for (int i = 0; i < 9; ++i) *(LAS u32x4*)(lds + L_HB + i * 1024 + lane * 16) = t[i];
      }
      LWAIT();
      const int ho = L_HB + (c & 7) * 128 + r * 16;
      f32x4 d[4];
#pragma unroll
      for (int e = 0; e < 4; ++e) d[e] = f32x4{0.f, 0.f, 0.f, 0.f};
#pragma unroll 1
      for (int s = 0; s < 9; ++s) {
        const rsrc_t n2 = mk(a.w2 + (size_t)ks * (576 * DM * 2) + cg * 128, s < 8 ? 576 * DM * 2 - cg * 128 : 0);
        const int sn = (s < 8 ? s + 1 : 8) * 64;
        const u32x4 hb = *(LAS u32x4*)(lds + ho + s * 1024), hc = *(LAS u32x4*)(lds + ho + s * 1024 + 64);
        u32x4 f[4];
#pragma unroll
        for (int i = 0; i < 8; ++i) { USE(ra[i]); rc[i] = LD64(r2, vo, (s * 64 + 32 + i) * (DM * 2), NT); PIN(); }
        tr8(ra, f);
#pragma unroll
        for (int e = 0; e < 4; ++e) d[e] = mfma(f[e], hb, d[e]);
        PIN();
#pragma unroll
        for (int i = 0; i < 8; ++i) { USE(rc[i]); ra[i] = LD64(n2, vo, (sn + i) * (DM * 2), NT); PIN(); }
        tr8(rc, f);
#pragma unroll
        for (int e = 0; e < 4; ++e) d[e] = mfma(f[e], hc, d[e]);
        PIN();
      }
#pragma unroll
      for (int p = 0; p < 2; ++p) {
        u32x4 o;
#pragma unroll
        for (int e = 0; e < 4; ++e) o[e] = __float_as_uint((c & 8) ? d[e][2 + p] : d[e][p]);
        *(LAS u32x4*)(lds + L_MO + (c & 7) * 256 + r * 64 + (c >> 3) * 32 + p * 16) = o;
      }
      LWAIT();
#pragma unroll
      for (int i = 0; i < 2; ++i) ST128(*(LAS u32x4*)(lds + L_MO + lane * 16 + i * 1024), rws, WS_MLP + task * MLP_LD + lane * 16, i * 1024, SC1NT);
      DRAIN();
      if (lane == 0 && ok) ST64(tg, rws, WS_MLP + task * MLP_LD + NB * 256, 0, SC1);
    }
    BAR();
    return;
  }
  PIN();
  softmax();
  if (h < NCONS) {
    bool ok = true;
    const u32x2 tg = *(LAS u32x2*)(lds + L_TG);
    const u32 t0 = tg[0], t1 = tg[1];
    const int col = h * 32 + (lane & 7) * 4;
    wait_batch(PRE_AT);
    int oo = (b * DM + col + r) * 2;
    const u32 xv = __builtin_amdgcn_raw_buffer_load_b16(mk(a.x, NB * DM * 2), oo, 0, NT);
    const u64 start = now();
    for (;;) {
      const u32x2 f1 = LD64(rws, WS_ATT + DM * 4 + (b * NH + (lane & 31)) * ATT_LD, 0, SC1);
      const u32x2 f2 = LD64(rws, WS_MLP + NB * 256 + ((lane & 7) * 9 + (h >> 1)) * MLP_LD, 0, SC1);
      bool all = f1[0] == t0 && f1[1] == t1 && f2[0] == t0 && f2[1] == t1;
      if (blk == 0) {
#pragma unroll
        for (int i = 0; i < 4; ++i) { const u32x2 f = LD64(rws, WS_ATT + DM * 4 + lane * ATT_LD, i * 64 * ATT_LD, SC1); all &= f[0] == t0 && f[1] == t1; }
      }
      if (__all(all)) break;
      if (now() - start > SPIN_CAP) { ok = false; break; }
      __builtin_amdgcn_s_sleep(16);
    }
    asm volatile("" ::: "memory");
    if (blk == 0 && lane == 0) ST64((u32x2{t0, t1}), rws, WS_EPOCH, 0, SC1);
    const u32x4 f = LD128(rws, WS_MLP + ((lane >> 3) * 9 + (h >> 1)) * MLP_LD + b * 256 + (h & 1) * 128 + (lane & 7) * 16, 0, SC1);
    float g = rb(silu(rb(sum8x4(__uint_as_float(f[0]), __uint_as_float(f[1]), __uint_as_float(f[2]), __uint_as_float(f[3]))))), xe = lo16(xv);
    int ao = WS_ATT + (b * NH + (lane >> 3)) * ATT_LD + col * 4;
    oo = c < 8 ? oo : NOWHERE;
    asm volatile("" : "+v"(g), "+v"(xe), "+v"(ao), "+v"(oo));
    const u32x4 dws = {(u32)(size_t)a.ws, (u32)((size_t)a.ws >> 32) & 0xffffu, WS_END, 0x00020000u};
    const u32x4 dout = {(u32)(size_t)a.out, (u32)((size_t)a.out >> 32) & 0xffffu, NB * DM * 2, 0x00020000u};
    BAR();
    if (ok) {
#define SWEEP(a0, a1, a2, a3) "buffer_load_dwordx4 " a0 ", %[ao], %[ws], 0 offen sc1\n\tbuffer_load_dwordx4 " a1 ", %[ao], %[ws], %[o1] offen sc1\n\t" \
                              "buffer_load_dwordx4 " a2 ", %[ao], %[ws], %[o2] offen sc1\n\tbuffer_load_dwordx4 " a3 ", %[ao], %[ws], %[o3] offen sc1\n\t"
      const u32 start2 = (u32)now();
      asm volatile(
        "s_waitcnt vmcnt(0)\n\t"
        SWEEP("v[64:67]", "v[68:71]", "v[72:75]", "v[76:79]")
        "s_sleep 0\nLsw%=:\n\t"
        SWEEP("v[80:83]", "v[84:87]", "v[88:91]", "v[92:95]")
        "s_waitcnt vmcnt(4)\n\t"
        "v_max3_u32 v96, v64, v65, v66\n\tv_max3_u32 v97, v67, v68, v69\n\tv_max3_u32 v98, v70, v71, v72\n\tv_max3_u32 v99, v73, v74, v75\n\t"
        "v_max3_u32 v96, v96, v76, v77\n\tv_max3_u32 v97, v97, v78, v79\n\tv_max3_u32 v96, v96, v97, v98\n\tv_max_u32 v96, v96, v99\n\t"
        "v_cmp_eq_u32 vcc, -1, v96\n\ts_nop 1\n\ts_cbranch_vccz LswA%=\n\t"
        SWEEP("v[64:67]", "v[68:71]", "v[72:75]", "v[76:79]")
        "s_waitcnt vmcnt(4)\n\t"
        "v_max3_u32 v96, v80, v81, v82\n\tv_max3_u32 v97, v83, v84, v85\n\tv_max3_u32 v98, v86, v87, v88\n\tv_max3_u32 v99, v89, v90, v91\n\t"
        "v_max3_u32 v96, v96, v92, v93\n\tv_max3_u32 v97, v97, v94, v95\n\tv_max3_u32 v96, v96, v97, v98\n\tv_max_u32 v96, v96, v99\n\t"
        "v_cmp_eq_u32 vcc, -1, v96\n\ts_nop 1\n\ts_cbranch_vccz LswB%=\n\t"
        "s_memrealtime vcc\n\ts_waitcnt lgkmcnt(0)\n\ts_sub_u32 vcc_lo, vcc_lo, %[st]\n\ts_cmp_lt_u32 vcc_lo, %[cap]\n\ts_cbranch_scc1 Lsw%=\n\t"
        "v_mov_b32 v96, 0x7fc0\n\ts_branch Lsws%=\n"
        "LswB%=:\n\t"
        "v_add_f32 v96, v80, v84\n\tv_add_f32 v88, v88, v92\n\tv_add_f32 v97, v81, v85\n\tv_add_f32 v89, v89, v93\n\t"
        "v_add_f32 v98, v82, v86\n\tv_add_f32 v90, v90, v94\n\tv_add_f32 v99, v83, v87\n\tv_add_f32 v91, v91, v95\n\t"
        "v_add_f32 v96, v96, v88\n\tv_add_f32 v97, v97, v89\n\tv_add_f32 v98, v98, v90\n\tv_add_f32 v99, v99, v91\n\t"
        "s_branch Lswf%=\n"
        "LswA%=:\n\t"
        "v_add_f32 v96, v64, v68\n\tv_add_f32 v72, v72, v76\n\tv_add_f32 v97, v65, v69\n\tv_add_f32 v73, v73, v77\n\t"
        "v_add_f32 v98, v66, v70\n\tv_add_f32 v74, v74, v78\n\tv_add_f32 v99, v67, v71\n\tv_add_f32 v75, v75, v79\n\t"
        "v_add_f32 v96, v96, v72\n\tv_add_f32 v97, v97, v73\n\tv_add_f32 v98, v98, v74\n\tv_add_f32 v99, v99, v75\n"
        "Lswf%=:\n\ts_nop 1\n\t"
        "v_permlane32_swap_b32 v96, v98\n\tv_permlane32_swap_b32 v97, v99\n\ts_nop 1\n\tv_add_f32 v96, v96, v98\n\tv_add_f32 v97, v97, v99\n\ts_nop 1\n\t"
        "v_permlane16_swap_b32 v96, v97\n\ts_nop 1\n\tv_add_f32 v96, v96, v97\n\ts_nop 1\n\t"
        "v_add_f32_dpp v96, v96, v96 row_ror:8 row_mask:0xf bank_mask:0xf\n\t"
        "v_cvt_pk_bf16_f32 v96, v96, v96\n\tv_lshlrev_b32 v96, 16, v96\n\tv_mul_f32 v96, v96, %[g]\n\t"
        "v_cvt_pk_bf16_f32 v96, v96, v96\n\tv_lshlrev_b32 v96, 16, v96\n\tv_add_f32 v96, %[x], v96\n\tv_cvt_pk_bf16_f32 v96, v96, v96\n"
        "Lsws%=:\n\tbuffer_store_short v96, %[oo], %[out], 0 offen\n\ts_waitcnt vmcnt(0)"
        :
        : [ao] "v"(ao), [ws] "s"(dws), [o1] "s"(8 * ATT_LD), [o2] "s"(16 * ATT_LD), [o3] "s"(24 * ATT_LD), [st] "s"(start2), [cap] "s"((u32)SPIN_CAP),
          [g] "v"(g), [x] "v"(xe), [oo] "v"(oo), [out] "s"(dout)
        : "memory", "vcc", "scc", "v64", "v65", "v66", "v67", "v68", "v69", "v70", "v71", "v72", "v73", "v74", "v75", "v76", "v77", "v78", "v79", "v80", "v81",
          "v82", "v83", "v84", "v85", "v86", "v87", "v88", "v89", "v90", "v91", "v92", "v93", "v94", "v95", "v96", "v97", "v98", "v99");
    } else {
      __builtin_amdgcn_raw_buffer_store_b16((u16)0x7fc0, mk(a.out, NB * DM * 2), oo, 0, 0);
    }
  } else {
    BAR();
  }
}

}

extern "C" void kernel_launch(void* const* d_in, const int* in_sizes, int n_in, void* d_out, int out_size, void* d_ws, size_t ws_size,
                              hipStream_t stream) {
  Args a;
  a.x = (const char*)d_in[0]; a.k = (const char*)d_in[1]; a.v = (const char*)d_in[2]; a.wq = (const char*)d_in[3]; a.wo = (const char*)d_in[4];
  a.w1 = (const char*)d_in[6]; a.w3 = (const char*)d_in[7]; a.w2 = (const char*)d_in[8];
  a.out = (char*)d_out; a.ws = (char*)d_ws;
  hipLaunchKernelGGL(fused_decode, dim3(NB * NH), dim3(1024), 0, stream, a);
}
```

```cpp
#include <hip/hip_runtime.h>
#include <hip/hip_bf16.h>
#include <cstdint>

namespace {

typedef unsigned u32;
typedef unsigned long long u64;
typedef unsigned short u16;
typedef u32 u32x4 __attribute__((ext_vector_type(4)));
typedef u32 u32x2 __attribute__((ext_vector_type(2)));
typedef float f32x4 __attribute__((ext_vector_type(4)));
typedef __bf16 bf16x8 __attribute__((ext_vector_type(8)));
typedef __bf16 bf16x2 __attribute__((ext_vector_type(2)));
typedef __bf16 bf16x16 __attribute__((ext_vector_type(16)));
typedef u32 u32x8 __attribute__((ext_vector_type(8)));
#define LAS __attribute__((address_space(3)))

constexpr int NB = 8, DM = 576, NH = 32, SK = 4096, DH = 128, FF = 4608;
constexpr int NTASK = FF / 64;
constexpr int NCONS = DM / 32;
constexpr int KEEP = 0, NT = 2, SC1 = 16, SC1NT = 18;

constexpr int WIN0 = 41, WIN_MLP = 2;
constexpr int WMID = 45, WLO = 36, WHI = 56, GAIN = 20;
constexpr int OPENW = 10;
constexpr u32 PACE_K = 0x19110903u, PACE_V = 0x39332b23u;
constexpr int PACE_DLY = 3;
constexpr int PRE_AT = 58;
constexpr int MLP_GAP = 6;
constexpr u64 SPIN_CAP = 5000000;

constexpr u32 ATT_LD = DM * 4 + 128, HID_LD = NB * 128 + 128, MLP_LD = NB * 256 + 128, PACE_LD = 4352;
constexpr u32 WS_EPOCH = 0, WS_ATT = 4096, WS_HID = WS_ATT + NB * NH * ATT_LD, WS_MLP = WS_HID + NTASK * HID_LD, WS_PACE = WS_MLP + NTASK * MLP_LD,
              WS_END = WS_PACE + 8 * 64 * PACE_LD;
constexpr u32 EMPTY = 0xffffffffu;
constexpr int NOWHERE = 0x40000000;

constexpr int XN_LD = 1168;
constexpr int L_XN = 0, L_QP = L_XN + NB * XN_LD, L_QB = L_QP + 9 * DH * 4, L_PS = L_QB + DH * 2, L_WM = L_PS + SK * 2, L_WS = L_WM + 64,
              L_OP = L_WS + 64, L_OB = L_OP + 4 * DH * 4, L_TG = L_OB + 9 * DH * 2, L_PG = L_TG + 8, L_TK = L_TG + 12, L_WN = L_TG + 16,
              L_XK = L_TG + 24, L_QC = L_TG + 28, L_QK = L_TG + 32, L_XB = L_TG + 48, L_MO = L_XB + DM * 2, L_HB = L_MO + NB * 256,
              L_END = L_HB + 9 * 1024;

struct Args { const char* x; const char* k; const char* v; const char* wq; const char* wo; const char* w1; const char* w3; const char* w2; char* out; char* ws; };

typedef __amdgpu_buffer_rsrc_t rsrc_t;
__device__ __forceinline__ rsrc_t mk(const char* p, u32 n) { return __builtin_amdgcn_make_buffer_rsrc((void*)p, 0, n, 0x00020000); }
#define LD128(r, vo, so, aux) __builtin_amdgcn_raw_buffer_load_b128(r, vo, so, aux)
#define LD64(r, vo, so, aux) __builtin_amdgcn_raw_buffer_load_b64(r, vo, so, aux)
#define LD32(r, vo, so, aux) __builtin_amdgcn_raw_buffer_load_b32(r, vo, so, aux)
#define ST128(v, r, vo, so, aux) __builtin_amdgcn_raw_buffer_store_b128(v, r, vo, so, aux)
#define ST64(v, r, vo, so, aux) __builtin_amdgcn_raw_buffer_store_b64(v, r, vo, so, aux)
#define PIN() __builtin_amdgcn_sched_barrier(0)
#define USE(x) asm volatile("" ::"v"(x) : "memory")
#define BAR() asm volatile("s_waitcnt lgkmcnt(0)\n\ts_barrier" ::: "memory")
#define RAWBAR() asm volatile("s_barrier" ::: "memory")
#define LWAIT() asm volatile("s_waitcnt lgkmcnt(0)" ::: "memory")
#define DRAIN() asm volatile("s_waitcnt vmcnt(0)" ::: "memory")
#define TOUCH(d, p) asm volatile("s_load_dword %0, %1, 0x0" : "=&s"(d) : "s"(p) : "memory")
#define LGET(off) __hip_atomic_load((LAS u32*)(lds + (off)), __ATOMIC_RELAXED, __HIP_MEMORY_SCOPE_WORKGROUP)
#define LPUT(off, v) __hip_atomic_store((LAS u32*)(lds + (off)), (u32)(v), __ATOMIC_RELAXED, __HIP_MEMORY_SCOPE_WORKGROUP)
#define LADD(off) __hip_atomic_fetch_add((LAS u32*)(lds + (off)), 1u, __ATOMIC_RELAXED, __HIP_MEMORY_SCOPE_WORKGROUP)
#define LSPIN(off, n) do { while (LGET(off) < (n)) __builtin_amdgcn_s_sleep(1); asm volatile("" ::: "memory"); } while (0)
#define DITH(i) (((i) & 1) * 4 + ((i) & 2) + ((i) >> 2))
#define GATE(i) asm volatile("s_cmp_gt_i32 %0, %1\n\ts_cbranch_scc1 Lg7_%=\n\ts_cmp_gt_i32 %0, %2\n\ts_cbranch_scc1 Lg5_%=\n\t" \
                             "s_cmp_gt_i32 %0, %3\n\ts_cbranch_scc1 Lg4_%=\n\ts_waitcnt vmcnt(3)\nLg4_%=:\n\ts_waitcnt vmcnt(4)\n" \
                             "Lg5_%=:\n\ts_waitcnt vmcnt(5)\nLg7_%=:" \
                             ::"s"(win), "i"(48 + DITH(i)), "i"(40 + DITH(i)), "i"(32 + DITH(i)) : "scc", "memory")
#define EIGHT(STEP) STEP(0) STEP(1) STEP(2) STEP(3) STEP(4) STEP(5) STEP(6) STEP(7)

__device__ __forceinline__ u32 pk(float lo, float hi) { bf16x2 t = {(__bf16)lo, (__bf16)hi}; return __builtin_bit_cast(u32, t); }
__device__ __forceinline__ float rb(float x) { return __uint_as_float(pk(x, x) << 16); }
__device__ __forceinline__ float lo16(u32 v) { return __uint_as_float(v << 16); }
__device__ __forceinline__ float hi16(u32 v) { return __uint_as_float(v & 0xffff0000u); }
__device__ __forceinline__ float dot2(u32 a, u32 b, float c) {
  return __builtin_amdgcn_fdot2_f32_bf16(__builtin_bit_cast(bf16x2, a), __builtin_bit_cast(bf16x2, b), c, false);
}
__device__ __forceinline__ f32x4 mfma(u32x4 a, u32x4 b, f32x4 c) {
  return __builtin_amdgcn_mfma_f32_16x16x32_bf16(__builtin_bit_cast(bf16x8, a), __builtin_bit_cast(bf16x8, b), c, 0, 0, 0);
}
template <int CTRL> __device__ __forceinline__ float dpp(float x) {
  return __builtin_bit_cast(float, __builtin_amdgcn_mov_dpp(__builtin_bit_cast(int, x), CTRL, 0xf, 0xf, true));
}
constexpr int X1 = 0xB1, X2 = 0x4E, HMIR = 0x141, MIR = 0x140, ROR8 = 0x128;
__device__ __forceinline__ float sum16(float x) { x += dpp<X1>(x); x += dpp<X2>(x); x += dpp<HMIR>(x); x += dpp<MIR>(x); return x; }
__device__ __forceinline__ float max16(float x) { x = fmaxf(x, dpp<X1>(x)); x = fmaxf(x, dpp<X2>(x)); x = fmaxf(x, dpp<HMIR>(x)); x = fmaxf(x, dpp<MIR>(x)); return x; }
__device__ __forceinline__ float sumrows(float x) {
  auto s = __builtin_amdgcn_permlane16_swap(__float_as_uint(x), __float_as_uint(x), false, false);
  x = __uint_as_float(s[0]) + __uint_as_float(s[1]);
  auto t = __builtin_amdgcn_permlane32_swap(__float_as_uint(x), __float_as_uint(x), false, false);
  return __uint_as_float(t[0]) + __uint_as_float(t[1]);
}
__device__ __forceinline__ float maxrows(float x) {
  auto s = __builtin_amdgcn_permlane16_swap(__float_as_uint(x), __float_as_uint(x), false, false);
  x = fmaxf(__uint_as_float(s[0]), __uint_as_float(s[1]));
  auto t = __builtin_amdgcn_permlane32_swap(__float_as_uint(x), __float_as_uint(x), false, false);
  return fmaxf(__uint_as_float(t[0]), __uint_as_float(t[1]));
}
__device__ __forceinline__ float sum8x4(float v0, float v1, float v2, float v3) {
  auto p = __builtin_amdgcn_permlane32_swap(__float_as_uint(v0), __float_as_uint(v2), false, false);
  auto q = __builtin_amdgcn_permlane32_swap(__float_as_uint(v1), __float_as_uint(v3), false, false);
  auto t = __builtin_amdgcn_permlane16_swap(__float_as_uint(__uint_as_float(p[0]) + __uint_as_float(p[1])), __float_as_uint(__uint_as_float(q[0]) + __uint_as_float(q[1])), false, false);
  const float x = __uint_as_float(t[0]) + __uint_as_float(t[1]);
  return x + dpp<ROR8>(x);
}
__device__ __forceinline__ float fdiv(float a, float d, float rd) { const float q = a * rd; return fmaf(fmaf(-q, d, a), rd, q); }
__device__ __forceinline__ float silu(float x) { const float d = 1.f + __expf(-x); return d < INFINITY ? fdiv(x, d, __builtin_amdgcn_rcpf(d)) : x * 0.f; }
__device__ __forceinline__ u64 now() { return __builtin_amdgcn_s_memrealtime(); }

struct Diag { u32 s[2][4]; };
__device__ __forceinline__ Diag make_diag(int c) {
  Diag d;
#pragma unroll
  for (int par = 0; par < 2; ++par)
#pragma unroll
    for (int w = 0; w < 4; ++w) {
      const u32 b = par ? 0x0302u : 0x0100u;
      d.s[par][w] = (c >> 1) == w ? ((c & 1) ? (b << 16) | 0x0c0cu : 0x0c0c0000u | b) : 0x0c0c0c0cu;
    }
  return d;
}
template <int PAR> __device__ __forceinline__ f32x4 axpy4(u32 val, u32x4 rows, const Diag& d, f32x4 acc) {
  u32x4 af;
#pragma unroll
  for (int w = 0; w < 4; ++w) af[w] = __builtin_amdgcn_perm(val, val, d.s[PAR][w]);
  return mfma(af, rows, acc);
}
__device__ __forceinline__ float score4(u32x4 k, u32x4 q) {
  float d = dot2(k[0], q[0], 0.f); d = dot2(k[1], q[1], d); d = dot2(k[2], q[2], d); d = dot2(k[3], q[3], d);
  return sum16(d);
}
__device__ __forceinline__ void tr8(const u32x2 (&raw)[8], u32x4 (&f)[4]) {
#pragma unroll
  for (int e = 0; e < 4; ++e)
#pragma unroll
    for (int d = 0; d < 4; ++d) f[e][d] = __builtin_amdgcn_perm(raw[2 * d + 1][e >> 1], raw[2 * d][e >> 1], (e & 1) ? 0x07060302u : 0x05040100u);
}

__global__ void __launch_bounds__(1024, 4) fused_decode(Args a) {
  __shared__ __attribute__((aligned(16))) char lds_[L_END];
  LAS char* lds = (LAS char*)lds_;
  if (threadIdx.x == 640) { LPUT(L_PG, 0); LPUT(L_TK, 0); LPUT(L_WN, 0); LPUT(L_WN + 4, 0); LPUT(L_XK, 0); LPUT(L_QC, 0); LPUT(L_QK, 0); LWAIT(); }
  PIN(); RAWBAR(); PIN();
  asm volatile("" ::"s"(a.x), "s"(a.ws));
  const int tid = threadIdx.x, lane = tid & 63, w = __builtin_amdgcn_readfirstlane(tid >> 6), r = lane >> 4, c = lane & 15;
  const int blk = blockIdx.x, b = blk >> 5, h = blk & 31;
  const int hrot = (h - b) & 31, job = hrot / 9, task = (h & 7) * 9 + (hrot % 9 < 8 ? b : 8);
  const char* kbase = a.k + ((size_t)blk << 20);
  const char* vbase = a.v + ((size_t)blk << 20);
  const rsrc_t rk = mk(kbase, 1u << 20);
  const int sw = w & 3;
  const int vA = lane * 16 + sw * 4096;
  auto kvbase = [](int beta) { return (beta >> 1) * 65536 + (beta & 1) * 16384; };
#define KVLD(rs, base, i) LD128(rs, vA + ((i) & 1) * 32768 + (((i) >> 1) ^ sw) * 1024, base, NT)
  float mx = -INFINITY;
  auto put_scores = [&](float sc, int batch) {
    const float s = rb(rb(sc) * 0.08838834764831845f);
    mx = fmaxf(mx, s);
    if (c < 8) *(LAS u16*)(lds + L_PS + batch * 64 + r * 16 + c * 2) = (u16)(__float_as_uint(s) >> 16);
  };
  auto softmax = [&]() {
    const float wm = maxrows(max16(mx));
    if (lane == 0) *(LAS float*)(lds + L_WM + w * 4) = wm;
    BAR();
    const float M = max16(*(LAS float*)(lds + L_WM + c * 4));
    const u32x2 sv = *(LAS u32x2*)(lds + L_PS + tid * 8);
    const float e0 = __expf(lo16(sv[0]) - M), e1 = __expf(hi16(sv[0]) - M), e2 = __expf(lo16(sv[1]) - M), e3 = __expf(hi16(sv[1]) - M);
    const float ws = sumrows(sum16((e0 + e1) + (e2 + e3)));
    if (lane == 0) *(LAS float*)(lds + L_WS + w * 4) = ws;
    BAR();
    const float inv = 1.f / sum16(*(LAS float*)(lds + L_WS + c * 4));
    *(LAS u32x2*)(lds + L_PS + tid * 8) = u32x2{pk(e0 * inv, e1 * inv), pk(e2 * inv, e3 * inv)};
    BAR();
  };

  if (w >= 12) {
    u32x4 ring[8];
    u32x4 ext[8];
    __builtin_amdgcn_s_sleep(32);
#pragma unroll
    for (int i = 0; i < 16; ++i) {
      if (i >= OPENW) USE((i - OPENW < 8 ? ring : ext)[(i - OPENW) & 7]);
      if (i < 8) ring[i] = KVLD(rk, kvbase(0), i); else ext[i - 8] = KVLD(rk, kvbase(1), i - 8);
      PIN();
    }
    __builtin_amdgcn_s_setprio(3);
    LSPIN(L_QK, 2);
    const u32x4 q = *(LAS u32x4*)(lds + L_QB + c * 16);
    float sc = 0.f;
    const int win0 = WIN0 + (job == 0 ? WIN_MLP : 0);
    int win = win0;
#define PACE_IN() const u32 tj = (u32)now(); const u64 sch = __hip_atomic_load((LAS u64*)(lds + L_WN), __ATOMIC_RELAXED, __HIP_MEMORY_SCOPE_WORKGROUP)
#define PACE_OUT(j) do { const u32 due_ = __builtin_amdgcn_readfirstlane((u32)sch), tau_ = __builtin_amdgcn_readfirstlane((u32)(sch >> 32)); \
      const int late_ = min(max((int)(tj * 256u - due_ - tau_ * (u32)(j)) >> 8, -4096), 4096); \
      win = tau_ ? min(max(WMID + ((late_ * GAIN) >> 8), WLO), WHI) : win0; } while (0)
#pragma unroll
    for (int i = 0; i < 8; ++i) { const float d = score4(ring[i], q); sc = (c & 7) == i ? d : sc; PIN(); }
    put_scores(sc, sw);
#define FSTEP(i) { if (i > 0) { GATE(i); ring[(i + 7) & 7] = KVLD(rk, kvbase(2), (i + 7) & 7); } PIN(); const float d = score4(ext[i], q); sc = (c & 7) == i ? d : sc; PIN(); }
    EIGHT(FSTEP)
    put_scores(sc, 4 + sw);
#pragma unroll 1
    for (int beta = 2; beta < 32; ++beta) {
      const rsrc_t rn = mk(beta < 31 ? kbase : vbase, 1u << 20);
      const int sc0 = __builtin_amdgcn_readfirstlane(kvbase(beta)), sn0 = __builtin_amdgcn_readfirstlane(kvbase(beta < 31 ? beta + 1 : 0));
      if (beta == 31) win = 64;
      PACE_IN();
      if (tid == 768) LPUT(L_PG, beta);
#define KSTEP(i) { GATE(i); if (i == 0) ring[7] = KVLD(rk, sc0, 7); else ring[(i + 7) & 7] = KVLD(rn, sn0, (i + 7) & 7); PIN(); \
        const float d = score4(ring[i], q); sc = (c & 7) == i ? d : sc; PIN(); }
      EIGHT(KSTEP)
      put_scores(sc, beta * 4 + sw);
      PACE_OUT(beta);
    }
    const rsrc_t rv = mk(vbase, 1u << 20);
    ring[7] = KVLD(rv, kvbase(0), 7);
#pragma unroll
    for (int i = 0; i < 8; ++i) { ext[i] = KVLD(rv, kvbase(1), i); PIN(); }
    softmax();
    const Diag dg = make_diag(c);
    f32x4 acc = {0.f, 0.f, 0.f, 0.f};
    const int po = L_PS + sw * 64 + r * 16;
    u32x4 pn = *(LAS u32x4*)(lds + po + 512);
    {
      const u32x4 p0 = *(LAS u32x4*)(lds + po), p1 = *(LAS u32x4*)(lds + po + 256);
#pragma unroll
      for (int i = 0; i < 8; ++i) { acc = (i & 1) ? axpy4<1>(p0[i >> 1], ring[i], dg, acc) : axpy4<0>(p0[i >> 1], ring[i], dg, acc); PIN(); }
#define ESTEP(i) { if (i > 0) { GATE(i); ring[(i + 7) & 7] = KVLD(rv, kvbase(2), (i + 7) & 7); } PIN(); acc = axpy4<i & 1>(p1[i >> 1], ext[i], dg, acc); PIN(); }
      EIGHT(ESTEP)
    }
#pragma unroll 1
    for (int beta = 2; beta < 32; ++beta) {
      const rsrc_t rn = mk(vbase, beta < 31 ? 1u << 20 : 0u);
      const int bn = beta < 31 ? beta + 1 : 0;
      const int sc0 = __builtin_amdgcn_readfirstlane(kvbase(beta)), sn0 = __builtin_amdgcn_readfirstlane(kvbase(bn));
      if (beta == 31) win = 64;
      const u32x4 pv = pn;
      pn = *(LAS u32x4*)(lds + po + bn * 256);
      PACE_IN();
      if (tid == 768) LPUT(L_PG, 32 + beta);
#define VSTEP(i) { GATE(i); if (i == 0) ring[7] = KVLD(rv, sc0, 7); else ring[(i + 7) & 7] = KVLD(rn, sn0, (i + 7) & 7); PIN(); \
        acc = axpy4<i & 1>(pv[i >> 1], ring[i], dg, acc); PIN(); }
      EIGHT(VSTEP)
      PACE_OUT(32 + beta);
    }
    if (r < 2) *(LAS f32x4*)(lds + L_OP + sw * 512 + c * 32 + r * 16) = acc;
    BAR();
    return;
  }

  const rsrc_t rws = mk(a.ws, WS_END);
  auto wait_batch = [&](u32 n) { while (LGET(L_PG) < n) __builtin_amdgcn_s_sleep(2); };
  if (w == 11) {
    u32 t0, t1, t2, t3, t4, t5, t6, t7;
    asm volatile("" ::"s"(a.ws), "s"(a.out), "s"(kbase), "s"(vbase), "s"(a.w2));
    TOUCH(t0, kbase); TOUCH(t1, a.ws);
    __builtin_amdgcn_s_sleep(32);
    TOUCH(t2, a.wo + (size_t)h * (DH * DM * 2)); TOUCH(t3, a.w1); TOUCH(t4, a.w3); TOUCH(t5, vbase); TOUCH(t6, a.w2); TOUCH(t7, a.out);
    const u32x2 ep = LD64(rws, WS_EPOCH, 0, SC1);
    if (lane < 48) {
#pragma unroll
      for (int i = 0; i < 3; ++i) ST128((u32x4{EMPTY, EMPTY, EMPTY, EMPTY}), rws, WS_ATT + blk * ATT_LD + lane * 16 + i * 768, 0, SC1);
    }
    asm volatile("s_waitcnt lgkmcnt(0)" : "+s"(t0), "+s"(t1), "+s"(t2), "+s"(t3), "+s"(t4), "+s"(t5), "+s"(t6), "+s"(t7)::"memory");
    const u64 tag = (((u64)(u32)__builtin_amdgcn_readfirstlane(ep[1]) << 32) | (u32)__builtin_amdgcn_readfirstlane(ep[0])) + 1;
    DRAIN();
    if (lane == 0) {
      *(LAS u64*)(lds + L_TG) = tag;
      LWAIT();
      LPUT(L_TK, 1);
      ST64((u32x2{(u32)tag, (u32)(tag >> 32)}), rws, WS_ATT + blk * ATT_LD + DM * 4, 0, SC1);
    }
    const u32 tb = (u32)now();
    const bool sample = ((blk >> 3) & 3) == (blk & 3);
    float mprev = 0.f;
    int atprev = 0;
    auto pace = [&](int k, int at) {
      wait_batch(at);
      const u32 tk = (u32)now();
      if (sample && lane < 8) ST128((u32x4{tk, (u32)tag, tk, (u32)tag}), rws, WS_PACE + (k * 64 + b * 8 + (blk & 7)) * PACE_LD + lane * 16, 0, SC1NT);
      wait_batch(at + PACE_DLY);
      const u32x2 v = LD64(rws, WS_PACE + (k * 64 + ((b + lane + blk) & 7) * 8 + (lane & 7)) * PACE_LD, 0, SC1);
      const bool in = v[1] == (u32)tag && lane < 8;
      const float n = (float)__popcll(__ballot(in));
      const float m = (float)(int)(tk - tb) + sum16(in ? (float)(int)(v[0] - tk) : 0.f) * __builtin_amdgcn_rcpf(fmaxf(n, 1.f));
      if (k > 0 && lane == 0) {
        const float tau = (m - mprev) * __builtin_amdgcn_rcpf((float)(at - atprev)) * 0.99f;
        const u32 tau256 = (u32)fminf(fmaxf(tau * 256.f, 1.f), 1e6f);
        __hip_atomic_store((LAS u64*)(lds + L_WN), ((u64)tau256 << 32) | (tb * 256u + (u32)(int)(m * 256.f) - tau256 * (u32)at), __ATOMIC_RELAXED, __HIP_MEMORY_SCOPE_WORKGROUP);
      }
      mprev = m; atprev = at;
    };
#pragma unroll 1
    for (int k = 0; k < 4; ++k) pace(k, (PACE_K >> (8 * k)) & 255);
    softmax();
#pragma unroll 1
    for (int k = 0; k < 4; ++k) pace(4 + k, (PACE_V >> (8 * k)) & 255);
    BAR();
    return;
  }
  if (w < 9) {
    __builtin_amdgcn_s_setprio(1);
    const int wr = (w + b) % 9;
    u32x4 wb[16];
    {
      const rsrc_t rq = mk(a.wq + h * 256, DM * 8192 - h * 256);
      const int vo = (64 * wr + 16 * r) * 8192 + c * 16;
#pragma unroll
      for (int i = 0; i < 16; ++i) wb[i] = LD128(rq, vo, i * 8192, KEEP);
    }
    PIN();
    const Diag dgq = make_diag(c);
    LSPIN(L_XK, 1);
    const u32x4 x0 = *(LAS u32x4*)(lds + L_XB + (64 * wr + 16 * r) * 2), x1 = *(LAS u32x4*)(lds + L_XB + (64 * wr + 16 * r) * 2 + 16);
    PIN();
    {
      f32x4 acc0 = {0.f, 0.f, 0.f, 0.f}, acc1 = acc0;
#pragma unroll
      for (int i = 0; i < 8; ++i) {
        const u32 xv = i < 4 ? x0[i & 3] : x1[i & 3];
        acc0 = axpy4<0>(xv, wb[2 * i], dgq, acc0); acc1 = axpy4<1>(xv, wb[2 * i + 1], dgq, acc1);
      }
      if (r < 2) *(LAS f32x4*)(lds + L_QP + wr * 512 + c * 32 + r * 16) = acc0 + acc1;
    }
    LWAIT();
    if (lane == 0) LADD(L_QC);
    if (w < 2) {
      LSPIN(L_QC, 9);
      float s = 0.f;
#pragma unroll
      for (int j = 0; j < 9; ++j) s += *(LAS float*)(lds + L_QP + j * 512 + tid * 4);
      *(LAS u16*)(lds + L_QB + tid * 2) = (u16)pk(s, s);
      LWAIT();
      if (lane == 0) LADD(L_QK);
    }
    __builtin_amdgcn_s_setprio(0);
    LSPIN(L_QK, 2);
  } else {
    if (w == 10) {
      __builtin_amdgcn_s_setprio(3);
      const rsrc_t rx = mk(a.x + b * (DM * 2), DM * 2);
      u32 xr[5];
#pragma unroll
      for (int i = 0; i < 5; ++i) xr[i] = LD32(rx, lane * 4 + i * 256, 0, KEEP);
      float s = 0.f;
#pragma unroll
      for (int i = 0; i < 5; ++i) s += lo16(xr[i]) + hi16(xr[i]);
      const float mean = sumrows(sum16(s)) * (1.f / DM);
      float ss = 0.f;
#pragma unroll
      for (int i = 0; i < 5; ++i) {
        const float d0 = lo16(xr[i]) - mean, d1 = hi16(xr[i]) - mean;
        ss += (i < 4 || lane < 32) ? d0 * d0 + d1 * d1 : 0.f;
      }
      const float var = sumrows(sum16(ss)) * (1.f / DM);
      const float m = rb(mean), den = rb(__builtin_amdgcn_sqrtf(rb(rb(var) + 1e-5f))), rd = __builtin_amdgcn_rcpf(den);
#pragma unroll
      for (int i = 0; i < 5; ++i)
        if (i < 4 || lane < 32) *(LAS u32*)(lds + L_XB + lane * 4 + i * 256) = pk(fdiv(rb(lo16(xr[i]) - m), den, rd), fdiv(rb(hi16(xr[i]) - m), den, rd));
      LWAIT();
      if (lane == 0) LPUT(L_XK, 1);
      __builtin_amdgcn_s_setprio(0);
    }
    LSPIN(L_QK, 2);
  }
  PIN();

  if (w < 9) {
    u32x4 wf[4][4];
    const rsrc_t ro = mk(a.wo + (size_t)h * (DH * DM * 2), DH * DM * 2);
    const int vo = r * 8 * (DM * 2) + w * 128 + c * 8;
    u32x2 raw[4][8];
#pragma unroll
    for (int n = 0; n < 32; ++n) {
      raw[n >> 3][n & 7] = LD64(ro, vo, ((n >> 3) * 32 + (n & 7)) * (DM * 2), KEEP);
      if (n >= 1) USE(raw[(n - 1) >> 3][(n - 1) & 7]);
      if (n >= 8 && (n & 7) == 0) tr8(raw[(n - 1) >> 3], wf[(n - 1) >> 3]);
      PIN();
    }
    tr8(raw[3], wf[3]);
    u32x8 wg[2][4];
    f32x4 d[4];
#pragma unroll
    for (int e = 0; e < 4; ++e) {
      wg[0][e] = __builtin_shufflevector(wf[0][e], wf[1][e], 0, 1, 2, 3, 4, 5, 6, 7);
      wg[1][e] = __builtin_shufflevector(wf[2][e], wf[3][e], 0, 1, 2, 3, 4, 5, 6, 7);
      d[e] = f32x4{0.f, 0.f, 0.f, 0.f};
      asm volatile("" : "+v"(wg[0][e]), "+v"(wg[1][e]), "+v"(d[e]));
    }
    int so = lane < 16 ? WS_ATT + blk * ATT_LD + w * 256 + c * 16 : NOWHERE, lo = L_OP + lane * 8, pick = (c & 1) ? 0xeeee : 0x4444;
    int lb = L_OB + w * 256 + (lane >> 3) * 32 + (lane & 1) * 16 + ((lane >> 1) & 3) * 4, lr = L_OB + w * 256 + r * 32 + (c & 1) * 16;
    asm volatile("" : "+v"(so), "+v"(lo), "+v"(lb), "+v"(lr), "+v"(pick));
    PIN();
    softmax();
    BAR();
    float o0, o1;
    {
      const u32x2 t0 = *(LAS u32x2*)(lds + lo), t1 = *(LAS u32x2*)(lds + lo + 512), t2 = *(LAS u32x2*)(lds + lo + 1024), t3 = *(LAS u32x2*)(lds + lo + 1536);
      o0 = (__uint_as_float(t0[0]) + __uint_as_float(t1[0])) + (__uint_as_float(t2[0]) + __uint_as_float(t3[0]));
      o1 = (__uint_as_float(t0[1]) + __uint_as_float(t1[1])) + (__uint_as_float(t2[1]) + __uint_as_float(t3[1]));
    }
    *(LAS u32*)(lds + lb) = pk(o0, o1);
    LWAIT();
    const u32x4 ob[2] = {*(LAS u32x4*)(lds + lr), *(LAS u32x4*)(lds + lr + 128)};
    PIN();
#pragma unroll
    for (int s = 0; s < 2; ++s)
#pragma unroll
      for (int e = 0; e < 4; ++e)
        d[e] = __builtin_amdgcn_smfmac_f32_16x16x64_bf16(__builtin_bit_cast(bf16x8, ob[s]), __builtin_bit_cast(bf16x16, wg[s][e]), d[e], pick, 0, 0);
    PIN();
    ST128((u32x4{min(__float_as_uint(d[0][0] + d[0][1]), EMPTY - 1), min(__float_as_uint(d[1][0] + d[1][1]), EMPTY - 1),
                 min(__float_as_uint(d[2][0] + d[2][1]), EMPTY - 1), min(__float_as_uint(d[3][0] + d[3][1]), EMPTY - 1)}), rws, so, 0, SC1);
    return;
  }
  if (w == 9) {
    if (job == 0) {
      {
        const rsrc_t rx = mk(a.x, NB * DM * 2);
        u32x4 xa[9];
#pragma unroll
        for (int i = 0; i < 9; ++i) xa[i] = LD128(rx, (lane >> 3) * (DM * 2) + (lane & 7) * 16, i * 128, NT);
        float s = 0.f;
#pragma unroll
        for (int i = 0; i < 9; ++i)
#pragma unroll
          for (int j = 0; j < 4; ++j) s += lo16(xa[i][j]) + hi16(xa[i][j]);
        s += dpp<X1>(s); s += dpp<X2>(s); s += dpp<HMIR>(s);
        const float mean = s * (1.f / DM);
        float ss = 0.f;
#pragma unroll
        for (int i = 0; i < 9; ++i)
#pragma unroll
          for (int j = 0; j < 4; ++j) { const float d0 = lo16(xa[i][j]) - mean, d1 = hi16(xa[i][j]) - mean; ss += d0 * d0 + d1 * d1; }
        ss += dpp<X1>(ss); ss += dpp<X2>(ss); ss += dpp<HMIR>(ss);
        const float m = rb(mean), den = rb(__builtin_amdgcn_sqrtf(rb(rb(ss * (1.f / DM)) + 1e-5f))), rd = __builtin_amdgcn_rcpf(den);
#pragma unroll
        for (int i = 0; i < 9; ++i) {
          u32x4 o;
#pragma unroll
          for (int j = 0; j < 4; ++j) o[j] = pk(fdiv(rb(lo16(xa[i][j]) - m), den, rd), fdiv(rb(hi16(xa[i][j]) - m), den, rd));
          *(LAS u32x4*)(lds + L_XN + (lane >> 3) * XN_LD + (lane & 7) * 16 + i * 128) = o;
        }
        LWAIT();
        PIN();
      }
      const rsrc_t r1 = mk(a.w1 + task * 128, DM * FF * 2 - task * 128), r3 = mk(a.w3 + task * 128, DM * FF * 2 - task * 128);
      const int vo = r * 8 * (FF * 2) + c * 8;
      const int xo = L_XN + (c & 7) * XN_LD + r * 16;
      u32x2 ra[8], rc[8];
      f32x4 g[4], u[4];
#pragma unroll
      for (int e = 0; e < 4; ++e) { g[e] = f32x4{0.f, 0.f, 0.f, 0.f}; u[e] = g[e]; }
#pragma unroll
      for (int i = 0; i < 8; ++i) ra[i] = LD64(r1, vo, i * (FF * 2), NT);
#pragma unroll
      for (int i = 0; i < 8 - MLP_GAP; ++i) rc[i] = LD64(r3, vo, i * (FF * 2), NT);
#define PAIR(x, y, d, rsx, sx, rsy, sy) { USE(x[2 * d + 1]); \
        _Pragma("unroll") for (int e = 0; e < 4; ++e) f[e][d] = __builtin_amdgcn_perm(x[2 * d + 1][e >> 1], x[2 * d][e >> 1], (e & 1) ? 0x07060302u : 0x05040100u); \
        PIN(); \
        _Pragma("unroll") for (int j = 2 * d - MLP_GAP; j < 2 * d + 2 - MLP_GAP; ++j) { \
          if (j < 0) y[8 + j] = LD64(rsy, vo, (sy + 8 + j) * (FF * 2), NT); else x[j] = LD64(rsx, vo, (sx + j) * (FF * 2), NT); } \
        PIN(); }
#pragma unroll 1
      for (int s = 0; s < 18; ++s) {
        const int sc_ = s * 32, sn = (s < 17 ? s + 1 : 17) * 32;
        const u32 nb = s < 17 ? DM * FF * 2 - task * 128 : 0;
        const rsrc_t n1 = mk(a.w1 + task * 128, nb), n3 = mk(a.w3 + task * 128, nb);
        const u32x4 xb = *(LAS u32x4*)(lds + xo + s * 64);
        u32x4 f[4];
        PAIR(ra, rc, 0, n1, sn, r3, sc_) PAIR(ra, rc, 1, n1, sn, r3, sc_) PAIR(ra, rc, 2, n1, sn, r3, sc_) PAIR(ra, rc, 3, n1, sn, r3, sc_)
#pragma unroll
        for (int e = 0; e < 4; ++e) g[e] = mfma(f[e], xb, g[e]);
        PIN();
        PAIR(rc, ra, 0, n3, sn, n1, sn) PAIR(rc, ra, 1, n3, sn, n1, sn) PAIR(rc, ra, 2, n3, sn, n1, sn) PAIR(rc, ra, 3, n3, sn, n1, sn)
#pragma unroll
        for (int e = 0; e < 4; ++e) u[e] = mfma(f[e], xb, u[e]);
        PIN();
      }
      float hv[4][4];
#pragma unroll
      for (int p = 0; p < 4; ++p)
#pragma unroll
        for (int e = 0; e < 4; ++e) hv[p][e] = rb(silu(rb(g[e][p]))) * rb(u[e][p]);
      const u32x4 h0 = {pk(hv[0][0], hv[0][1]), pk(hv[0][2], hv[0][3]), pk(hv[1][0], hv[1][1]), pk(hv[1][2], hv[1][3])};
      const u32x4 h1 = {pk(hv[2][0], hv[2][1]), pk(hv[2][2], hv[2][3]), pk(hv[3][0], hv[3][1]), pk(hv[3][2], hv[3][3])};
      ST128((c & 8) ? h1 : h0, rws, WS_HID + task * HID_LD + (c & 7) * 128 + r * 32 + (c >> 3) * 16, 0, SC1NT);
      DRAIN();
      while (!LGET(L_TK)) __builtin_amdgcn_s_sleep(2);
      asm volatile("" ::: "memory");
      if (lane == 0) ST64(*(LAS u32x2*)(lds + L_TG), rws, WS_HID + task * HID_LD + NB * 128, 0, SC1);
    }
    PIN();
    softmax();
    if (job == 1) {
      const int ks = task / 9, cg = task % 9;
      const rsrc_t r2 = mk(a.w2 + (size_t)ks * (576 * DM * 2) + cg * 128, 576 * DM * 2 - cg * 128);
      const int vo = r * 8 * (DM * 2) + c * 8;
      u32x2 ra[8], rc[8];
#pragma unroll
      for (int i = 0; i < 8; ++i) ra[i] = LD64(r2, vo, i * (DM * 2), NT);
      const u32x2 tg = *(LAS u32x2*)(lds + L_TG);
      bool ok = true;
      const u64 start = now();
      for (;;) {
        const u32x2 f = LD64(rws, WS_HID + NB * 128 + (ks * 9 + min(lane, 8)) * HID_LD, 0, SC1);
        if (__all(f[0] == tg[0] && f[1] == tg[1])) break;
        if (now() - start > SPIN_CAP) { ok = false; break; }
        __builtin_amdgcn_s_sleep(16);
      }
      asm volatile("" ::: "memory");
      {
        u32x4 t[9];
#pragma unroll
        for (int i = 0; i < 9; ++i) t[i] = LD128(rws, WS_HID + ks * 9 * HID_LD + lane * 16, i * HID_LD, SC1);
#pragma unroll
        for (int i = 0; i < 9; ++i) *(LAS u32x4*)(lds + L_HB + i * 1024 + lane * 16) = t[i];
      }
      LWAIT();
      const int ho = L_HB + (c & 7) * 128 + r * 16;
      f32x4 d[4];
#pragma unroll
      for (int e = 0; e < 4; ++e) d[e] = f32x4{0.f, 0.f, 0.f, 0.f};
#pragma unroll 1
      for (int s = 0; s < 9; ++s) {
        const rsrc_t n2 = mk(a.w2 + (size_t)ks * (576 * DM * 2) + cg * 128, s < 8 ? 576 * DM * 2 - cg * 128 : 0);
        const int sn = (s < 8 ? s + 1 : 8) * 64;
        const u32x4 hb = *(LAS u32x4*)(lds + ho + s * 1024), hc = *(LAS u32x4*)(lds + ho + s * 1024 + 64);
        u32x4 f[4];
#pragma unroll
        for (int i = 0; i < 8; ++i) { USE(ra[i]); rc[i] = LD64(r2, vo, (s * 64 + 32 + i) * (DM * 2), NT); PIN(); }
        tr8(ra, f);
#pragma unroll
        for (int e = 0; e < 4; ++e) d[e] = mfma(f[e], hb, d[e]);
        PIN();
#pragma unroll
        for (int i = 0; i < 8; ++i) { USE(rc[i]); ra[i] = LD64(n2, vo, (sn + i) * (DM * 2), NT); PIN(); }
        tr8(rc, f);
#pragma unroll
        for (int e = 0; e < 4; ++e) d[e] = mfma(f[e], hc, d[e]);
        PIN();
      }
#pragma unroll
      for (int p = 0; p < 2; ++p) {
        u32x4 o;
#pragma unroll
        for (int e = 0; e < 4; ++e) o[e] = __float_as_uint((c & 8) ? d[e][2 + p] : d[e][p]);
        *(LAS u32x4*)(lds + L_MO + (c & 7) * 256 + r * 64 + (c >> 3) * 32 + p * 16) = o;
      }
      LWAIT();
#pragma unroll
      for (int i = 0; i < 2; ++i) ST128(*(LAS u32x4*)(lds + L_MO + lane * 16 + i * 1024), rws, WS_MLP + task * MLP_LD + lane * 16, i * 1024, SC1NT);
      DRAIN();
      if (lane == 0 && ok) ST64(tg, rws, WS_MLP + task * MLP_LD + NB * 256, 0, SC1);
    }
    BAR();
    return;
  }
  PIN();
  softmax();
  if (h < NCONS) {
    bool ok = true;
    const u32x2 tg = *(LAS u32x2*)(lds + L_TG);
    const u32 t0 = tg[0], t1 = tg[1];
    const int col = h * 32 + (lane & 7) * 4;
    wait_batch(PRE_AT);
    int oo = (b * DM + col + r) * 2;
    const u32 xv = __builtin_amdgcn_raw_buffer_load_b16(mk(a.x, NB * DM * 2), oo, 0, NT);
    const u64 start = now();
    for (;;) {
      const u32x2 f1 = LD64(rws, WS_ATT + DM * 4 + (b * NH + (lane & 31)) * ATT_LD, 0, SC1);
      const u32x2 f2 = LD64(rws, WS_MLP + NB * 256 + ((lane & 7) * 9 + (h >> 1)) * MLP_LD, 0, SC1);
      bool all = f1[0] == t0 && f1[1] == t1 && f2[0] == t0 && f2[1] == t1;
      if (blk == 0) {
#pragma unroll
        for (int i = 0; i < 4; ++i) { const u32x2 f = LD64(rws, WS_ATT + DM * 4 + lane * ATT_LD, i * 64 * ATT_LD, SC1); all &= f[0] == t0 && f[1] == t1; }
      }
      if (__all(all)) break;
      if (now() - start > SPIN_CAP) { ok = false; break; }
      __builtin_amdgcn_s_sleep(16);
    }
    asm volatile("" ::: "memory");
    if (blk == 0 && lane == 0) ST64((u32x2{t0, t1}), rws, WS_EPOCH, 0, SC1);
    const u32x4 f = LD128(rws, WS_MLP + ((lane >> 3) * 9 + (h >> 1)) * MLP_LD + b * 256 + (h & 1) * 128 + (lane & 7) * 16, 0, SC1);
    float g = rb(silu(rb(sum8x4(__uint_as_float(f[0]), __uint_as_float(f[1]), __uint_as_float(f[2]), __uint_as_float(f[3]))))), xe = lo16(xv);
    int ao = WS_ATT + (b * NH + (lane >> 3)) * ATT_LD + col * 4;
    oo = c < 8 ? oo : NOWHERE;
    asm volatile("" : "+v"(g), "+v"(xe), "+v"(ao), "+v"(oo));
    const u32x4 dws = {(u32)(size_t)a.ws, (u32)((size_t)a.ws >> 32) & 0xffffu, WS_END, 0x00020000u};
    const u32x4 dout = {(u32)(size_t)a.out, (u32)((size_t)a.out >> 32) & 0xffffu, NB * DM * 2, 0x00020000u};
    BAR();
    if (ok) {
#define SWEEP(a0, a1, a2, a3) "buffer_load_dwordx4 " a0 ", %[ao], %[ws], 0 offen sc1\n\tbuffer_load_dwordx4 " a1 ", %[ao], %[ws], %[o1] offen sc1\n\t" \
                              "buffer_load_dwordx4 " a2 ", %[ao], %[ws], %[o2] offen sc1\n\tbuffer_load_dwordx4 " a3 ", %[ao], %[ws], %[o3] offen sc1\n\t"
      const u32 start2 = (u32)now();
      asm volatile(
        "s_waitcnt vmcnt(0)\n\t"
        SWEEP("v[64:67]", "v[68:71]", "v[72:75]", "v[76:79]")
        "\nLsw%=:\n\t"
        SWEEP("v[80:83]", "v[84:87]", "v[88:91]", "v[92:95]")
        "s_waitcnt vmcnt(4)\n\t"
        "v_max3_u32 v96, v64, v65, v66\n\tv_max3_u32 v97, v67, v68, v69\n\tv_max3_u32 v98, v70, v71, v72\n\tv_max3_u32 v99, v73, v74, v75\n\t"
        "v_max3_u32 v96, v96, v76, v77\n\tv_max3_u32 v97, v97, v78, v79\n\tv_max3_u32 v96, v96, v97, v98\n\tv_max_u32 v96, v96, v99\n\t"
        "v_cmp_eq_u32 vcc, -1, v96\n\ts_nop 1\n\ts_cbranch_vccz LswA%=\n\t"
        SWEEP("v[64:67]", "v[68:71]", "v[72:75]", "v[76:79]")
        "s_waitcnt vmcnt(4)\n\t"
        "v_max3_u32 v96, v80, v81, v82\n\tv_max3_u32 v97, v83, v84, v85\n\tv_max3_u32 v98, v86, v87, v88\n\tv_max3_u32 v99, v89, v90, v91\n\t"
        "v_max3_u32 v96, v96, v92, v93\n\tv_max3_u32 v97, v97, v94, v95\n\tv_max3_u32 v96, v96, v97, v98\n\tv_max_u32 v96, v96, v99\n\t"
        "v_cmp_eq_u32 vcc, -1, v96\n\ts_nop 1\n\ts_cbranch_vccz LswB%=\n\t"
        "s_memrealtime vcc\n\ts_waitcnt lgkmcnt(0)\n\ts_sub_u32 vcc_lo, vcc_lo, %[st]\n\ts_cmp_lt_u32 vcc_lo, %[cap]\n\ts_cbranch_scc1 Lsw%=\n\t"
        "v_mov_b32 v96, 0x7fc0\n\ts_branch Lsws%=\n"
        "LswB%=:\n\t"
        "v_add_f32 v96, v80, v84\n\tv_add_f32 v88, v88, v92\n\tv_add_f32 v97, v81, v85\n\tv_add_f32 v89, v89, v93\n\t"
        "v_add_f32 v98, v82, v86\n\tv_add_f32 v90, v90, v94\n\tv_add_f32 v99, v83, v87\n\tv_add_f32 v91, v91, v95\n\t"
        "v_add_f32 v96, v96, v88\n\tv_add_f32 v97, v97, v89\n\tv_add_f32 v98, v98, v90\n\tv_add_f32 v99, v99, v91\n\t"
        "s_branch Lswf%=\n"
        "LswA%=:\n\t"
        "v_add_f32 v96, v64, v68\n\tv_add_f32 v72, v72, v76\n\tv_add_f32 v97, v65, v69\n\tv_add_f32 v73, v73, v77\n\t"
        "v_add_f32 v98, v66, v70\n\tv_add_f32 v74, v74, v78\n\tv_add_f32 v99, v67, v71\n\tv_add_f32 v75, v75, v79\n\t"
        "v_add_f32 v96, v96, v72\n\tv_add_f32 v97, v97, v73\n\tv_add_f32 v98, v98, v74\n\tv_add_f32 v99, v99, v75\n"
        "Lswf%=:\n\ts_nop 1\n\t"
        "v_permlane32_swap_b32 v96, v98\n\tv_permlane32_swap_b32 v97, v99\n\ts_nop 1\n\tv_add_f32 v96, v96, v98\n\tv_add_f32 v97, v97, v99\n\ts_nop 1\n\t"
        "v_permlane16_swap_b32 v96, v97\n\ts_nop 1\n\tv_add_f32 v96, v96, v97\n\ts_nop 1\n\t"
        "v_add_f32_dpp v96, v96, v96 row_ror:8 row_mask:0xf bank_mask:0xf\n\t"
        "v_cvt_pk_bf16_f32 v96, v96, v96\n\tv_lshlrev_b32 v96, 16, v96\n\tv_mul_f32 v96, v96, %[g]\n\t"
        "v_cvt_pk_bf16_f32 v96, v96, v96\n\tv_lshlrev_b32 v96, 16, v96\n\tv_add_f32 v96, %[x], v96\n\tv_cvt_pk_bf16_f32 v96, v96, v96\n"
        "Lsws%=:\n\tbuffer_store_short v96, %[oo], %[out], 0 offen\n\ts_waitcnt vmcnt(0)"
        :
        : [ao] "v"(ao), [ws] "s"(dws), [o1] "s"(8 * ATT_LD), [o2] "s"(16 * ATT_LD), [o3] "s"(24 * ATT_LD), [st] "s"(start2), [cap] "s"((u32)SPIN_CAP),
          [g] "v"(g), [x] "v"(xe), [oo] "v"(oo), [out] "s"(dout)
        : "memory", "vcc", "scc", "v64", "v65", "v66", "v67", "v68", "v69", "v70", "v71", "v72", "v73", "v74", "v75", "v76", "v77", "v78", "v79", "v80", "v81",
          "v82", "v83", "v84", "v85", "v86", "v87", "v88", "v89", "v90", "v91", "v92", "v93", "v94", "v95", "v96", "v97", "v98", "v99");
    } else {
      __builtin_amdgcn_raw_buffer_store_b16((u16)0x7fc0, mk(a.out, NB * DM * 2), oo, 0, 0);
    }
  } else {
    BAR();
  }
}

}

extern "C" void kernel_launch(void* const* d_in, const int* in_sizes, int n_in, void* d_out, int out_size, void* d_ws, size_t ws_size,
                              hipStream_t stream) {
  Args a;
  a.x = (const char*)d_in[0]; a.k = (const char*)d_in[1]; a.v = (const char*)d_in[2]; a.wq = (const char*)d_in[3]; a.wo = (const char*)d_in[4];
  a.w1 = (const char*)d_in[6]; a.w3 = (const char*)d_in[7]; a.w2 = (const char*)d_in[8];
  a.out = (char*)d_out; a.ws = (char*)d_ws;
  hipLaunchKernelGGL(fused_decode, dim3(NB * NH), dim3(1024), 0, stream, a);
}
```

```cpp
#include <hip/hip_runtime.h>
#include <hip/hip_bf16.h>
#include <cstdint>

namespace {

typedef unsigned u32;
typedef unsigned long long u64;
typedef unsigned short u16;
typedef u32 u32x4 __attribute__((ext_vector_type(4)));
typedef u32 u32x2 __attribute__((ext_vector_type(2)));
typedef float f32x4 __attribute__((ext_vector_type(4)));
typedef __bf16 bf16x8 __attribute__((ext_vector_type(8)));
typedef __bf16 bf16x2 __attribute__((ext_vector_type(2)));
typedef __bf16 bf16x16 __attribute__((ext_vector_type(16)));
typedef u32 u32x8 __attribute__((ext_vector_type(8)));
#define LAS __attribute__((address_space(3)))

constexpr int NB = 8, DM = 576, NH = 32, SK = 4096, DH = 128, FF = 4608;
constexpr int NTASK = FF / 64;
constexpr int NCONS = DM / 32;
constexpr int KEEP = 0, NT = 2, SC1 = 16, SC1NT = 18;

constexpr int WIN0 = 41, WIN_MLP = 2;
constexpr int WMID = 45, WLO = 36, WHI = 56, GAIN = 20;
constexpr int OPENW = 10;
constexpr u32 PACE_K = 0x19110903u, PACE_V = 0x39332b23u;
constexpr int PACE_DLY = 3;
constexpr int PRE_AT = 58;
constexpr int MLP_GAP = 6;
constexpr u64 SPIN_CAP = 5000000;

constexpr u32 ATT_LD = DM * 4 + 128, HID_LD = NB * 128 + 128, MLP_LD = NB * 256 + 128, PACE_LD = 4352;
constexpr u32 WS_EPOCH = 0, WS_UNUSED = 2048, WS_ATT = 4096, WS_HID = WS_ATT + NB * NH * ATT_LD, WS_MLP = WS_HID + NTASK * HID_LD, WS_PACE = WS_MLP + NTASK * MLP_LD,
              WS_END = WS_PACE + 8 * 64 * PACE_LD;
constexpr u32 EMPTY = 0xffffffffu;
constexpr int NOWHERE = 0x40000000;

constexpr int XN_LD = 1168;
constexpr int L_XN = 0, L_QP = L_XN + NB * XN_LD, L_QB = L_QP + 9 * DH * 4, L_PS = L_QB + DH * 2, L_WM = L_PS + SK * 2, L_WS = L_WM + 64,
              L_OP = L_WS + 64, L_OB = L_OP + 4 * DH * 4, L_TG = L_OB + 9 * DH * 2, L_PG = L_TG + 8, L_TK = L_TG + 12, L_WN = L_TG + 16,
              L_XK = L_TG + 24, L_QC = L_TG + 28, L_QK = L_TG + 32, L_XB = L_TG + 48, L_MO = L_XB + DM * 2, L_HB = L_MO + NB * 256,
              L_END = L_HB + 9 * 1024;

struct Args { const char* x; const char* k; const char* v; const char* wq; const char* wo; const char* w1; const char* w3; const char* w2; char* out; char* ws; };

typedef __amdgpu_buffer_rsrc_t rsrc_t;
__device__ __forceinline__ rsrc_t mk(const char* p, u32 n) { return __builtin_amdgcn_make_buffer_rsrc((void*)p, 0, n, 0x00020000); }
#define LD128(r, vo, so, aux) __builtin_amdgcn_raw_buffer_load_b128(r, vo, so, aux)
#define LD64(r, vo, so, aux) __builtin_amdgcn_raw_buffer_load_b64(r, vo, so, aux)
#define LD32(r, vo, so, aux) __builtin_amdgcn_raw_buffer_load_b32(r, vo, so, aux)
#define ST128(v, r, vo, so, aux) __builtin_amdgcn_raw_buffer_store_b128(v, r, vo, so, aux)
#define ST64(v, r, vo, so, aux) __builtin_amdgcn_raw_buffer_store_b64(v, r, vo, so, aux)
#define PIN() __builtin_amdgcn_sched_barrier(0)
#define USE(x) asm volatile("" ::"v"(x) : "memory")
#define BAR() asm volatile("s_waitcnt lgkmcnt(0)\n\ts_barrier" ::: "memory")
#define RAWBAR() asm volatile("s_barrier" ::: "memory")
#define LWAIT() asm volatile("s_waitcnt lgkmcnt(0)" ::: "memory")
#define DRAIN() asm volatile("s_waitcnt vmcnt(0)" ::: "memory")
#define TOUCH(d, p) asm volatile("s_load_dword %0, %1, 0x0" : "=&s"(d) : "s"(p) : "memory")
#define LGET(off) __hip_atomic_load((LAS u32*)(lds + (off)), __ATOMIC_RELAXED, __HIP_MEMORY_SCOPE_WORKGROUP)
#define LPUT(off, v) __hip_atomic_store((LAS u32*)(lds + (off)), (u32)(v), __ATOMIC_RELAXED, __HIP_MEMORY_SCOPE_WORKGROUP)
#define LADD(off) __hip_atomic_fetch_add((LAS u32*)(lds + (off)), 1u, __ATOMIC_RELAXED, __HIP_MEMORY_SCOPE_WORKGROUP)
#define LSPIN(off, n) do { while (LGET(off) < (n)) __builtin_amdgcn_s_sleep(1); asm volatile("" ::: "memory"); } while (0)
#define DITH(i) (((i) & 1) * 4 + ((i) & 2) + ((i) >> 2))
#define GATE(i) asm volatile("s_cmp_gt_i32 %0, %1\n\ts_cbranch_scc1 Lg7_%=\n\ts_cmp_gt_i32 %0, %2\n\ts_cbranch_scc1 Lg5_%=\n\t" \
                             "s_cmp_gt_i32 %0, %3\n\ts_cbranch_scc1 Lg4_%=\n\ts_waitcnt vmcnt(3)\nLg4_%=:\n\ts_waitcnt vmcnt(4)\n" \
                             "Lg5_%=:\n\ts_waitcnt vmcnt(5)\nLg7_%=:" \
                             ::"s"(win), "i"(48 + DITH(i)), "i"(40 + DITH(i)), "i"(32 + DITH(i)) : "scc", "memory")
#define EIGHT(STEP) STEP(0) STEP(1) STEP(2) STEP(3) STEP(4) STEP(5) STEP(6) STEP(7)

__device__ __forceinline__ u32 pk(float lo, float hi) { bf16x2 t = {(__bf16)lo, (__bf16)hi}; return __builtin_bit_cast(u32, t); }
__device__ __forceinline__ float rb(float x) { return __uint_as_float(pk(x, x) << 16); }
__device__ __forceinline__ float lo16(u32 v) { return __uint_as_float(v << 16); }
__device__ __forceinline__ float hi16(u32 v) { return __uint_as_float(v & 0xffff0000u); }
__device__ __forceinline__ float dot2(u32 a, u32 b, float c) {
  return __builtin_amdgcn_fdot2_f32_bf16(__builtin_bit_cast(bf16x2, a), __builtin_bit_cast(bf16x2, b), c, false);
}
__device__ __forceinline__ f32x4 mfma(u32x4 a, u32x4 b, f32x4 c) {
  return __builtin_amdgcn_mfma_f32_16x16x32_bf16(__builtin_bit_cast(bf16x8, a), __builtin_bit_cast(bf16x8, b), c, 0, 0, 0);
}
template <int CTRL> __device__ __forceinline__ float dpp(float x) {
  return __builtin_bit_cast(float, __builtin_amdgcn_mov_dpp(__builtin_bit_cast(int, x), CTRL, 0xf, 0xf, true));
}
constexpr int X1 = 0xB1, X2 = 0x4E, HMIR = 0x141, MIR = 0x140, ROR8 = 0x128;
__device__ __forceinline__ float sum16(float x) { x += dpp<X1>(x); x += dpp<X2>(x); x += dpp<HMIR>(x); x += dpp<MIR>(x); return x; }
__device__ __forceinline__ float max16(float x) { x = fmaxf(x, dpp<X1>(x)); x = fmaxf(x, dpp<X2>(x)); x = fmaxf(x, dpp<HMIR>(x)); x = fmaxf(x, dpp<MIR>(x)); return x; }
__device__ __forceinline__ float sumrows(float x) {
  auto s = __builtin_amdgcn_permlane16_swap(__float_as_uint(x), __float_as_uint(x), false, false);
  x = __uint_as_float(s[0]) + __uint_as_float(s[1]);
  auto t = __builtin_amdgcn_permlane32_swap(__float_as_uint(x), __float_as_uint(x), false, false);
  return __uint_as_float(t[0]) + __uint_as_float(t[1]);
}
__device__ __forceinline__ float maxrows(float x) {
  auto s = __builtin_amdgcn_permlane16_swap(__float_as_uint(x), __float_as_uint(x), false, false);
  x = fmaxf(__uint_as_float(s[0]), __uint_as_float(s[1]));
  auto t = __builtin_amdgcn_permlane32_swap(__float_as_uint(x), __float_as_uint(x), false, false);
  return fmaxf(__uint_as_float(t[0]), __uint_as_float(t[1]));
}
__device__ __forceinline__ float sum8x4(float v0, float v1, float v2, float v3) {
  auto p = __builtin_amdgcn_permlane32_swap(__float_as_uint(v0), __float_as_uint(v2), false, false);
  auto q = __builtin_amdgcn_permlane32_swap(__float_as_uint(v1), __float_as_uint(v3), false, false);
  auto t = __builtin_amdgcn_permlane16_swap(__float_as_uint(__uint_as_float(p[0]) + __uint_as_float(p[1])), __float_as_uint(__uint_as_float(q[0]) + __uint_as_float(q[1])), false, false);
  const float x = __uint_as_float(t[0]) + __uint_as_float(t[1]);
  return x + dpp<ROR8>(x);
}
__device__ __forceinline__ float fdiv(float a, float d, float rd) { const float q = a * rd; return fmaf(fmaf(-q, d, a), rd, q); }
__device__ __forceinline__ float silu(float x) { const float d = 1.f + __expf(-x); return d < INFINITY ? fdiv(x, d, __builtin_amdgcn_rcpf(d)) : x * 0.f; }
__device__ __forceinline__ u64 now() { return __builtin_amdgcn_s_memrealtime(); }

struct Diag { u32 s[2][4]; };
__device__ __forceinline__ Diag make_diag(int c) {
  Diag d;
#pragma unroll
  for (int par = 0; par < 2; ++par)
#pragma unroll
    for (int w = 0; w < 4; ++w) {
      const u32 b = par ? 0x0302u : 0x0100u;
      d.s[par][w] = (c >> 1) == w ? ((c & 1) ? (b << 16) | 0x0c0cu : 0x0c0c0000u | b) : 0x0c0c0c0cu;
    }
  return d;
}
template <int PAR> __device__ __forceinline__ f32x4 axpy4(u32 val, u32x4 rows, const Diag& d, f32x4 acc) {
  u32x4 af;
#pragma unroll
  for (int w = 0; w < 4; ++w) af[w] = __builtin_amdgcn_perm(val, val, d.s[PAR][w]);
  return mfma(af, rows, acc);
}
__device__ __forceinline__ float score4(u32x4 k, u32x4 q) {
  float d = dot2(k[0], q[0], 0.f); d = dot2(k[1], q[1], d); d = dot2(k[2], q[2], d); d = dot2(k[3], q[3], d);
  return sum16(d);
}
__device__ __forceinline__ void tr8(const u32x2 (&raw)[8], u32x4 (&f)[4]) {
#pragma unroll
  for (int e = 0; e < 4; ++e)
#pragma unroll
    for (int d = 0; d < 4; ++d) f[e][d] = __builtin_amdgcn_perm(raw[2 * d + 1][e >> 1], raw[2 * d][e >> 1], (e & 1) ? 0x07060302u : 0x05040100u);
}

__global__ void __launch_bounds__(1024, 4) fused_decode(Args a) {
  __shared__ __attribute__((aligned(16))) char lds_[L_END];
  LAS char* lds = (LAS char*)lds_;
  if (threadIdx.x == 640) { LPUT(L_PG, 0); LPUT(L_TK, 0); LPUT(L_WN, 0); LPUT(L_WN + 4, 0); LPUT(L_XK, 0); LPUT(L_QC, 0); LPUT(L_QK, 0); LWAIT(); }
  PIN(); RAWBAR(); PIN();
  asm volatile("" ::"s"(a.x), "s"(a.ws));
  const int tid = threadIdx.x, lane = tid & 63, w = __builtin_amdgcn_readfirstlane(tid >> 6), r = lane >> 4, c = lane & 15;
  const int blk = blockIdx.x, b = blk >> 5, h = blk & 31;
  const int hrot = (h - b) & 31, job = hrot / 9, task = (h & 7) * 9 + (hrot % 9 < 8 ? b : 8);
  const char* kbase = a.k + ((size_t)blk << 20);
  const char* vbase = a.v + ((size_t)blk << 20);
  const rsrc_t rk = mk(kbase, 1u << 20);
  const int sw = w & 3;
  const int vA = lane * 16 + sw * 4096;
  auto kvbase = [](int beta) { return (beta >> 1) * 65536 + (beta & 1) * 16384; };
#define KVLD(rs, base, i) LD128(rs, vA + ((i) & 1) * 32768 + (((i) >> 1) ^ sw) * 1024, base, NT)
  float mx = -INFINITY;
  auto put_scores = [&](float sc, int batch) {
    const float s = rb(rb(sc) * 0.08838834764831845f);
    mx = fmaxf(mx, s);
    if (c < 8) *(LAS u16*)(lds + L_PS + batch * 64 + r * 16 + c * 2) = (u16)(__float_as_uint(s) >> 16);
  };
  auto softmax = [&]() {
    const float wm = maxrows(max16(mx));
    if (lane == 0) *(LAS float*)(lds + L_WM + w * 4) = wm;
    BAR();
    const float M = max16(*(LAS float*)(lds + L_WM + c * 4));
    const u32x2 sv = *(LAS u32x2*)(lds + L_PS + tid * 8);
    const float e0 = __expf(lo16(sv[0]) - M), e1 = __expf(hi16(sv[0]) - M), e2 = __expf(lo16(sv[1]) - M), e3 = __expf(hi16(sv[1]) - M);
    const float ws = sumrows(sum16((e0 + e1) + (e2 + e3)));
    if (lane == 0) *(LAS float*)(lds + L_WS + w * 4) = ws;
    BAR();
    const float inv = 1.f / sum16(*(LAS float*)(lds + L_WS + c * 4));
    *(LAS u32x2*)(lds + L_PS + tid * 8) = u32x2{pk(e0 * inv, e1 * inv), pk(e2 * inv, e3 * inv)};
    BAR();
  };

  if (w >= 12) {
    u32x4 ring[8];
    u32x4 ext[8];
    __builtin_amdgcn_s_sleep(32);
#pragma unroll
    for (int i = 0; i < 16; ++i) {
      if (i >= OPENW) USE((i - OPENW < 8 ? ring : ext)[(i - OPENW) & 7]);
      if (i < 8) ring[i] = KVLD(rk, kvbase(0), i); else ext[i - 8] = KVLD(rk, kvbase(1), i - 8);
      PIN();
    }
    __builtin_amdgcn_s_setprio(3);
    LSPIN(L_QK, 2);
    const u32x4 q = *(LAS u32x4*)(lds + L_QB + c * 16);
    float sc = 0.f;
    const int win0 = WIN0 + (job == 0 ? WIN_MLP : 0);
    int win = win0;
#define PACE_IN() const u32 tj = (u32)now(); const u64 sch = __hip_atomic_load((LAS u64*)(lds + L_WN), __ATOMIC_RELAXED, __HIP_MEMORY_SCOPE_WORKGROUP)
#define PACE_OUT(j) do { const u32 due_ = __builtin_amdgcn_readfirstlane((u32)sch), tau_ = __builtin_amdgcn_readfirstlane((u32)(sch >> 32)); \
      const int late_ = min(max((int)(tj * 256u - due_ - tau_ * (u32)(j)) >> 8, -4096), 4096); \
      win = tau_ ? min(max(WMID + ((late_ * GAIN) >> 8), WLO), WHI) : win0; } while (0)
#pragma unroll
    for (int i = 0; i < 8; ++i) { const float d = score4(ring[i], q); sc = (c & 7) == i ? d : sc; PIN(); }
    put_scores(sc, sw);
#define FSTEP(i) { if (i > 0) { GATE(i); ring[(i + 7) & 7] = KVLD(rk, kvbase(2), (i + 7) & 7); } PIN(); const float d = score4(ext[i], q); sc = (c & 7) == i ? d : sc; PIN(); }
    EIGHT(FSTEP)
    put_scores(sc, 4 + sw);
#pragma unroll 1
    for (int beta = 2; beta < 32; ++beta) {
      const rsrc_t rn = mk(beta < 31 ? kbase : vbase, 1u << 20);
      const int sc0 = __builtin_amdgcn_readfirstlane(kvbase(beta)), sn0 = __builtin_amdgcn_readfirstlane(kvbase(beta < 31 ? beta + 1 : 0));
      if (beta == 31) win = 64;
      PACE_IN();
      if (tid == 768) LPUT(L_PG, beta);
#define KSTEP(i) { GATE(i); if (i == 0) ring[7] = KVLD(rk, sc0, 7); else ring[(i + 7) & 7] = KVLD(rn, sn0, (i + 7) & 7); PIN(); \
        const float d = score4(ring[i], q); sc = (c & 7) == i ? d : sc; PIN(); }
      EIGHT(KSTEP)
      put_scores(sc, beta * 4 + sw);
      PACE_OUT(beta);
    }
    const rsrc_t rv = mk(vbase, 1u << 20);
    ring[7] = KVLD(rv, kvbase(0), 7);
#pragma unroll
    for (int i = 0; i < 8; ++i) { ext[i] = KVLD(rv, kvbase(1), i); PIN(); }
    softmax();
    const Diag dg = make_diag(c);
    f32x4 acc = {0.f, 0.f, 0.f, 0.f};
    const int po = L_PS + sw * 64 + r * 16;
    u32x4 pn = *(LAS u32x4*)(lds + po + 512);
    {
      const u32x4 p0 = *(LAS u32x4*)(lds + po), p1 = *(LAS u32x4*)(lds + po + 256);
#pragma unroll
      for (int i = 0; i < 8; ++i) { acc = (i & 1) ? axpy4<1>(p0[i >> 1], ring[i], dg, acc) : axpy4<0>(p0[i >> 1], ring[i], dg, acc); PIN(); }
#define ESTEP(i) { if (i > 0) { GATE(i); ring[(i + 7) & 7] = KVLD(rv, kvbase(2), (i + 7) & 7); } PIN(); acc = axpy4<i & 1>(p1[i >> 1], ext[i], dg, acc); PIN(); }
      EIGHT(ESTEP)
    }
#pragma unroll 1
    for (int beta = 2; beta < 32; ++beta) {
      const rsrc_t rn = mk(vbase, beta < 31 ? 1u << 20 : 0u);
      const int bn = beta < 31 ? beta + 1 : 0;
      const int sc0 = __builtin_amdgcn_readfirstlane(kvbase(beta)), sn0 = __builtin_amdgcn_readfirstlane(kvbase(bn));
      if (beta == 31) win = 64;
      const u32x4 pv = pn;
      pn = *(LAS u32x4*)(lds + po + bn * 256);
      PACE_IN();
      if (tid == 768) LPUT(L_PG, 32 + beta);
#define VSTEP(i) { GATE(i); if (i == 0) ring[7] = KVLD(rv, sc0, 7); else ring[(i + 7) & 7] = KVLD(rn, sn0, (i + 7) & 7); PIN(); \
        acc = axpy4<i & 1>(pv[i >> 1], ring[i], dg, acc); PIN(); }
      EIGHT(VSTEP)
      PACE_OUT(32 + beta);
    }
    if (r < 2) *(LAS f32x4*)(lds + L_OP + sw * 512 + c * 32 + r * 16) = acc;
    BAR();
    return;
  }

  const rsrc_t rws = mk(a.ws, WS_END);
  auto wait_batch = [&](u32 n) { while (LGET(L_PG) < n) __builtin_amdgcn_s_sleep(2); };
  if (w == 11) {
    u32 t0, t1, t2, t3, t4, t5, t6, t7;
    asm volatile("" ::"s"(a.ws), "s"(a.out), "s"(kbase), "s"(vbase), "s"(a.w2));
    TOUCH(t0, kbase); TOUCH(t1, a.ws + WS_UNUSED);
    __builtin_amdgcn_s_sleep(32);
    TOUCH(t2, a.wo + (size_t)h * (DH * DM * 2)); TOUCH(t3, a.w1); TOUCH(t4, a.w3); TOUCH(t5, vbase); TOUCH(t6, a.w2);
    TOUCH(t7, h < NCONS ? a.out + (b * DM + h * 32) * 2 : a.x);
    const u32x2 ep = LD64(rws, WS_EPOCH, 0, SC1);
    if (lane < 48) {
#pragma unroll
      for (int i = 0; i < 3; ++i) ST128((u32x4{EMPTY, EMPTY, EMPTY, EMPTY}), rws, WS_ATT + blk * ATT_LD + lane * 16 + i * 768, 0, SC1);
    }
    asm volatile("s_waitcnt lgkmcnt(0)" : "+s"(t0), "+s"(t1), "+s"(t2), "+s"(t3), "+s"(t4), "+s"(t5), "+s"(t6), "+s"(t7)::"memory");
    const u64 tag = (((u64)(u32)__builtin_amdgcn_readfirstlane(ep[1]) << 32) | (u32)__builtin_amdgcn_readfirstlane(ep[0])) + 1;
    DRAIN();
    if (lane == 0) {
      *(LAS u64*)(lds + L_TG) = tag;
      LWAIT();
      LPUT(L_TK, 1);
      ST64((u32x2{(u32)tag, (u32)(tag >> 32)}), rws, WS_ATT + blk * ATT_LD + DM * 4, 0, SC1);
    }
    const u32 tb = (u32)now();
    const bool sample = ((blk >> 3) & 3) == (blk & 3);
    float mprev = 0.f;
    int atprev = 0;
    auto pace = [&](int k, int at) {
      wait_batch(at);
      const u32 tk = (u32)now();
      if (sample && lane < 8) ST128((u32x4{tk, (u32)tag, tk, (u32)tag}), rws, WS_PACE + (k * 64 + b * 8 + (blk & 7)) * PACE_LD + lane * 16, 0, SC1NT);
      wait_batch(at + PACE_DLY);
      const u32x2 v = LD64(rws, WS_PACE + (k * 64 + ((b + lane + blk) & 7) * 8 + (lane & 7)) * PACE_LD, 0, SC1);
      const bool in = v[1] == (u32)tag && lane < 8;
      const float n = (float)__popcll(__ballot(in));
      const float m = (float)(int)(tk - tb) + sum16(in ? (float)(int)(v[0] - tk) : 0.f) * __builtin_amdgcn_rcpf(fmaxf(n, 1.f));
      if (k > 0 && lane == 0) {
        const float tau = (m - mprev) * __builtin_amdgcn_rcpf((float)(at - atprev)) * 0.99f;
        const u32 tau256 = (u32)fminf(fmaxf(tau * 256.f, 1.f), 1e6f);
        __hip_atomic_store((LAS u64*)(lds + L_WN), ((u64)tau256 << 32) | (tb * 256u + (u32)(int)(m * 256.f) - tau256 * (u32)at), __ATOMIC_RELAXED, __HIP_MEMORY_SCOPE_WORKGROUP);
      }
      mprev = m; atprev = at;
    };
#pragma unroll 1
    for (int k = 0; k < 4; ++k) pace(k, (PACE_K >> (8 * k)) & 255);
    softmax();
#pragma unroll 1
    for (int k = 0; k < 4; ++k) pace(4 + k, (PACE_V >> (8 * k)) & 255);
    BAR();
    return;
  }
  if (w < 9) {
    __builtin_amdgcn_s_setprio(1);
    const int wr = (w + b) % 9;
    u32x4 wb[16];
    {
      const rsrc_t rq = mk(a.wq + h * 256, DM * 8192 - h * 256);
      const int vo = (64 * wr + 16 * r) * 8192 + c * 16;
#pragma unroll
      for (int i = 0; i < 16; ++i) wb[i] = LD128(rq, vo, i * 8192, KEEP);
    }
    PIN();
    const Diag dgq = make_diag(c);
    LSPIN(L_XK, 1);
    const u32x4 x0 = *(LAS u32x4*)(lds + L_XB + (64 * wr + 16 * r) * 2), x1 = *(LAS u32x4*)(lds + L_XB + (64 * wr + 16 * r) * 2 + 16);
    PIN();
    {
      f32x4 acc0 = {0.f, 0.f, 0.f, 0.f}, acc1 = acc0;
#pragma unroll
      for (int i = 0; i < 8; ++i) {
        const u32 xv = i < 4 ? x0[i & 3] : x1[i & 3];
        acc0 = axpy4<0>(xv, wb[2 * i], dgq, acc0); acc1 = axpy4<1>(xv, wb[2 * i + 1], dgq, acc1);
      }
      if (r < 2) *(LAS f32x4*)(lds + L_QP + wr * 512 + c * 32 + r * 16) = acc0 + acc1;
    }
    LWAIT();
    if (lane == 0) LADD(L_QC);
    if (w < 2) {
      LSPIN(L_QC, 9);
      float s = 0.f;
#pragma unroll
      for (int j = 0; j < 9; ++j) s += *(LAS float*)(lds + L_QP + j * 512 + tid * 4);
      *(LAS u16*)(lds + L_QB + tid * 2) = (u16)pk(s, s);
      LWAIT();
      if (lane == 0) LADD(L_QK);
    }
    __builtin_amdgcn_s_setprio(0);
    LSPIN(L_QK, 2);
  } else {
    if (w == 10) {
      __builtin_amdgcn_s_setprio(3);
      const rsrc_t rx = mk(a.x + b * (DM * 2), DM * 2);
      u32 xr[5];
#pragma unroll
      for (int i = 0; i < 5; ++i) xr[i] = LD32(rx, lane * 4 + i * 256, 0, KEEP);
      float s = 0.f;
#pragma unroll
      for (int i = 0; i < 5; ++i) s += lo16(xr[i]) + hi16(xr[i]);
      const float mean = sumrows(sum16(s)) * (1.f / DM);
      float ss = 0.f;
#pragma unroll
      for (int i = 0; i < 5; ++i) {
        const float d0 = lo16(xr[i]) - mean, d1 = hi16(xr[i]) - mean;
        ss += (i < 4 || lane < 32) ? d0 * d0 + d1 * d1 : 0.f;
      }
      const float var = sumrows(sum16(ss)) * (1.f / DM);
      const float m = rb(mean), den = rb(__builtin_amdgcn_sqrtf(rb(rb(var) + 1e-5f))), rd = __builtin_amdgcn_rcpf(den);
#pragma unroll
      for (int i = 0; i < 5; ++i)
        if (i < 4 || lane < 32) *(LAS u32*)(lds + L_XB + lane * 4 + i * 256) = pk(fdiv(rb(lo16(xr[i]) - m), den, rd), fdiv(rb(hi16(xr[i]) - m), den, rd));
      LWAIT();
      if (lane == 0) LPUT(L_XK, 1);
      __builtin_amdgcn_s_setprio(0);
    }
    LSPIN(L_QK, 2);
  }
  PIN();

  if (w < 9) {
    u32x4 wf[4][4];
    const rsrc_t ro = mk(a.wo + (size_t)h * (DH * DM * 2), DH * DM * 2);
    const int vo = r * 8 * (DM * 2) + w * 128 + c * 8;
    u32x2 raw[4][8];
#pragma unroll
    for (int n = 0; n < 32; ++n) {
      raw[n >> 3][n & 7] = LD64(ro, vo, ((n >> 3) * 32 + (n & 7)) * (DM * 2), KEEP);
      if (n >= 1) USE(raw[(n - 1) >> 3][(n - 1) & 7]);
      if (n >= 8 && (n & 7) == 0) tr8(raw[(n - 1) >> 3], wf[(n - 1) >> 3]);
      PIN();
    }
    tr8(raw[3], wf[3]);
    u32x8 wg[2][4];
    f32x4 d[4];
#pragma unroll
    for (int e = 0; e < 4; ++e) {
      wg[0][e] = __builtin_shufflevector(wf[0][e], wf[1][e], 0, 1, 2, 3, 4, 5, 6, 7);
      wg[1][e] = __builtin_shufflevector(wf[2][e], wf[3][e], 0, 1, 2, 3, 4, 5, 6, 7);
      d[e] = f32x4{0.f, 0.f, 0.f, 0.f};
      asm volatile("" : "+v"(wg[0][e]), "+v"(wg[1][e]), "+v"(d[e]));
    }
    int so = lane < 16 ? WS_ATT + blk * ATT_LD + w * 256 + c * 16 : NOWHERE, lo = L_OP + lane * 8, pick = (c & 1) ? 0xeeee : 0x4444;
    int lb = L_OB + w * 256 + (lane >> 3) * 32 + (lane & 1) * 16 + ((lane >> 1) & 3) * 4, lr = L_OB + w * 256 + r * 32 + (c & 1) * 16;
    asm volatile("" : "+v"(so), "+v"(lo), "+v"(lb), "+v"(lr), "+v"(pick));
    PIN();
    softmax();
    BAR();
    float o0, o1;
    {
      const u32x2 t0 = *(LAS u32x2*)(lds + lo), t1 = *(LAS u32x2*)(lds + lo + 512), t2 = *(LAS u32x2*)(lds + lo + 1024), t3 = *(LAS u32x2*)(lds + lo + 1536);
      o0 = (__uint_as_float(t0[0]) + __uint_as_float(t1[0])) + (__uint_as_float(t2[0]) + __uint_as_float(t3[0]));
      o1 = (__uint_as_float(t0[1]) + __uint_as_float(t1[1])) + (__uint_as_float(t2[1]) + __uint_as_float(t3[1]));
    }
    *(LAS u32*)(lds + lb) = pk(o0, o1);
    LWAIT();
    const u32x4 ob[2] = {*(LAS u32x4*)(lds + lr), *(LAS u32x4*)(lds + lr + 128)};
    PIN();
#pragma unroll
    for (int s = 0; s < 2; ++s)
#pragma unroll
      for (int e = 0; e < 4; ++e)
        d[e] = __builtin_amdgcn_smfmac_f32_16x16x64_bf16(__builtin_bit_cast(bf16x8, ob[s]), __builtin_bit_cast(bf16x16, wg[s][e]), d[e], pick, 0, 0);
    PIN();
    ST128((u32x4{min(__float_as_uint(d[0][0] + d[0][1]), EMPTY - 1), min(__float_as_uint(d[1][0] + d[1][1]), EMPTY - 1),
                 min(__float_as_uint(d[2][0] + d[2][1]), EMPTY - 1), min(__float_as_uint(d[3][0] + d[3][1]), EMPTY - 1)}), rws, so, 0, SC1);
    return;
  }
  if (w == 9) {
    if (job == 0) {
      {
        const rsrc_t rx = mk(a.x, NB * DM * 2);
        u32x4 xa[9];
#pragma unroll
        for (int i = 0; i < 9; ++i) xa[i] = LD128(rx, (lane >> 3) * (DM * 2) + (lane & 7) * 16, i * 128, NT);
        float s = 0.f;
#pragma unroll
        for (int i = 0; i < 9; ++i)
#pragma unroll
          for (int j = 0; j < 4; ++j) s += lo16(xa[i][j]) + hi16(xa[i][j]);
        s += dpp<X1>(s); s += dpp<X2>(s); s += dpp<HMIR>(s);
        const float mean = s * (1.f / DM);
        float ss = 0.f;
#pragma unroll
        for (int i = 0; i < 9; ++i)
#pragma unroll
          for (int j = 0; j < 4; ++j) { const float d0 = lo16(xa[i][j]) - mean, d1 = hi16(xa[i][j]) - mean; ss += d0 * d0 + d1 * d1; }
        ss += dpp<X1>(ss); ss += dpp<X2>(ss); ss += dpp<HMIR>(ss);
        const float m = rb(mean), den = rb(__builtin_amdgcn_sqrtf(rb(rb(ss * (1.f / DM)) + 1e-5f))), rd = __builtin_amdgcn_rcpf(den);
#pragma unroll
        for (int i = 0; i < 9; ++i) {
          u32x4 o;
#pragma unroll
          for (int j = 0; j < 4; ++j) o[j] = pk(fdiv(rb(lo16(xa[i][j]) - m), den, rd), fdiv(rb(hi16(xa[i][j]) - m), den, rd));
          *(LAS u32x4*)(lds + L_XN + (lane >> 3) * XN_LD + (lane & 7) * 16 + i * 128) = o;
        }
        LWAIT();
        PIN();
      }
      const rsrc_t r1 = mk(a.w1 + task * 128, DM * FF * 2 - task * 128), r3 = mk(a.w3 + task * 128, DM * FF * 2 - task * 128);
      const int vo = r * 8 * (FF * 2) + c * 8;
      const int xo = L_XN + (c & 7) * XN_LD + r * 16;
      u32x2 ra[8], rc[8];
      f32x4 g[4], u[4];
#pragma unroll
      for (int e = 0; e < 4; ++e) { g[e] = f32x4{0.f, 0.f, 0.f, 0.f}; u[e] = g[e]; }
#pragma unroll
      for (int i = 0; i < 8; ++i) ra[i] = LD64(r1, vo, i * (FF * 2), NT);
#pragma unroll
      for (int i = 0; i < 8 - MLP_GAP; ++i) rc[i] = LD64(r3, vo, i * (FF * 2), NT);
#define PAIR(x, y, d, rsx, sx, rsy, sy) { USE(x[2 * d + 1]); \
        _Pragma("unroll") for (int e = 0; e < 4; ++e) f[e][d] = __builtin_amdgcn_perm(x[2 * d + 1][e >> 1], x[2 * d][e >> 1], (e & 1) ? 0x07060302u : 0x05040100u); \
        PIN(); \
        _Pragma("unroll") for (int j = 2 * d - MLP_GAP; j < 2 * d + 2 - MLP_GAP; ++j) { \
          if (j < 0) y[8 + j] = LD64(rsy, vo, (sy + 8 + j) * (FF * 2), NT); else x[j] = LD64(rsx, vo, (sx + j) * (FF * 2), NT); } \
        PIN(); }
#pragma unroll 1
      for (int s = 0; s < 18; ++s) {
        const int sc_ = s * 32, sn = (s < 17 ? s + 1 : 17) * 32;
        const u32 nb = s < 17 ? DM * FF * 2 - task * 128 : 0;
        const rsrc_t n1 = mk(a.w1 + task * 128, nb), n3 = mk(a.w3 + task * 128, nb);
        const u32x4 xb = *(LAS u32x4*)(lds + xo + s * 64);
        u32x4 f[4];
        PAIR(ra, rc, 0, n1, sn, r3, sc_) PAIR(ra, rc, 1, n1, sn, r3, sc_) PAIR(ra, rc, 2, n1, sn, r3, sc_) PAIR(ra, rc, 3, n1, sn, r3, sc_)
#pragma unroll
        for (int e = 0; e < 4; ++e) g[e] = mfma(f[e], xb, g[e]);
        PIN();
        PAIR(rc, ra, 0, n3, sn, n1, sn) PAIR(rc, ra, 1, n3, sn, n1, sn) PAIR(rc, ra, 2, n3, sn, n1, sn) PAIR(rc, ra, 3, n3, sn, n1, sn)
#pragma unroll
        for (int e = 0; e < 4; ++e) u[e] = mfma(f[e], xb, u[e]);
        PIN();
      }
      float hv[4][4];
#pragma unroll
      for (int p = 0; p < 4; ++p)
#pragma unroll
        for (int e = 0; e < 4; ++e) hv[p][e] = rb(silu(rb(g[e][p]))) * rb(u[e][p]);
      const u32x4 h0 = {pk(hv[0][0], hv[0][1]), pk(hv[0][2], hv[0][3]), pk(hv[1][0], hv[1][1]), pk(hv[1][2], hv[1][3])};
      const u32x4 h1 = {pk(hv[2][0], hv[2][1]), pk(hv[2][2], hv[2][3]), pk(hv[3][0], hv[3][1]), pk(hv[3][2], hv[3][3])};
      ST128((c & 8) ? h1 : h0, rws, WS_HID + task * HID_LD + (c & 7) * 128 + r * 32 + (c >> 3) * 16, 0, SC1NT);
      DRAIN();
      while (!LGET(L_TK)) __builtin_amdgcn_s_sleep(2);
      asm volatile("" ::: "memory");
      if (lane == 0) ST64(*(LAS u32x2*)(lds + L_TG), rws, WS_HID + task * HID_LD + NB * 128, 0, SC1);
    }
    PIN();
    softmax();
    if (job == 1) {
      const int ks = task / 9, cg = task % 9;
      const rsrc_t r2 = mk(a.w2 + (size_t)ks * (576 * DM * 2) + cg * 128, 576 * DM * 2 - cg * 128);
      const int vo = r * 8 * (DM * 2) + c * 8;
      u32x2 ra[8], rc[8];
#pragma unroll
      for (int i = 0; i < 8; ++i) ra[i] = LD64(r2, vo, i * (DM * 2), NT);
      const u32x2 tg = *(LAS u32x2*)(lds + L_TG);
      bool ok = true;
      const u64 start = now();
      for (;;) {
        const u32x2 f = LD64(rws, WS_HID + NB * 128 + (ks * 9 + min(lane, 8)) * HID_LD, 0, SC1);
        if (__all(f[0] == tg[0] && f[1] == tg[1])) break;
        if (now() - start > SPIN_CAP) { ok = false; break; }
        __builtin_amdgcn_s_sleep(16);
      }
      asm volatile("" ::: "memory");
      {
        u32x4 t[9];
#pragma unroll
        for (int i = 0; i < 9; ++i) t[i] = LD128(rws, WS_HID + ks * 9 * HID_LD + lane * 16, i * HID_LD, SC1);
#pragma unroll
        for (int i = 0; i < 9; ++i) *(LAS u32x4*)(lds + L_HB + i * 1024 + lane * 16) = t[i];
      }
      LWAIT();
      const int ho = L_HB + (c & 7) * 128 + r * 16;
      f32x4 d[4];
#pragma unroll
      for (int e = 0; e < 4; ++e) d[e] = f32x4{0.f, 0.f, 0.f, 0.f};
#pragma unroll 1
      for (int s = 0; s < 9; ++s) {
        const rsrc_t n2 = mk(a.w2 + (size_t)ks * (576 * DM * 2) + cg * 128, s < 8 ? 576 * DM * 2 - cg * 128 : 0);
        const int sn = (s < 8 ? s + 1 : 8) * 64;
        const u32x4 hb = *(LAS u32x4*)(lds + ho + s * 1024), hc = *(LAS u32x4*)(lds + ho + s * 1024 + 64);
        u32x4 f[4];
#pragma unroll
        for (int i = 0; i < 8; ++i) { USE(ra[i]); rc[i] = LD64(r2, vo, (s * 64 + 32 + i) * (DM * 2), NT); PIN(); }
        tr8(ra, f);
#pragma unroll
        for (int e = 0; e < 4; ++e) d[e] = mfma(f[e], hb, d[e]);
        PIN();
#pragma unroll
        for (int i = 0; i < 8; ++i) { USE(rc[i]); ra[i] = LD64(n2, vo, (sn + i) * (DM * 2), NT); PIN(); }
        tr8(rc, f);
#pragma unroll
        for (int e = 0; e < 4; ++e) d[e] = mfma(f[e], hc, d[e]);
        PIN();
      }
#pragma unroll
      for (int p = 0; p < 2; ++p) {
        u32x4 o;
#pragma unroll
        for (int e = 0; e < 4; ++e) o[e] = __float_as_uint((c & 8) ? d[e][2 + p] : d[e][p]);
        *(LAS u32x4*)(lds + L_MO + (c & 7) * 256 + r * 64 + (c >> 3) * 32 + p * 16) = o;
      }
      LWAIT();
#pragma unroll
      for (int i = 0; i < 2; ++i) ST128(*(LAS u32x4*)(lds + L_MO + lane * 16 + i * 1024), rws, WS_MLP + task * MLP_LD + lane * 16, i * 1024, SC1NT);
      DRAIN();
      if (lane == 0 && ok) ST64(tg, rws, WS_MLP + task * MLP_LD + NB * 256, 0, SC1);
    }
    BAR();
    return;
  }
  PIN();
  softmax();
  if (h < NCONS) {
    bool ok = true;
    const u32x2 tg = *(LAS u32x2*)(lds + L_TG);
    const u32 t0 = tg[0], t1 = tg[1];
    const int col = h * 32 + (lane & 7) * 4;
    wait_batch(PRE_AT);
    int oo = (b * DM + col + r) * 2;
    const u32 xv = __builtin_amdgcn_raw_buffer_load_b16(mk(a.x, NB * DM * 2), oo, 0, NT);
    const u64 start = now();
    for (;;) {
      const u32x2 f1 = LD64(rws, WS_ATT + DM * 4 + (b * NH + (lane & 31)) * ATT_LD, 0, SC1);
      const u32x2 f2 = LD64(rws, WS_MLP + NB * 256 + ((lane & 7) * 9 + (h >> 1)) * MLP_LD, 0, SC1);
      bool all = f1[0] == t0 && f1[1] == t1 && f2[0] == t0 && f2[1] == t1;
      if (blk == 0) {
#pragma unroll
        for (int i = 0; i < 4; ++i) { const u32x2 f = LD64(rws, WS_ATT + DM * 4 + lane * ATT_LD, i * 64 * ATT_LD, SC1); all &= f[0] == t0 && f[1] == t1; }
      }
      if (__all(all)) break;
      if (now() - start > SPIN_CAP) { ok = false; break; }
      __builtin_amdgcn_s_sleep(16);
    }
    asm volatile("" ::: "memory");
    if (blk == 0 && lane == 0) ST64((u32x2{t0, t1}), rws, WS_EPOCH, 0, SC1);
    const u32x4 f = LD128(rws, WS_MLP + ((lane >> 3) * 9 + (h >> 1)) * MLP_LD + b * 256 + (h & 1) * 128 + (lane & 7) * 16, 0, SC1);
    float g = rb(silu(rb(sum8x4(__uint_as_float(f[0]), __uint_as_float(f[1]), __uint_as_float(f[2]), __uint_as_float(f[3]))))), xe = lo16(xv);
    int ao = WS_ATT + (b * NH + (lane >> 3)) * ATT_LD + col * 4;
    oo = c < 8 ? oo : NOWHERE;
    asm volatile("" : "+v"(g), "+v"(xe), "+v"(ao), "+v"(oo));
    const u32x4 dws = {(u32)(size_t)a.ws, (u32)((size_t)a.ws >> 32) & 0xffffu, WS_END, 0x00020000u};
    const u32x4 dout = {(u32)(size_t)a.out, (u32)((size_t)a.out >> 32) & 0xffffu, NB * DM * 2, 0x00020000u};
    BAR();
    if (ok) {
#define SWEEP(a0, a1, a2, a3) "buffer_load_dwordx4 " a0 ", %[ao], %[ws], 0 offen sc1\n\tbuffer_load_dwordx4 " a1 ", %[ao], %[ws], %[o1] offen sc1\n\t" \
                              "buffer_load_dwordx4 " a2 ", %[ao], %[ws], %[o2] offen sc1\n\tbuffer_load_dwordx4 " a3 ", %[ao], %[ws], %[o3] offen sc1\n\t"
      const u32 start2 = (u32)now();
      asm volatile(
        "s_waitcnt vmcnt(0)\n\t"
        SWEEP("v[64:67]", "v[68:71]", "v[72:75]", "v[76:79]")
        "\nLsw%=:\n\t"
        SWEEP("v[80:83]", "v[84:87]", "v[88:91]", "v[92:95]")
        "s_waitcnt vmcnt(4)\n\t"
        "v_max3_u32 v96, v64, v65, v66\n\tv_max3_u32 v97, v67, v68, v69\n\tv_max3_u32 v98, v70, v71, v72\n\tv_max3_u32 v99, v73, v74, v75\n\t"
        "v_max3_u32 v96, v96, v76, v77\n\tv_max3_u32 v97, v97, v78, v79\n\tv_max3_u32 v96, v96, v97, v98\n\tv_max_u32 v96, v96, v99\n\t"
        "v_cmp_eq_u32 vcc, -1, v96\n\ts_nop 1\n\ts_cbranch_vccz LswA%=\n\t"
        SWEEP("v[64:67]", "v[68:71]", "v[72:75]", "v[76:79]")
        "s_waitcnt vmcnt(4)\n\t"
        "v_max3_u32 v96, v80, v81, v82\n\tv_max3_u32 v97, v83, v84, v85\n\tv_max3_u32 v98, v86, v87, v88\n\tv_max3_u32 v99, v89, v90, v91\n\t"
        "v_max3_u32 v96, v96, v92, v93\n\tv_max3_u32 v97, v97, v94, v95\n\tv_max3_u32 v96, v96, v97, v98\n\tv_max_u32 v96, v96, v99\n\t"
        "v_cmp_eq_u32 vcc, -1, v96\n\ts_nop 1\n\ts_cbranch_vccz LswB%=\n\t"
        "s_memrealtime vcc\n\ts_waitcnt lgkmcnt(0)\n\ts_sub_u32 vcc_lo, vcc_lo, %[st]\n\ts_cmp_lt_u32 vcc_lo, %[cap]\n\ts_cbranch_scc1 Lsw%=\n\t"
        "v_mov_b32 v96, 0x7fc0\n\ts_branch Lsws%=\n"
        "LswB%=:\n\t"
        "v_add_f32 v96, v80, v84\n\tv_add_f32 v88, v88, v92\n\tv_add_f32 v97, v81, v85\n\tv_add_f32 v89, v89, v93\n\t"
        "v_add_f32 v98, v82, v86\n\tv_add_f32 v90, v90, v94\n\tv_add_f32 v99, v83, v87\n\tv_add_f32 v91, v91, v95\n\t"
        "v_add_f32 v96, v96, v88\n\tv_add_f32 v97, v97, v89\n\tv_add_f32 v98, v98, v90\n\tv_add_f32 v99, v99, v91\n\t"
        "s_branch Lswf%=\n"
        "LswA%=:\n\t"
        "v_add_f32 v96, v64, v68\n\tv_add_f32 v72, v72, v76\n\tv_add_f32 v97, v65, v69\n\tv_add_f32 v73, v73, v77\n\t"
        "v_add_f32 v98, v66, v70\n\tv_add_f32 v74, v74, v78\n\tv_add_f32 v99, v67, v71\n\tv_add_f32 v75, v75, v79\n\t"
        "v_add_f32 v96, v96, v72\n\tv_add_f32 v97, v97, v73\n\tv_add_f32 v98, v98, v74\n\tv_add_f32 v99, v99, v75\n"
        "Lswf%=:\n\ts_nop 1\n\t"
        "v_permlane32_swap_b32 v96, v98\n\tv_permlane32_swap_b32 v97, v99\n\ts_nop 1\n\tv_add_f32 v96, v96, v98\n\tv_add_f32 v97, v97, v99\n\ts_nop 1\n\t"
        "v_permlane16_swap_b32 v96, v97\n\ts_nop 1\n\tv_add_f32 v96, v96, v97\n\ts_nop 1\n\t"
        "v_add_f32_dpp v96, v96, v96 row_ror:8 row_mask:0xf bank_mask:0xf\n\t"
        "v_cvt_pk_bf16_f32 v96, v96, v96\n\tv_lshlrev_b32 v96, 16, v96\n\tv_mul_f32 v96, v96, %[g]\n\t"
        "v_cvt_pk_bf16_f32 v96, v96, v96\n\tv_lshlrev_b32 v96, 16, v96\n\tv_add_f32 v96, %[x], v96\n\tv_cvt_pk_bf16_f32 v96, v96, v96\n"
        "Lsws%=:\n\tbuffer_store_short v96, %[oo], %[out], 0 offen\n\ts_waitcnt vmcnt(0)"
        :
        : [ao] "v"(ao), [ws] "s"(dws), [o1] "s"(8 * ATT_LD), [o2] "s"(16 * ATT_LD), [o3] "s"(24 * ATT_LD), [st] "s"(start2), [cap] "s"((u32)SPIN_CAP),
          [g] "v"(g), [x] "v"(xe), [oo] "v"(oo), [out] "s"(dout)
        : "memory", "vcc", "scc", "v64", "v65", "v66", "v67", "v68", "v69", "v70", "v71", "v72", "v73", "v74", "v75", "v76", "v77", "v78", "v79", "v80", "v81",
          "v82", "v83", "v84", "v85", "v86", "v87", "v88", "v89", "v90", "v91", "v92", "v93", "v94", "v95", "v96", "v97", "v98", "v99");
    } else {
      __builtin_amdgcn_raw_buffer_store_b16((u16)0x7fc0, mk(a.out, NB * DM * 2), oo, 0, 0);
    }
  } else {
    BAR();
  }
}

}

extern "C" void kernel_launch(void* const* d_in, const int* in_sizes, int n_in, void* d_out, int out_size, void* d_ws, size_t ws_size,
                              hipStream_t stream) {
  Args a;
  a.x = (const char*)d_in[0]; a.k = (const char*)d_in[1]; a.v = (const char*)d_in[2]; a.wq = (const char*)d_in[3]; a.wo = (const char*)d_in[4];
  a.w1 = (const char*)d_in[6]; a.w3 = (const char*)d_in[7]; a.w2 = (const char*)d_in[8];
  a.out = (char*)d_out; a.ws = (char*)d_ws;
  hipLaunchKernelGGL(fused_decode, dim3(NB * NH), dim3(1024), 0, stream, a);
}
```
